```python
import torch

def _softmax(x, axis=-1):
    return torch.softmax(x, dim=axis)
def _gelu(x):
    return torch.nn.functional.gelu(x, approximate="tanh")
def _silu(x):
    return torch.nn.functional.silu(x)
def _sigmoid(x):
    return torch.sigmoid(x)
def _layernorm(x, axis=-1, eps=1e-5):
    m = x.mean(dim=axis, keepdim=True)
    v = x.var(dim=axis, keepdim=True, unbiased=False)
    return (x - m) / torch.sqrt(v + eps)
def _scatter_add(tgt, idx, src, axis=0):
    return tgt.clone().index_add_(axis, idx, src)
def _segment_sum(src, seg, n):
    out = torch.zeros((n,) + src.shape[1:], dtype=src.dtype)
    return out.index_add_(0, seg, src)
def _pad(x, pw):
    flat = tuple(v for pair in reversed(pw) for v in pair)
    return torch.nn.functional.pad(x, flat)

kernel_name = 'standalone__mha_softmax__near_ridge'

def setup_inputs():

    _w = (lambda a: (lambda t: t.to(torch.bfloat16) if t.is_floating_point() else t)(torch.as_tensor(a)))
    B, H, Sq, Sk, D = 8, 32, 496, 512, 192
    return {
        "q": _w((lambda *s: torch.randn(*s, dtype=torch.bfloat16) * 0.8)(B, H, Sq, D)),
        "k": _w((lambda *s: torch.randn(*s, dtype=torch.bfloat16) * 0.8)(B, H, Sk, D)),
        "v": _w((lambda *s: torch.randn(*s, dtype=torch.bfloat16) * 0.05)(B, H, Sk, D)),
    }

def reference(q, k, v):
    scale = q.shape[-1] ** -0.5
    scores = torch.einsum("bhqd,bhkd->bhqk", q, k) * scale
    p = _softmax(scores, axis=-1)
    return torch.einsum("bhqk,bhkd->bhqd", p, v)
```

```cpp
#include <hip/hip_runtime.h>
#include <hip/hip_bf16.h>

typedef float    f32x16 __attribute__((ext_vector_type(16)));
typedef short    bf16x8 __attribute__((ext_vector_type(8)));
typedef short    s16x4  __attribute__((ext_vector_type(4)));
typedef unsigned u32x4  __attribute__((ext_vector_type(4)));
typedef unsigned u32x2  __attribute__((ext_vector_type(2)));
#define DI __device__ __forceinline__

constexpr int SQ = 496, SK = 512, D = 192, RB = 384;

DI unsigned cvtpk(float lo, float hi) { unsigned r; asm volatile("v_cvt_pk_bf16_f32 %0, %1, %2" : "=v"(r) : "v"(lo), "v"(hi)); return r; }
DI unsigned gk(unsigned r) { return ((r >> 1) & 3u) | ((r >> 2) & 4u); }

__global__ __launch_bounds__(256) void attn_base(const unsigned short* __restrict__ Q, const unsigned short* __restrict__ K,
                                                 const unsigned short* __restrict__ V, unsigned short* __restrict__ O) {
  __shared__ __attribute__((aligned(16))) char lds[24576];
  const unsigned tid = threadIdx.x, lane = tid & 63, w = tid >> 6, r = lane & 31, h = lane >> 5;
  const unsigned head = blockIdx.x >> 2, qblk = blockIdx.x & 3;
  const unsigned row = qblk * 128 + w * 32 + r;
  const unsigned rowc = row < SQ ? row : SQ - 1;
  const char* Qp = (const char*)Q + ((size_t)head * SQ + rowc) * RB + 16 * h;
  bf16x8 q[12];
#pragma unroll
  for (int d0 = 0; d0 < 12; ++d0) q[d0] = *(const bf16x8*)(Qp + 32 * d0);
  const char* Kp = (const char*)K + (size_t)head * SK * RB + 3072 * w;
  const char* Vp = (const char*)V + (size_t)head * SK * RB + 3072 * w;
  unsigned vK[3], vV[3];
#pragma unroll
  for (int i = 0; i < 3; ++i) {
    unsigned rl = (64 * i + lane) / 24;
    vK[i] = 16 * (lane ^ gk(8 * w + rl)) + 1024 * i;
    vV[i] = 16 * (lane ^ (4 * ((rl >> 1) & 1))) + 1024 * i;
  }
  unsigned kb[4];
#pragma unroll
  for (int j = 0; j < 4; ++j) kb[j] = 384 * r + 16 * ((2 * j + h) ^ gk(r));
  const unsigned blk = (lane >> 4) & 1, qq = (lane >> 2) & 3, p = lane & 3;
  unsigned vb[2];
#pragma unroll
  for (int e = 0; e < 2; ++e) vb[e] = (unsigned)(size_t)lds + 12288 + 384 * (4 * h + qq) + 64 * (e ^ (qq >> 1)) + 32 * blk + 8 * p;
  f32x16 o[6];
#pragma unroll
  for (int i = 0; i < 6; ++i) o[i] = f32x16{};
  float m = -1e30f, l = 0.f;
  const float C = 0.07216878364870322f * 1.4426950408889634f;
  for (int u = 0; u < 16; ++u) {
    __syncthreads();
#pragma unroll
    for (int i = 0; i < 3; ++i) {
      *(u32x4*)(lds + 3072 * w + 1024 * i + 16 * lane) = *(const u32x4*)(Kp + (size_t)u * 12288 + vK[i]);
      *(u32x4*)(lds + 12288 + 3072 * w + 1024 * i + 16 * lane) = *(const u32x4*)(Vp + (size_t)u * 12288 + vV[i]);
    }
    __syncthreads();
    f32x16 s = f32x16{};
#pragma unroll
    for (int d0 = 0; d0 < 12; ++d0) {
      bf16x8 kf = *(const bf16x8*)(lds + kb[d0 & 3] + 128 * (d0 >> 2));
      s = __builtin_amdgcn_mfma_f32_32x32x16_bf16(kf, q[d0], s, 0, 0, 0);
    }
    float mx = s[0];
#pragma unroll
    for (int i = 1; i < 16; ++i) mx = fmaxf(mx, s[i]);
    mx = fmaxf(mx, __shfl_xor(mx, 32));
    float mn = fmaxf(m, mx * C);
    float f = __builtin_amdgcn_exp2f(m - mn);
    m = mn;
    float ps = 0.f;
#pragma unroll
    for (int i = 0; i < 16; ++i) { s[i] = __builtin_amdgcn_exp2f(fmaf(s[i], C, -mn)); ps += s[i]; }
    l = l * f + ps;
#pragma unroll
    for (int db = 0; db < 6; ++db)
#pragma unroll
      for (int i = 0; i < 16; ++i) o[db][i] *= f;
    bf16x8 pf[2];
#pragma unroll
    for (int ks = 0; ks < 2; ++ks) {
      u32x4 x;
#pragma unroll
      for (int j = 0; j < 4; ++j) x[j] = cvtpk(s[8 * ks + 2 * j], s[8 * ks + 2 * j + 1]);
      pf[ks] = __builtin_bit_cast(bf16x8, x);
    }
#pragma unroll
    for (int db = 0; db < 6; ++db) {
#pragma unroll
      for (int ks = 0; ks < 2; ++ks) {
        s16x4 lo, hi;
        unsigned a0 = vb[db & 1] + 384 * 16 * ks + 128 * (db >> 1), a1 = a0 + 384 * 8;
        asm volatile("ds_read_b64_tr_b16 %0, %2\n\tds_read_b64_tr_b16 %1, %3\n\ts_waitcnt lgkmcnt(0)" : "=&v"(lo), "=&v"(hi) : "v"(a0), "v"(a1) : "memory");
        bf16x8 vf = __builtin_shufflevector(lo, hi, 0, 1, 2, 3, 4, 5, 6, 7);
        o[db] = __builtin_amdgcn_mfma_f32_32x32x16_bf16(vf, pf[ks], o[db], 0, 0, 0);
      }
    }
  }
  l += __shfl_xor(l, 32);
  float il = 1.f / l;
  if (row < SQ) {
    char* Op = (char*)O + ((size_t)head * SQ + row) * RB + 8 * h;
#pragma unroll
    for (int db = 0; db < 6; ++db)
#pragma unroll
      for (int g = 0; g < 4; ++g) {
        u32x2 x = {cvtpk(o[db][4 * g] * il, o[db][4 * g + 1] * il), cvtpk(o[db][4 * g + 2] * il, o[db][4 * g + 3] * il)};
        *(u32x2*)(Op + 64 * db + 16 * g) = x;
      }
  }
}

extern "C" void kernel_launch(void* const* d_in, const int* in_sizes, int n_in, void* d_out, int out_size, void* d_ws, size_t ws_size,
                              hipStream_t stream) {
  attn_base<<<1024, 256, 0, stream>>>((const unsigned short*)d_in[0], (const unsigned short*)d_in[1], (const unsigned short*)d_in[2],
                                      (unsigned short*)d_out);
}
```

```cpp
#include <hip/hip_runtime.h>
typedef float    f32x16 __attribute__((ext_vector_type(16)));
typedef short    bf16x8 __attribute__((ext_vector_type(8)));
typedef short    s16x4  __attribute__((ext_vector_type(4)));
typedef unsigned u32x4  __attribute__((ext_vector_type(4)));
typedef unsigned u32x2  __attribute__((ext_vector_type(2)));
__device__ __forceinline__ unsigned cvtpk(float lo, float hi) { unsigned r; asm volatile("v_cvt_pk_bf16_f32 %0, %1, %2" : "=v"(r) : "v"(lo), "v"(hi)); return r; }
__device__ __forceinline__ unsigned gk(unsigned r) { return ((r >> 1) & 3u) | ((r >> 2) & 4u); }
__device__ __forceinline__ float swap32(float x, unsigned lane) { return __int_as_float(__builtin_amdgcn_ds_bpermute((int)((lane ^ 32u) << 2), __float_as_int(x))); }
__device__ __forceinline__ void redo_rows(const char* Q, const char* K, const char* V, char* O, char* lds, unsigned head, unsigned row0, unsigned rowend,
                                          unsigned w, unsigned lane) {
  const unsigned r = lane & 31, h = lane >> 5;
  const unsigned row = row0 + w * 32 + r;
  const unsigned rowc = row < rowend ? row : rowend - 1;
  const char* Qp = Q + ((size_t)head * 496 + rowc) * 384 + 16 * h;
  bf16x8 q[12];
#pragma unroll
  for (int d0 = 0; d0 < 12; ++d0) q[d0] = *(const bf16x8*)(Qp + 32 * d0);
  const char* Kp = K + (size_t)head * 512 * 384 + 3072 * w;
  const char* Vp = V + (size_t)head * 512 * 384 + 3072 * w;
  unsigned vK[3], vV[3];
#pragma unroll
  for (int i = 0; i < 3; ++i) {
    unsigned rl = (64 * i + lane) / 24;
    vK[i] = 16 * (lane ^ gk(8 * w + rl)) + 1024 * i;
    vV[i] = 16 * (lane ^ (4 * ((rl >> 1) & 1))) + 1024 * i;
  }
  unsigned kb[4];
#pragma unroll
  for (int j = 0; j < 4; ++j) kb[j] = 384 * r + 16 * ((2 * j + h) ^ gk(r));
  const unsigned blk = (lane >> 4) & 1, qq = (lane >> 2) & 3, p = lane & 3;
  unsigned vb[2];
#pragma unroll
  for (int e = 0; e < 2; ++e) vb[e] = (unsigned)(size_t)lds + 12288 + 384 * (4 * h + qq) + 64 * (e ^ (qq >> 1)) + 32 * blk + 8 * p;
  f32x16 o[6];
#pragma unroll
  for (int i = 0; i < 6; ++i) o[i] = f32x16{};
  float m = -INFINITY, l = 0.f;
  const float C = 0.07216878364870322f * 1.4426950408889634f;
  for (int u = 0; u < 16; ++u) {
    __syncthreads();
#pragma unroll
    for (int i = 0; i < 3; ++i) {
      *(u32x4*)(lds + 3072 * w + 1024 * i + 16 * lane) = *(const u32x4*)(Kp + (size_t)u * 12288 + vK[i]);
      *(u32x4*)(lds + 12288 + 3072 * w + 1024 * i + 16 * lane) = *(const u32x4*)(Vp + (size_t)u * 12288 + vV[i]);
    }
    __syncthreads();
    f32x16 s = f32x16{};
#pragma unroll
    for (int d0 = 0; d0 < 12; ++d0) {
      bf16x8 kf = *(const bf16x8*)(lds + kb[d0 & 3] + 128 * (d0 >> 2));
      s = __builtin_amdgcn_mfma_f32_32x32x16_bf16(kf, q[d0], s, 0, 0, 0);
    }
    float mx = s[0];
#pragma unroll
    for (int i = 1; i < 16; ++i) mx = fmaxf(mx, s[i]);
    mx = fmaxf(mx, swap32(mx, lane));
    const float mn = fmaxf(m, mx * C);
    const float f = u ? __builtin_amdgcn_exp2f(m - mn) : 0.f;
    m = mn;
    float ps = 0.f;
#pragma unroll
    for (int i = 0; i < 16; ++i) { s[i] = __builtin_amdgcn_exp2f(fmaf(s[i], C, -mn)); ps += s[i]; }
    l = l * f + ps;
#pragma unroll
    for (int db = 0; db < 6; ++db)
#pragma unroll
      for (int i = 0; i < 16; ++i) o[db][i] *= f;
    bf16x8 pf[2];
#pragma unroll
    for (int ks = 0; ks < 2; ++ks) {
      u32x4 x;
#pragma unroll
      for (int jj = 0; jj < 4; ++jj) x[jj] = cvtpk(s[8 * ks + 2 * jj], s[8 * ks + 2 * jj + 1]);
      pf[ks] = __builtin_bit_cast(bf16x8, x);
    }
#pragma unroll
    for (int db = 0; db < 6; ++db) {
#pragma unroll
      for (int ks = 0; ks < 2; ++ks) {
        s16x4 lo, hi;
        unsigned a0 = vb[db & 1] + 384 * 16 * ks + 128 * (db >> 1), a1 = a0 + 384 * 8;
        asm volatile("ds_read_b64_tr_b16 %0, %2\n\tds_read_b64_tr_b16 %1, %3\n\ts_waitcnt lgkmcnt(0)" : "=&v"(lo), "=&v"(hi) : "v"(a0), "v"(a1) : "memory");
        bf16x8 vf = __builtin_shufflevector(lo, hi, 0, 1, 2, 3, 4, 5, 6, 7);
        o[db] = __builtin_amdgcn_mfma_f32_32x32x16_bf16(vf, pf[ks], o[db], 0, 0, 0);
      }
    }
  }
  l += swap32(l, lane);
  const float il = 1.f / l;
  if (row < rowend) {
    char* Op = O + ((size_t)head * 496 + row) * 384 + 8 * h;
#pragma unroll
    for (int db = 0; db < 6; ++db)
#pragma unroll
      for (int g = 0; g < 4; ++g) {
        u32x2 x = {cvtpk(o[db][4 * g] * il, o[db][4 * g + 1] * il), cvtpk(o[db][4 * g + 2] * il, o[db][4 * g + 3] * il)};
        *(u32x2*)(Op + 64 * db + 16 * g) = x;
      }
  }
}

__global__ __launch_bounds__(256, 1) void mha_fwd(const char* __restrict__ Q, const char* __restrict__ K, const char* __restrict__ V, char* __restrict__ O, char* ws) {
  __shared__ __attribute__((aligned(128))) char lds[163840];
  const unsigned w = __builtin_amdgcn_readfirstlane(threadIdx.x >> 6);
  const unsigned b = blockIdx.x;
  unsigned flag;
  asm volatile(
    "s_load_dwordx8 s[72:79], %1, 0\n"
    "s_getreg_b32 s64, hwreg(HW_REG_XCC_ID, 0, 1)\n"
    "s_lshr_b32 s63, %2, 4\n"
    "s_cmp_eq_u32 s64, 1\n"
    "s_cselect_b32 s64, 8, 0\n"
    "s_cselect_b32 s71, 28, 30\n"
    "s_cselect_b32 s52, 0, 20\n"
    "s_max_i32 s63, s63, s64\n"
    "s_sub_u32 s63, s63, s64\n"
    "s_mul_i32 s63, s63, s71\n"
    "s_add_u32 s63, s63, s52\n"
    "s_lshr_b32 s63, s63, 2\n"
    "v_mbcnt_lo_u32_b32 v24, -1, 0\n"
    "v_mbcnt_hi_u32_b32 v24, -1, v24\n"
    "s_lshr_b32 s58, %2, 4\n"
    "s_and_b32 s59, %2, 7\n"
    "s_lshl3_add_u32 s59, s58, s59\n"
    "s_mul_i32 s58, s59, 393216\n"
    "s_mul_i32 s55, %3, 3072\n"
    "s_add_u32 s54, s55, %4\n"
    "s_mov_b32 s38, 393216\n"
    "s_mov_b32 s39, 0x20000\n"
    "v_lshlrev_b32 v26, 4, v24\n"
    "v_mov_b32 v27, 171\n"
    "v_mul_u32_u24 v25, 171, v24\n"
    "v_bfe_u32 v25, v25, 13, 2\n"
    "v_lshlrev_b32 v25, 5, v25\n"
    "v_xor_b32 v6, v25, v26\n"
    "s_add_u32 m0, s54, 0\n"
    "SL_%=:\ns_cmp_eq_u32 s63, 0\n"
    "s_cbranch_scc1 SLE_%=\n"
    "s_sleep 1\n"
    "s_sub_u32 s63, s63, 1\n"
    "s_branch SL_%=\nSLE_%=:\n"
    "s_waitcnt lgkmcnt(0)\n"
    "s_add_u32 s36, s74, s58\n"
    "s_addc_u32 s37, s75, 0\n"
    "buffer_load_dwordx4 v6, s[36:39], s55 offen offset:0 nt lds\n"
    "s_movk_i32 s53, 10944\n"
    "v_mad_u32_u24 v25, v24, v27, s53\n"
    "v_bfe_u32 v25, v25, 13, 2\n"
    "v_lshlrev_b32 v25, 5, v25\n"
    "v_xor_b32 v7, v25, v26\n"
    "buffer_load_dwordx4 v7, s[36:39], s55 offen offset:1024 nt lds\n"
    "s_movk_i32 s53, 21888\n"
    "v_mad_u32_u24 v25, v24, v27, s53\n"
    "v_bfe_u32 v25, v25, 13, 2\n"
    "v_lshlrev_b32 v25, 5, v25\n"
    "v_xor_b32 v8, v25, v26\n"
    "buffer_load_dwordx4 v8, s[36:39], s55 offen offset:2048 nt lds\n"
    "v_lshrrev_b32 v25, 3, v24\n"
    "v_mul_u32_u24 v25, 384, v25\n"
    "v_bfe_u32 v26, v24, 4, 2\n"
    "v_lshlrev_b32 v26, 1, v26\n"
    "v_and_b32 v27, 7, v24\n"
    "v_xor_b32 v26, v26, v27\n"
    "v_lshl_add_u32 v9, v26, 4, v25\n"
    "s_mul_i32 s59, s59, 380928\n"
    "s_bfe_u32 s63, %2, 0x10003\n"
    "s_mul_i32 s63, s63, 95232\n"
    "s_add_u32 s59, s59, s63\n"
    "s_add_u32 s44, s72, s59\n"
    "s_addc_u32 s45, s73, 0\n"
    "s_mov_b32 s46, 95232\n"
    "s_mov_b32 s47, 0x20000\n"
    "s_mul_i32 s62, %3, 24576\n"
    "s_lshl_b32 s53, %3, 3\n"
    "s_lshr_b32 s65, 353045001, s53\n"
    "s_and_b32 s65, s65, 0xff\n"
    "s_lshl_b32 s65, s65, 12\n"
    "s_add_u32 s65, s65, %4\n"
    "s_mov_b32 m0, s65\n"
    "s_add_u32 s63, s62, 0\n"
    "buffer_load_dwordx4 v9, s[44:47], s63 offen offset:0 nt lds\n"
    "s_add_u32 s64, s62, 2048\n"
    "buffer_load_dwordx4 v9, s[44:47], s64 offen offset:1024 nt lds\n"
    "s_add_u32 s63, s62, 4096\n"
    "buffer_load_dwordx4 v9, s[44:47], s63 offen offset:2048 nt lds\n"
    "s_add_u32 s64, s62, 6144\n"
    "buffer_load_dwordx4 v9, s[44:47], s64 offen offset:3072 nt lds\n"
    "s_lshr_b32 s66, 218896150, s53\n"
    "s_and_b32 s66, s66, 0xff\n"
    "s_lshl_b32 s66, s66, 12\n"
    "s_add_u32 s66, s66, %4\n"
    "s_mov_b32 m0, s66\n"
    "s_add_u32 s63, s62, 12288\n"
    "buffer_load_dwordx4 v9, s[44:47], s63 offen offset:0 nt lds\n"
    "s_add_u32 s64, s62, 14336\n"
    "buffer_load_dwordx4 v9, s[44:47], s64 offen offset:1024 nt lds\n"
    "s_add_u32 s63, s62, 16384\n"
    "buffer_load_dwordx4 v9, s[44:47], s63 offen offset:2048 nt lds\n"
    "s_add_u32 s64, s62, 18432\n"
    "buffer_load_dwordx4 v9, s[44:47], s64 offen offset:3072 nt lds\n"
    "s_getpc_b64 s[48:49]\nPCL_%=:\n"
    "s_mov_b32 s50, END_%=-PCL_%=\n"
    "s_mov_b32 s51, 0x20000\n"
    "s_lshr_b32 s63, %2, 3\n"
    "s_lshl_b32 s63, s63, 2\n"
    "s_add_u32 s63, s63, %3\n"
    "s_lshl_b32 s63, s63, 10\n"
    "v_and_b32 v25, 7, v24\n"
    "v_lshlrev_b32 v25, 7, v25\n"
    "buffer_load_dword v16, v25, s[48:51], s63 offen\n"
    "s_mov_b32 %0, 0\n"
    "s_mov_b32 s42, 393216\n"
    "s_mov_b32 s43, 0x20000\n"
    "s_add_u32 s40, s76, s58\n"
    "s_addc_u32 s41, s77, 0\n"
    "s_add_u32 s56, s55, 12288\n"
    "s_lshr_b32 s67, 437852174, s53\n"
    "s_and_b32 s67, s67, 0xff\n"
    "s_lshl_b32 s67, s67, 12\n"
    "s_add_u32 s67, s67, %4\n"
    "s_mov_b32 m0, s67\n"
    "s_add_u32 s63, s62, 128\n"
    "buffer_load_dwordx4 v9, s[44:47], s63 offen offset:0 nt lds\n"
    "s_add_u32 s64, s62, 2176\n"
    "buffer_load_dwordx4 v9, s[44:47], s64 offen offset:1024 nt lds\n"
    "s_add_u32 s63, s62, 4224\n"
    "buffer_load_dwordx4 v9, s[44:47], s63 offen offset:2048 nt lds\n"
    "s_add_u32 s64, s62, 6272\n"
    "buffer_load_dwordx4 v9, s[44:47], s64 offen offset:3072 nt lds\n"
    "s_lshr_b32 s68, 505224219, s53\n"
    "s_and_b32 s68, s68, 0xff\n"
    "s_lshl_b32 s68, s68, 12\n"
    "s_add_u32 s68, s68, %4\n"
    "s_mov_b32 m0, s68\n"
    "s_add_u32 s63, s62, 12416\n"
    "buffer_load_dwordx4 v9, s[44:47], s63 offen offset:0 nt lds\n"
    "s_add_u32 s64, s62, 14464\n"
    "buffer_load_dwordx4 v9, s[44:47], s64 offen offset:1024 nt lds\n"
    "s_add_u32 s63, s62, 16512\n"
    "buffer_load_dwordx4 v9, s[44:47], s63 offen offset:2048 nt lds\n"
    "s_add_u32 s64, s62, 18560\n"
    "buffer_load_dwordx4 v9, s[44:47], s64 offen offset:3072 nt lds\n"
    "s_lshr_b32 s69, 572596255, s53\n"
    "s_and_b32 s69, s69, 0xff\n"
    "s_lshl_b32 s69, s69, 12\n"
    "s_add_u32 s69, s69, %4\n"
    "s_lshr_b32 s70, 639968291, s53\n"
    "s_and_b32 s70, s70, 0xff\n"
    "s_lshl_b32 s70, s70, 12\n"
    "s_add_u32 s70, s70, %4\n"
    "s_add_u32 s48, s78, s59\n"
    "s_addc_u32 s49, s79, 0\n"
    "s_mov_b32 s50, 95232\n"
    "s_mov_b32 s51, 0x20000\n"
    "s_mov_b32 s52, 0x3dd53b95\n"
    "s_mov_b32 s53, 0x53800000\n"
    "s_mul_i32 s57, %3, 10240\n"
    "s_add_u32 s57, s57, %4\n"
    "s_add_u32 s57, s57, 122880\n"
    "s_cmp_eq_u32 %3, 3\n"
    "s_cselect_b32 s60, 0xff00ff00, 0\n"
    "s_mov_b32 s61, s60\n"
    "v_lshrrev_b32 v12, 4, v24\n"
    "v_and_b32 v14, 15, v24\n"
    "v_bfe_u32 v25, v24, 1, 2\n"
    "v_lshlrev_b32 v25, 1, v25\n"
    "v_xor_b32 v25, v25, v12\n"
    "v_lshlrev_b32 v13, 4, v25\n"
    "v_mul_u32_u24 v25, 384, v14\n"
    "v_add3_u32 v0, v25, v13, %4\n"
    "v_xor_b32 v1, 64, v0\n"
    "v_lshl_add_u32 v13, v14, 7, v13\n"
    "v_add_u32 v84, s65, v13\n"
    "v_xor_b32 v85, 64, v84\n"
    "v_add_u32 v86, s66, v13\n"
    "v_xor_b32 v87, 64, v86\n"
    "v_add_u32 v88, s67, v13\n"
    "v_xor_b32 v89, 64, v88\n"
    "v_add_u32 v90, s68, v13\n"
    "v_xor_b32 v91, 64, v90\n"
    "v_add_u32 v92, s69, v13\n"
    "v_xor_b32 v93, 64, v92\n"
    "v_add_u32 v94, s70, v13\n"
    "v_xor_b32 v95, 64, v94\n"
    "v_add_u32 v10, s57, v13\n"
    "v_xor_b32 v11, 64, v10\n"
    "v_lshrrev_b32 v25, 2, v24\n"
    "v_mul_u32_u24 v25, 384, v25\n"
    "v_bfe_u32 v26, v24, 3, 2\n"
    "v_lshl_add_u32 v25, v26, 5, v25\n"
    "v_and_b32 v26, 3, v24\n"
    "v_lshl_add_u32 v25, v26, 3, v25\n"
    "s_add_u32 s63, %4, 61440\n"
    "v_add_u32 v2, s63, v25\n"
    "v_xor_b32 v3, 32, v2\n"
    "v_xor_b32 v4, 64, v2\n"
    "v_xor_b32 v5, 96, v2\n"
    "v_bfe_u32 v25, v24, 1, 1\n"
    "v_lshrrev_b32 v26, 5, v24\n"
    "v_xor_b32 v25, v25, v26\n"
    "v_add_u32 v25, -1, v25\n"
    "v_and_b32 v25, 0x3f803f80, v25\n"
    "v_accvgpr_write_b32 a252, v25\n"
    "v_accvgpr_write_b32 a253, v25\n"
    "v_accvgpr_write_b32 a254, v25\n"
    "v_accvgpr_write_b32 a255, v25\n"
    "v_and_b32 v25, 1, v24\n"
    "v_mul_u32_u24 v25, 0xaaaa, v25\n"
    "v_or_b32 v23, 0x4444, v25\n"
    "v_accvgpr_write_b32 a236, 0\n"
    "v_accvgpr_write_b32 a237, 0\n"
    "v_accvgpr_write_b32 a238, 0\n"
    "v_accvgpr_write_b32 a239, 0\n"
    "v_accvgpr_write_b32 a240, 0\n"
    "v_accvgpr_write_b32 a241, 0\n"
    "v_accvgpr_write_b32 a242, 0\n"
    "v_accvgpr_write_b32 a243, 0\n"
    "v_accvgpr_write_b32 a244, 0\n"
    "v_accvgpr_write_b32 a245, 0\n"
    "v_accvgpr_write_b32 a246, 0\n"
    "v_accvgpr_write_b32 a247, 0\n"
    "v_accvgpr_write_b32 a248, 0\n"
    "v_accvgpr_write_b32 a249, 0\n"
    "v_accvgpr_write_b32 a250, 0\n"
    "v_accvgpr_write_b32 a251, 0\n"
    "s_mov_b32 m0, s69\n"
    "s_add_u32 s63, s62, 256\n"
    "buffer_load_dwordx4 v9, s[44:47], s63 offen offset:0 nt lds\n"
    "s_add_u32 s64, s62, 2304\n"
    "buffer_load_dwordx4 v9, s[44:47], s64 offen offset:1024 nt lds\n"
    "s_add_u32 s63, s62, 4352\n"
    "buffer_load_dwordx4 v9, s[44:47], s63 offen offset:2048 nt lds\n"
    "s_add_u32 s64, s62, 6400\n"
    "buffer_load_dwordx4 v9, s[44:47], s64 offen offset:3072 nt lds\n"
    "s_mov_b32 m0, s70\n"
    "s_add_u32 s63, s62, 12544\n"
    "buffer_load_dwordx4 v9, s[44:47], s63 offen offset:0 nt lds\n"
    "s_add_u32 s64, s62, 14592\n"
    "buffer_load_dwordx4 v9, s[44:47], s64 offen offset:1024 nt lds\n"
    "s_add_u32 s63, s62, 16640\n"
    "buffer_load_dwordx4 v9, s[44:47], s63 offen offset:2048 nt lds\n"
    "s_add_u32 s64, s62, 18688\n"
    "buffer_load_dwordx4 v9, s[44:47], s64 offen offset:3072 nt lds\n"
    "s_nop 0\n"
    "s_waitcnt vmcnt(17)\n"
    "s_barrier\n"
    "s_add_u32 m0, s54, 12288\n"
    "s_add_u32 s44, s44, 190464\ns_addc_u32 s45, s45, 0\n"
    "s_add_u32 s63, s62, 0\n"
    "s_add_u32 s64, s62, 2048\n"
    "ds_read_b128 a[0:3], v84 offset:0\n"
    "ds_read_b128 a[192:195], v0 offset:0\n"
    "s_waitcnt lgkmcnt(0)\n"
    "v_mfma_f32_16x16x32_bf16 v[28:31], a[192:195], a[0:3], 0\n"
    "ds_read_b128 a[24:27], v84 offset:2048\n"
    "ds_read_b128 a[48:51], v86 offset:0\n"
    "ds_read_b128 a[72:75], v86 offset:2048\n"
    "ds_read_b128 a[4:7], v85 offset:0\n"
    "ds_read_b128 a[196:199], v1 offset:0\n"
    "ds_read_b128 a[28:31], v85 offset:2048\n"
    "ds_read_b128 a[52:55], v87 offset:0\n"
    "ds_read_b128 a[76:79], v87 offset:2048\n"
    "s_waitcnt lgkmcnt(7)\n"
    "v_mfma_f32_16x16x32_bf16 v[32:35], a[192:195], a[24:27], 0\n"
    "s_waitcnt lgkmcnt(6)\n"
    "ds_read_b128 a[200:203], v0 offset:6144\n"
    "buffer_load_dwordx4 v6, s[36:39], s56 offen offset:0 nt lds\n"
    "ds_read_b128 a[204:207], v1 offset:6144\n"
    "ds_read_b128 a[208:211], v0 offset:128\n"
    "ds_read_b128 a[212:215], v1 offset:128\n"
    "v_mfma_f32_16x16x32_bf16 v[36:39], a[192:195], a[48:51], 0\n"
    "s_waitcnt lgkmcnt(9)\n"
    "v_mfma_f32_16x16x32_bf16 v[40:43], a[192:195], a[72:75], 0\n"
    "s_waitcnt lgkmcnt(7)\n"
    "buffer_load_dwordx4 v7, s[36:39], s56 offen offset:1024 nt lds\n"
    "ds_read_b128 a[192:195], v0 offset:6272\n"
    "v_mfma_f32_16x16x32_bf16 v[28:31], a[196:199], a[4:7], v[28:31]\n"
    "s_waitcnt lgkmcnt(7)\n"
    "v_mfma_f32_16x16x32_bf16 v[32:35], a[196:199], a[28:31], v[32:35]\n"
    "s_waitcnt lgkmcnt(6)\n"
    "buffer_load_dwordx4 v8, s[36:39], s56 offen offset:2048 nt lds\n"
    "s_add_u32 m0, s54, 61440\n"
    "v_mfma_f32_16x16x32_bf16 v[36:39], a[196:199], a[52:55], v[36:39]\n"
    "s_waitcnt lgkmcnt(5)\n"
    "v_mfma_f32_16x16x32_bf16 v[40:43], a[196:199], a[76:79], v[40:43]\n"
    "s_waitcnt lgkmcnt(4)\n"
    "buffer_load_dwordx4 v6, s[40:43], s55 offen offset:0 nt lds\n"
    "ds_read_b128 a[196:199], v1 offset:6272\n"
    "v_mfma_f32_16x16x32_bf16 v[60:63], a[200:203], a[0:3], 0\n"
    "v_mfma_f32_16x16x32_bf16 v[64:67], a[200:203], a[24:27], 0\n"
    "buffer_load_dwordx4 v7, s[40:43], s55 offen offset:1024 nt lds\n"
    "v_mfma_f32_16x16x32_bf16 v[68:71], a[200:203], a[48:51], 0\n"
    "v_mfma_f32_16x16x32_bf16 v[72:75], a[200:203], a[72:75], 0\n"
    "s_waitcnt lgkmcnt(4)\n"
    "buffer_load_dwordx4 v8, s[40:43], s55 offen offset:2048 nt lds\n"
    "s_add_u32 s55, s55, 24576\n"
    "s_add_u32 m0, s54, 24576\n"
    "ds_read_b128 a[200:203], v0 offset:256\n"
    "v_mfma_f32_16x16x32_bf16 v[60:63], a[204:207], a[4:7], v[60:63]\n"
    "s_waitcnt vmcnt(14)\n"
    "v_mfma_f32_16x16x32_bf16 v[64:67], a[204:207], a[28:31], v[64:67]\n"
    "ds_read_b128 a[8:11], v88 offset:0\n"
    "ds_read_b128 a[32:35], v88 offset:2048\n"
    "ds_read_b128 a[56:59], v90 offset:0\n"
    "ds_read_b128 a[80:83], v90 offset:2048\n"
    "ds_read_b128 a[12:15], v89 offset:0\n"
    "ds_read_b128 a[36:39], v89 offset:2048\n"
    "ds_read_b128 a[60:63], v91 offset:0\n"
    "ds_read_b128 a[84:87], v91 offset:2048\n"
    "v_mfma_f32_16x16x32_bf16 v[68:71], a[204:207], a[52:55], v[68:71]\n"
    "v_mfma_f32_16x16x32_bf16 v[72:75], a[204:207], a[76:79], v[72:75]\n"
    "s_waitcnt lgkmcnt(7)\n"
    "ds_read_b128 a[204:207], v1 offset:256\n"
    "v_mfma_f32_16x16x32_bf16 v[28:31], a[208:211], a[8:11], v[28:31]\n"
    "s_waitcnt lgkmcnt(7)\n"
    "v_mfma_f32_16x16x32_bf16 v[32:35], a[208:211], a[32:35], v[32:35]\n"
    "s_waitcnt lgkmcnt(6)\n"
    "v_mfma_f32_16x16x32_bf16 v[36:39], a[208:211], a[56:59], v[36:39]\n"
    "s_waitcnt lgkmcnt(5)\n"
    "v_mfma_f32_16x16x32_bf16 v[40:43], a[208:211], a[80:83], v[40:43]\n"
    "s_waitcnt lgkmcnt(4)\n"
    "ds_read_b128 a[208:211], v0 offset:6400\n"
    "v_mfma_f32_16x16x32_bf16 v[28:31], a[212:215], a[12:15], v[28:31]\n"
    "s_waitcnt lgkmcnt(4)\n"
    "buffer_load_dwordx4 v6, s[36:39], s55 offen offset:0 nt lds\n"
    "v_mfma_f32_16x16x32_bf16 v[32:35], a[212:215], a[36:39], v[32:35]\n"
    "s_waitcnt lgkmcnt(3)\n"
    "v_mfma_f32_16x16x32_bf16 v[36:39], a[212:215], a[60:63], v[36:39]\n"
    "s_waitcnt lgkmcnt(2)\n"
    "v_mfma_f32_16x16x32_bf16 v[40:43], a[212:215], a[84:87], v[40:43]\n"
    "ds_read_b128 a[212:215], v1 offset:6400\n"
    "v_mfma_f32_16x16x32_bf16 v[60:63], a[192:195], a[8:11], v[60:63]\n"
    "v_mfma_f32_16x16x32_bf16 v[64:67], a[192:195], a[32:35], v[64:67]\n"
    "v_mfma_f32_16x16x32_bf16 v[68:71], a[192:195], a[56:59], v[68:71]\n"
    "v_mfma_f32_16x16x32_bf16 v[72:75], a[192:195], a[80:83], v[72:75]\n"
    "v_mfma_f32_16x16x32_bf16 v[60:63], a[196:199], a[12:15], v[60:63]\n"
    "s_waitcnt vmcnt(7)\n"
    "v_mfma_f32_16x16x32_bf16 v[64:67], a[196:199], a[36:39], v[64:67]\n"
    "ds_read_b128 a[16:19], v92 offset:0\n"
    "ds_read_b128 a[20:23], v93 offset:0\n"
    "ds_read_b128 a[44:47], v93 offset:2048\n"
    "ds_read_b128 a[40:43], v92 offset:2048\n"
    "ds_read_b128 a[64:67], v94 offset:0\n"
    "ds_read_b128 a[68:71], v95 offset:0\n"
    "ds_read_b128 a[92:95], v95 offset:2048\n"
    "ds_read_b128 a[88:91], v94 offset:2048\n"
    "v_mfma_f32_16x16x32_bf16 v[68:71], a[196:199], a[60:63], v[68:71]\n"
    "buffer_load_dwordx4 v7, s[36:39], s55 offen offset:1024 nt lds\n"
    "v_mfma_f32_16x16x32_bf16 v[72:75], a[196:199], a[84:87], v[72:75]\n"
    "s_waitcnt lgkmcnt(7)\n"
    "v_mfma_f32_16x16x32_bf16 v[28:31], a[200:203], a[16:19], v[28:31]\n"
    "s_waitcnt lgkmcnt(6)\n"
    "v_mfma_f32_16x16x32_bf16 v[28:31], a[204:207], a[20:23], v[28:31]\n"
    "s_waitcnt lgkmcnt(5)\n"
    "v_mfma_f32_16x16x32_bf16 v[32:35], a[204:207], a[44:47], v[32:35]\n"
    "s_waitcnt lgkmcnt(4)\n"
    "v_mfma_f32_16x16x32_bf16 v[32:35], a[200:203], a[40:43], v[32:35]\n"
    "s_waitcnt lgkmcnt(3)\n"
    "v_mfma_f32_16x16x32_bf16 v[36:39], a[200:203], a[64:67], v[36:39]\n"
    "s_waitcnt lgkmcnt(2)\n"
    "v_mfma_f32_16x16x32_bf16 v[36:39], a[204:207], a[68:71], v[36:39]\n"
    "s_waitcnt lgkmcnt(1)\n"
    "v_max3_f32 v12, v28, v29, v30\n"
    "v_max_f32 v12, v12, v31\n"
    "v_mov_b32 v24, v12\n"
    "v_max3_f32 v13, v32, v33, v34\n"
    "v_max_f32 v13, v13, v35\n"
    "v_permlane32_swap_b32 v24, v12\n"
    "v_mov_b32 v25, v13\n"
    "v_mfma_f32_16x16x32_bf16 v[40:43], a[204:207], a[92:95], v[40:43]\n"
    "s_waitcnt lgkmcnt(0)\n"
    "v_max_f32 v12, v12, v24\n"
    "v_permlane32_swap_b32 v25, v13\n"
    "v_mov_b32 v24, v12\n"
    "v_max_f32 v13, v13, v25\n"
    "v_max3_f32 v14, v36, v37, v38\n"
    "v_permlane16_swap_b32 v24, v12\n"
    "v_mfma_f32_16x16x32_bf16 v[40:43], a[200:203], a[88:91], v[40:43]\n"
    "v_max_f32 v12, v12, v24\n"
    "v_mov_b32 v25, v13\n"
    "v_mul_f32 v12, -s52, v12\n"
    "v_max_f32 v14, v14, v39\n"
    "v_add_f32 v12, 0xc2200000, v12\n"
    "v_permlane16_swap_b32 v25, v13\n"
    "v_fma_f32 v28, v28, s52, v12\n"
    "v_mfma_f32_16x16x32_bf16 v[60:63], a[208:211], a[16:19], v[60:63]\n"
    "buffer_load_dwordx4 v8, s[36:39], s55 offen offset:2048 nt lds\n"
    "s_add_u32 m0, s54, 73728\n"
    "v_fma_f32 v29, v29, s52, v12\n"
    "v_exp_f32 v28, v28\n"
    "v_exp_f32 v29, v29\n"
    "v_mfma_f32_16x16x32_bf16 v[60:63], a[212:215], a[20:23], v[60:63]\n"
    "v_fma_f32 v30, v30, s52, v12\n"
    "v_fma_f32 v31, v31, s52, v12\n"
    "v_exp_f32 v30, v30\n"
    "v_exp_f32 v31, v31\n"
    "v_max_f32 v13, v13, v25\n"
    "v_cvt_pk_bf16_f32 v28, v28, v29\n"
    "v_mfma_f32_16x16x32_bf16 v[64:67], a[212:215], a[44:47], v[64:67]\n"
    "v_mul_f32 v13, -s52, v13\n"
    "v_fma_f32 v60, v60, s52, v12\n"
    "v_fma_f32 v61, v61, s52, v12\n"
    "v_fma_f32 v62, v62, s52, v12\n"
    "v_fma_f32 v63, v63, s52, v12\n"
    "v_exp_f32 v60, v60\n"
    "v_cvt_pk_bf16_f32 v29, v30, v31\n"
    "v_mfma_f32_16x16x32_bf16 v[64:67], a[208:211], a[40:43], v[64:67]\n"
    "v_exp_f32 v61, v61\n"
    "v_exp_f32 v62, v62\n"
    "v_exp_f32 v63, v63\n"
    "v_add_f32 v13, 0xc2200000, v13\n"
    "v_cvt_pk_bf16_f32 v30, v60, v61\n"
    "v_mfma_f32_16x16x32_bf16 v[68:71], a[208:211], a[64:67], v[68:71]\n"
    "v_cvt_pk_bf16_f32 v31, v62, v63\n"
    "v_fma_f32 v32, v32, s52, v13\n"
    "v_fma_f32 v33, v33, s52, v13\n"
    "v_exp_f32 v32, v32\n"
    "v_exp_f32 v33, v33\n"
    "v_fma_f32 v34, v34, s52, v13\n"
    "v_mfma_f32_16x16x32_bf16 v[68:71], a[212:215], a[68:71], v[68:71]\n"
    "s_waitcnt vmcnt(6)\n"
    "v_fma_f32 v35, v35, s52, v13\n"
    "v_exp_f32 v34, v34\n"
    "v_exp_f32 v35, v35\n"
    "v_fma_f32 v64, v64, s52, v13\n"
    "v_fma_f32 v65, v65, s52, v13\n"
    "v_fma_f32 v66, v66, s52, v13\n"
    "v_mfma_f32_16x16x32_bf16 v[72:75], a[212:215], a[92:95], v[72:75]\n"
    "s_barrier\n"
    "ds_read_b128 a[192:195], v0 offset:12288\n"
    "ds_read_b128 a[196:199], v1 offset:12288\n"
    "ds_read_b128 a[200:203], v0 offset:12416\n"
    "ds_read_b128 a[204:207], v1 offset:12416\n"
    "ds_read_b128 a[212:215], v1 offset:12544\n"
    "v_fma_f32 v67, v67, s52, v13\n"
    "v_cvt_pk_bf16_f32 v32, v32, v33\n"
    "v_max3_f32 v15, v40, v41, v42\n"
    "v_mfma_f32_16x16x32_bf16 v[72:75], a[208:211], a[88:91], v[72:75]\n"
    "s_waitcnt lgkmcnt(4)\n"
    "ds_read_b128 a[208:211], v0 offset:12544\n"
    "v_exp_f32 v64, v64\n"
    "v_exp_f32 v65, v65\n"
    "v_exp_f32 v66, v66\n"
    "v_cvt_pk_bf16_f32 v33, v34, v35\n"
    "v_mfma_f32_16x16x32_bf16 v[44:47], a[192:195], a[0:3], 0\n"
    "s_waitcnt lgkmcnt(4)\n"
    "v_exp_f32 v67, v67\n"
    "v_mfma_f32_16x16x32_bf16 v[44:47], a[196:199], a[4:7], v[44:47]\n"
    "s_waitcnt lgkmcnt(3)\n"
    "v_cvt_pk_bf16_f32 v34, v64, v65\n"
    "v_cvt_pk_bf16_f32 v35, v66, v67\n"
    "v_mfma_f32_16x16x32_bf16 v[44:47], a[200:203], a[8:11], v[44:47]\n"
    "buffer_load_dwordx4 v6, s[40:43], s56 offen offset:0 nt lds\n"
    "v_mfma_f32_16x16x32_bf16 v[48:51], a[200:203], a[32:35], 0\n"
    "v_max_f32 v15, v15, v43\n"
    "v_mov_b32 v26, v14\n"
    "v_mfma_f32_16x16x32_bf16 v[48:51], a[196:199], a[28:31], v[48:51]\n"
    "v_mov_b32 v27, v15\n"
    "v_mfma_f32_16x16x32_bf16 v[48:51], a[192:195], a[24:27], v[48:51]\n"
    "v_permlane32_swap_b32 v26, v14\n"
    "v_mfma_f32_16x16x32_bf16 v[52:55], a[192:195], a[48:51], 0\n"
    "v_permlane32_swap_b32 v27, v15\n"
    "v_mfma_f32_16x16x32_bf16 v[52:55], a[196:199], a[52:55], v[52:55]\n"
    "v_max_f32 v15, v15, v27\n"
    "v_max_f32 v14, v14, v26\n"
    "v_mfma_f32_16x16x32_bf16 v[52:55], a[200:203], a[56:59], v[52:55]\n"
    "v_mov_b32 v27, v15\n"
    "v_mov_b32 v26, v14\n"
    "v_mfma_f32_16x16x32_bf16 v[56:59], a[200:203], a[80:83], 0\n"
    "v_permlane16_swap_b32 v27, v15\n"
    "v_mfma_f32_16x16x32_bf16 v[56:59], a[196:199], a[76:79], v[56:59]\n"
    "ds_read_b128 a[196:199], v1 offset:18432\n"
    "v_max_f32 v15, v15, v27\n"
    "v_mfma_f32_16x16x32_bf16 v[56:59], a[192:195], a[72:75], v[56:59]\n"
    "s_waitcnt lgkmcnt(3)\n"
    "ds_read_b128 a[192:195], v0 offset:18432\n"
    "v_mul_f32 v15, -s52, v15\n"
    "v_mfma_f32_16x16x32_bf16 v[44:47], a[204:207], a[12:15], v[44:47]\n"
    "s_waitcnt lgkmcnt(2)\n"
    "buffer_load_dwordx4 v7, s[40:43], s56 offen offset:1024 nt lds\n"
    "v_mfma_f32_16x16x32_bf16 v[44:47], a[208:211], a[16:19], v[44:47]\n"
    "ds_read_b128 a[200:203], v0 offset:18560\n"
    "v_add_f32 v15, 0xc2200000, v15\n"
    "v_mfma_f32_16x16x32_bf16 v[44:47], a[212:215], a[20:23], v[44:47]\n"
    "v_permlane16_swap_b32 v26, v14\n"
    "v_mfma_f32_16x16x32_bf16 v[48:51], a[212:215], a[44:47], v[48:51]\n"
    "v_max_f32 v14, v14, v26\n"
    "v_mov_b32 v27, 0xf149f2ca\n"
    "v_mfma_f32_16x16x32_bf16 v[48:51], a[208:211], a[40:43], v[48:51]\n"
    "v_mul_f32 v14, -s52, v14\n"
    "v_cndmask_b32 v15, v15, v27, s[60:61]\n"
    "v_mfma_f32_16x16x32_bf16 v[48:51], a[204:207], a[36:39], v[48:51]\n"
    "v_add_f32 v14, 0xc2200000, v14\n"
    "v_fma_f32 v40, v40, s52, v15\n"
    "v_mfma_f32_16x16x32_bf16 v[52:55], a[204:207], a[60:63], v[52:55]\n"
    "v_fma_f32 v36, v36, s52, v14\n"
    "v_fma_f32 v37, v37, s52, v14\n"
    "v_mfma_f32_16x16x32_bf16 v[52:55], a[208:211], a[64:67], v[52:55]\n"
    "v_fma_f32 v41, v41, s52, v15\n"
    "v_fma_f32 v38, v38, s52, v14\n"
    "v_mfma_f32_16x16x32_bf16 v[52:55], a[212:215], a[68:71], v[52:55]\n"
    "v_exp_f32 v36, v36\n"
    "v_mfma_f32_16x16x32_bf16 v[56:59], a[212:215], a[92:95], v[56:59]\n"
    "ds_read_b128 a[212:215], v1 offset:18688\n"
    "v_fma_f32 v39, v39, s52, v14\n"
    "v_mfma_f32_16x16x32_bf16 v[56:59], a[208:211], a[88:91], v[56:59]\n"
    "buffer_load_dwordx4 v8, s[40:43], s56 offen offset:2048 nt lds\n"
    "v_mfma_f32_16x16x32_bf16 v[56:59], a[204:207], a[84:87], v[56:59]\n"
    "s_waitcnt lgkmcnt(2)\n"
    "ds_read_b128 a[204:207], v1 offset:18560\n"
    "v_fma_f32 v42, v42, s52, v15\n"
    "v_mfma_f32_16x16x32_bf16 v[76:79], a[192:195], a[0:3], 0\n"
    "s_add_u32 s56, s56, 24576\n"
    "ds_read_b128 a[208:211], v0 offset:18688\n"
    "v_fma_f32 v43, v43, s52, v15\n"
    "v_mfma_f32_16x16x32_bf16 v[76:79], a[196:199], a[4:7], v[76:79]\n"
    "s_waitcnt lgkmcnt(3)\n"
    "v_exp_f32 v37, v37\n"
    "v_mfma_f32_16x16x32_bf16 v[76:79], a[200:203], a[8:11], v[76:79]\n"
    "s_add_u32 m0, s54, 36864\n"
    "v_exp_f32 v40, v40\n"
    "v_mfma_f32_16x16x32_bf16 v[80:83], a[200:203], a[32:35], 0\n"
    "v_exp_f32 v41, v41\n"
    "v_mfma_f32_16x16x32_bf16 v[80:83], a[196:199], a[28:31], v[80:83]\n"
    "v_exp_f32 v38, v38\n"
    "v_mfma_f32_16x16x32_bf16 v[80:83], a[192:195], a[24:27], v[80:83]\n"
    "v_exp_f32 v39, v39\n"
    "v_mfma_f32_16x16x32_bf16 v[60:63], a[192:195], a[48:51], 0\n"
    "v_exp_f32 v42, v42\n"
    "v_mfma_f32_16x16x32_bf16 v[60:63], a[196:199], a[52:55], v[60:63]\n"
    "v_exp_f32 v43, v43\n"
    "v_mfma_f32_16x16x32_bf16 v[60:63], a[200:203], a[56:59], v[60:63]\n"
    "v_fma_f32 v68, v68, s52, v14\n"
    "v_fma_f32 v69, v69, s52, v14\n"
    "v_mfma_f32_16x16x32_bf16 v[64:67], a[200:203], a[80:83], 0\n"
    "v_fma_f32 v70, v70, s52, v14\n"
    "v_fma_f32 v71, v71, s52, v14\n"
    "v_mfma_f32_16x16x32_bf16 v[64:67], a[196:199], a[76:79], v[64:67]\n"
    "v_fma_f32 v72, v72, s52, v15\n"
    "v_fma_f32 v73, v73, s52, v15\n"
    "v_mfma_f32_16x16x32_bf16 v[64:67], a[192:195], a[72:75], v[64:67]\n"
    "s_waitcnt vmcnt(6) lgkmcnt(1)\n"
    "v_fma_f32 v74, v74, s52, v15\n"
    "v_fma_f32 v75, v75, s52, v15\n"
    "v_mfma_f32_16x16x32_bf16 v[76:79], a[204:207], a[12:15], v[76:79]\n"
    "s_waitcnt lgkmcnt(0)\n"
    "v_cvt_pk_bf16_f32 v36, v36, v37\n"
    "v_cvt_pk_bf16_f32 v40, v40, v41\n"
    "v_mfma_f32_16x16x32_bf16 v[76:79], a[208:211], a[16:19], v[76:79]\n"
    "s_barrier\n"
    "ds_read_b64_tr_b16 a[192:193], v2 offset:0\n"
    "v_cvt_pk_bf16_f32 v37, v38, v39\n"
    "v_mfma_f32_16x16x32_bf16 v[76:79], a[212:215], a[20:23], v[76:79]\n"
    "ds_read_b64_tr_b16 a[194:195], v2 offset:6144\n"
    "v_cvt_pk_bf16_f32 v41, v42, v43\n"
    "v_mfma_f32_16x16x32_bf16 v[80:83], a[212:215], a[44:47], v[80:83]\n"
    "ds_read_b64_tr_b16 a[196:197], v3 offset:0\n"
    "v_fma_f32 v44, v44, s52, v12\n"
    "v_mfma_f32_16x16x32_bf16 v[80:83], a[208:211], a[40:43], v[80:83]\n"
    "ds_read_b64_tr_b16 a[198:199], v3 offset:6144\n"
    "v_fma_f32 v45, v45, s52, v12\n"
    "v_mfma_f32_16x16x32_bf16 v[80:83], a[204:207], a[36:39], v[80:83]\n"
    "v_exp_f32 v68, v68\n"
    "v_mfma_f32_16x16x32_bf16 v[60:63], a[204:207], a[60:63], v[60:63]\n"
    "v_exp_f32 v69, v69\n"
    "v_mfma_f32_16x16x32_bf16 v[60:63], a[208:211], a[64:67], v[60:63]\n"
    "v_exp_f32 v70, v70\n"
    "v_mfma_f32_16x16x32_bf16 v[60:63], a[212:215], a[68:71], v[60:63]\n"
    "v_exp_f32 v71, v71\n"
    "v_mfma_f32_16x16x32_bf16 v[64:67], a[212:215], a[92:95], v[64:67]\n"
    "v_exp_f32 v72, v72\n"
    "v_mfma_f32_16x16x32_bf16 v[64:67], a[208:211], a[88:91], v[64:67]\n"
    "v_exp_f32 v73, v73\n"
    "v_mfma_f32_16x16x32_bf16 v[64:67], a[204:207], a[84:87], v[64:67]\n"
    "v_exp_f32 v74, v74\n"
    "v_smfmac_f32_16x16x64_bf16 a[236:239], a[252:255], v[28:35], v23\n"
    "v_exp_f32 v75, v75\n"
    "v_cvt_pk_bf16_f32 v38, v68, v69\n"
    "v_cvt_pk_bf16_f32 v39, v70, v71\n"
    "v_cvt_pk_bf16_f32 v42, v72, v73\n"
    "v_cvt_pk_bf16_f32 v43, v74, v75\n"
    "s_nop 1\n"
    "v_smfmac_f32_16x16x64_bf16 a[240:243], a[252:255], v[36:43], v23\n"
    "s_waitcnt lgkmcnt(2)\n"
    "ds_read_b64_tr_b16 a[200:201], v4 offset:0\n"
    "v_fma_f32 v46, v46, s52, v12\n"
    "v_mfma_f32_16x16x32_bf16 v[96:99], a[192:195], v[40:43], 0\n"
    "ds_read_b64_tr_b16 a[202:203], v4 offset:6144\n"
    "v_fma_f32 v47, v47, s52, v12\n"
    "v_mfma_f32_16x16x32_bf16 v[92:95], a[192:195], v[36:39], 0\n"
    "ds_read_b64_tr_b16 a[204:205], v5 offset:0\n"
    "v_fma_f32 v76, v76, s52, v12\n"
    "v_mfma_f32_16x16x32_bf16 v[88:91], a[192:195], v[32:35], 0\n"
    "ds_read_b64_tr_b16 a[206:207], v5 offset:6144\n"
    "v_fma_f32 v77, v77, s52, v12\n"
    "v_mfma_f32_16x16x32_bf16 v[84:87], a[192:195], v[28:31], 0\n"
    "s_waitcnt lgkmcnt(4)\n"
    "ds_read_b64_tr_b16 a[208:209], v2 offset:128\n"
    "v_fma_f32 v78, v78, s52, v12\n"
    "v_mfma_f32_16x16x32_bf16 a[216:219], a[196:199], v[28:31], 0\n"
    "ds_read_b64_tr_b16 a[210:211], v2 offset:6272\n"
    "v_fma_f32 v79, v79, s52, v12\n"
    "v_mfma_f32_16x16x32_bf16 a[220:223], a[196:199], v[32:35], 0\n"
    "ds_read_b64_tr_b16 a[212:213], v3 offset:128\n"
    "v_fma_f32 v48, v48, s52, v13\n"
    "v_mfma_f32_16x16x32_bf16 a[224:227], a[196:199], v[36:39], 0\n"
    "ds_read_b64_tr_b16 a[214:215], v3 offset:6272\n"
    "v_fma_f32 v49, v49, s52, v13\n"
    "v_mfma_f32_16x16x32_bf16 a[228:231], a[196:199], v[40:43], 0\n"
    "s_waitcnt lgkmcnt(6)\n"
    "ds_read_b64_tr_b16 a[192:193], v4 offset:128\n"
    "v_fma_f32 v50, v50, s52, v13\n"
    "v_mfma_f32_16x16x32_bf16 v[112:115], a[200:203], v[40:43], 0\n"
    "ds_read_b64_tr_b16 a[194:195], v4 offset:6272\n"
    "v_fma_f32 v51, v51, s52, v13\n"
    "v_mfma_f32_16x16x32_bf16 v[108:111], a[200:203], v[36:39], 0\n"
    "ds_read_b64_tr_b16 a[196:197], v5 offset:128\n"
    "v_fma_f32 v80, v80, s52, v13\n"
    "v_mfma_f32_16x16x32_bf16 v[104:107], a[200:203], v[32:35], 0\n"
    "ds_read_b64_tr_b16 a[198:199], v5 offset:6272\n"
    "v_fma_f32 v81, v81, s52, v13\n"
    "v_mfma_f32_16x16x32_bf16 v[100:103], a[200:203], v[28:31], 0\n"
    "s_waitcnt lgkmcnt(8)\n"
    "ds_read_b64_tr_b16 a[200:201], v2 offset:256\n"
    "v_fma_f32 v82, v82, s52, v13\n"
    "v_mfma_f32_16x16x32_bf16 v[116:119], a[204:207], v[28:31], 0\n"
    "buffer_load_dwordx4 v6, s[36:39], s56 offen offset:0 nt lds\n"
    "v_mfma_f32_16x16x32_bf16 v[120:123], a[204:207], v[32:35], 0\n"
    "ds_read_b64_tr_b16 a[202:203], v2 offset:6400\n"
    "v_fma_f32 v83, v83, s52, v13\n"
    "v_mfma_f32_16x16x32_bf16 v[124:127], a[204:207], v[36:39], 0\n"
    "v_exp_f32 v44, v44\n"
    "v_mfma_f32_16x16x32_bf16 v[128:131], a[204:207], v[40:43], 0\n"
    "s_waitcnt lgkmcnt(8)\n"
    "ds_read_b64_tr_b16 a[204:205], v3 offset:256\n"
    "v_fma_f32 v52, v52, s52, v14\n"
    "v_mfma_f32_16x16x32_bf16 v[144:147], a[208:211], v[40:43], 0\n"
    "ds_read_b64_tr_b16 a[206:207], v3 offset:6400\n"
    "v_fma_f32 v53, v53, s52, v14\n"
    "v_mfma_f32_16x16x32_bf16 v[140:143], a[208:211], v[36:39], 0\n"
    "v_exp_f32 v45, v45\n"
    "v_mfma_f32_16x16x32_bf16 v[136:139], a[208:211], v[32:35], 0\n"
    "v_exp_f32 v46, v46\n"
    "v_mfma_f32_16x16x32_bf16 v[132:135], a[208:211], v[28:31], 0\n"
    "s_waitcnt lgkmcnt(8)\n"
    "ds_read_b64_tr_b16 a[208:209], v4 offset:256\n"
    "v_cvt_pk_bf16_f32 v44, v44, v45\n"
    "v_mfma_f32_16x16x32_bf16 a[232:235], a[212:215], v[28:31], 0\n"
    "ds_read_b64_tr_b16 a[210:211], v4 offset:6400\n"
    "v_fma_f32 v56, v56, s52, v15\n"
    "v_mfma_f32_16x16x32_bf16 v[148:151], a[212:215], v[32:35], 0\n"
    "v_exp_f32 v47, v47\n"
    "v_mfma_f32_16x16x32_bf16 v[152:155], a[212:215], v[36:39], 0\n"
    "buffer_load_dwordx4 v7, s[36:39], s56 offen offset:1024 nt lds\n"
    "v_mfma_f32_16x16x32_bf16 v[156:159], a[212:215], v[40:43], 0\n"
    "s_waitcnt lgkmcnt(8)\n"
    "ds_read_b64_tr_b16 a[212:213], v5 offset:256\n"
    "v_cvt_pk_bf16_f32 v45, v46, v47\n"
    "v_mfma_f32_16x16x32_bf16 v[172:175], a[192:195], v[40:43], 0\n"
    "ds_read_b64_tr_b16 a[214:215], v5 offset:6400\n"
    "v_fma_f32 v57, v57, s52, v15\n"
    "v_mfma_f32_16x16x32_bf16 v[168:171], a[192:195], v[36:39], 0\n"
    "v_exp_f32 v76, v76\n"
    "v_mfma_f32_16x16x32_bf16 v[164:167], a[192:195], v[32:35], 0\n"
    "v_exp_f32 v77, v77\n"
    "v_mfma_f32_16x16x32_bf16 v[160:163], a[192:195], v[28:31], 0\n"
    "s_waitcnt lgkmcnt(8)\n"
    "v_exp_f32 v78, v78\n"
    "v_mfma_f32_16x16x32_bf16 v[176:179], a[196:199], v[28:31], 0\n"
    "v_exp_f32 v79, v79\n"
    "v_mfma_f32_16x16x32_bf16 v[180:183], a[196:199], v[32:35], 0\n"
    "v_cvt_pk_bf16_f32 v46, v76, v77\n"
    "v_cvt_pk_bf16_f32 v47, v78, v79\n"
    "v_mfma_f32_16x16x32_bf16 v[184:187], a[196:199], v[36:39], 0\n"
    "s_waitcnt vmcnt(2) lgkmcnt(0)\n"
    "v_exp_f32 v48, v48\n"
    "v_mfma_f32_16x16x32_bf16 v[188:191], a[196:199], v[40:43], 0\n"
    "s_barrier\n"
    "ds_read_b128 a[192:195], v0 offset:24576\n"
    "v_fma_f32 v54, v54, s52, v14\n"
    "v_mfma_f32_16x16x32_bf16 v[204:207], a[200:203], v[40:43], 0\n"
    "buffer_load_dwordx4 v8, s[36:39], s56 offen offset:2048 nt lds\n"
    "v_mfma_f32_16x16x32_bf16 v[200:203], a[200:203], v[36:39], 0\n"
    "s_add_u32 m0, s54, 86016\n"
    "ds_read_b128 a[196:199], v1 offset:24576\n"
    "v_fma_f32 v55, v55, s52, v14\n"
    "v_mfma_f32_16x16x32_bf16 v[196:199], a[200:203], v[32:35], 0\n"
    "v_exp_f32 v49, v49\n"
    "v_mfma_f32_16x16x32_bf16 v[192:195], a[200:203], v[28:31], 0\n"
    "ds_read_b128 a[200:203], v0 offset:24704\n"
    "v_cvt_pk_bf16_f32 v48, v48, v49\n"
    "v_mfma_f32_16x16x32_bf16 v[208:211], a[204:207], v[28:31], 0\n"
    "v_exp_f32 v50, v50\n"
    "v_mfma_f32_16x16x32_bf16 v[212:215], a[204:207], v[32:35], 0\n"
    "v_exp_f32 v51, v51\n"
    "v_mfma_f32_16x16x32_bf16 v[216:219], a[204:207], v[36:39], 0\n"
    "v_exp_f32 v80, v80\n"
    "v_mfma_f32_16x16x32_bf16 v[220:223], a[204:207], v[40:43], 0\n"
    "ds_read_b128 a[204:207], v1 offset:24704\n"
    "v_cvt_pk_bf16_f32 v49, v50, v51\n"
    "v_mfma_f32_16x16x32_bf16 v[236:239], a[208:211], v[40:43], 0\n"
    "v_exp_f32 v81, v81\n"
    "v_mfma_f32_16x16x32_bf16 v[232:235], a[208:211], v[36:39], 0\n"
    "v_exp_f32 v82, v82\n"
    "v_mfma_f32_16x16x32_bf16 v[228:231], a[208:211], v[32:35], 0\n"
    "buffer_load_dwordx4 v6, s[40:43], s55 offen offset:0 nt lds\n"
    "v_mfma_f32_16x16x32_bf16 v[224:227], a[208:211], v[28:31], 0\n"
    "ds_read_b128 a[208:211], v0 offset:24832\n"
    "v_cvt_pk_bf16_f32 v50, v80, v81\n"
    "v_mfma_f32_16x16x32_bf16 v[240:243], a[212:215], v[28:31], 0\n"
    "v_exp_f32 v83, v83\n"
    "v_mfma_f32_16x16x32_bf16 v[244:247], a[212:215], v[32:35], 0\n"
    "v_cvt_pk_bf16_f32 v51, v82, v83\n"
    "v_fma_f32 v58, v58, s52, v15\n"
    "v_mfma_f32_16x16x32_bf16 v[248:251], a[212:215], v[36:39], 0\n"
    "v_exp_f32 v52, v52\n"
    "v_mfma_f32_16x16x32_bf16 v[252:255], a[212:215], v[40:43], 0\n"
    "s_waitcnt lgkmcnt(4)\n"
    "ds_read_b128 a[212:215], v1 offset:24832\n"
    "v_fma_f32 v59, v59, s52, v15\n"
    "v_mfma_f32_16x16x32_bf16 v[28:31], a[192:195], a[0:3], 0\n"
    "s_waitcnt lgkmcnt(4)\n"
    "v_exp_f32 v53, v53\n"
    "v_mfma_f32_16x16x32_bf16 v[28:31], a[196:199], a[4:7], v[28:31]\n"
    "s_waitcnt lgkmcnt(3)\n"
    "v_exp_f32 v56, v56\n"
    "v_mfma_f32_16x16x32_bf16 v[28:31], a[200:203], a[8:11], v[28:31]\n"
    "v_exp_f32 v57, v57\n"
    "v_mfma_f32_16x16x32_bf16 v[32:35], a[200:203], a[32:35], 0\n"
    "v_exp_f32 v54, v54\n"
    "v_mfma_f32_16x16x32_bf16 v[32:35], a[196:199], a[28:31], v[32:35]\n"
    "buffer_load_dwordx4 v7, s[40:43], s55 offen offset:1024 nt lds\n"
    "v_mfma_f32_16x16x32_bf16 v[32:35], a[192:195], a[24:27], v[32:35]\n"
    "v_exp_f32 v55, v55\n"
    "v_mfma_f32_16x16x32_bf16 v[36:39], a[192:195], a[48:51], 0\n"
    "v_exp_f32 v58, v58\n"
    "v_mfma_f32_16x16x32_bf16 v[36:39], a[196:199], a[52:55], v[36:39]\n"
    "v_exp_f32 v59, v59\n"
    "v_mfma_f32_16x16x32_bf16 v[36:39], a[200:203], a[56:59], v[36:39]\n"
    "v_fma_f32 v60, v60, s52, v14\n"
    "v_fma_f32 v61, v61, s52, v14\n"
    "v_mfma_f32_16x16x32_bf16 v[40:43], a[200:203], a[80:83], 0\n"
    "ds_read_b128 a[200:203], v0 offset:30848\n"
    "v_fma_f32 v62, v62, s52, v14\n"
    "v_mfma_f32_16x16x32_bf16 v[40:43], a[196:199], a[76:79], v[40:43]\n"
    "ds_read_b128 a[196:199], v1 offset:30720\n"
    "v_fma_f32 v63, v63, s52, v14\n"
    "v_mfma_f32_16x16x32_bf16 v[40:43], a[192:195], a[72:75], v[40:43]\n"
    "s_waitcnt lgkmcnt(4)\n"
    "ds_read_b128 a[192:195], v0 offset:30720\n"
    "v_fma_f32 v64, v64, s52, v15\n"
    "v_mfma_f32_16x16x32_bf16 v[28:31], a[204:207], a[12:15], v[28:31]\n"
    "s_waitcnt lgkmcnt(4)\n"
    "v_fma_f32 v65, v65, s52, v15\n"
    "v_fma_f32 v66, v66, s52, v15\n"
    "v_mfma_f32_16x16x32_bf16 v[28:31], a[208:211], a[16:19], v[28:31]\n"
    "s_waitcnt lgkmcnt(3)\n"
    "v_fma_f32 v67, v67, s52, v15\n"
    "v_cvt_pk_bf16_f32 v52, v52, v53\n"
    "v_mfma_f32_16x16x32_bf16 v[28:31], a[212:215], a[20:23], v[28:31]\n"
    "buffer_load_dwordx4 v8, s[40:43], s55 offen offset:2048 nt lds\n"
    "v_mfma_f32_16x16x32_bf16 v[32:35], a[212:215], a[44:47], v[32:35]\n"
    "s_add_u32 s55, s55, 24576\n"
    "v_cvt_pk_bf16_f32 v56, v56, v57\n"
    "v_cvt_pk_bf16_f32 v53, v54, v55\n"
    "v_mfma_f32_16x16x32_bf16 v[32:35], a[208:211], a[40:43], v[32:35]\n"
    "s_add_u32 m0, s54, 49152\n"
    "v_exp_f32 v60, v60\n"
    "v_mfma_f32_16x16x32_bf16 v[32:35], a[204:207], a[36:39], v[32:35]\n"
    "v_exp_f32 v61, v61\n"
    "v_mfma_f32_16x16x32_bf16 v[36:39], a[204:207], a[60:63], v[36:39]\n"
    "v_exp_f32 v62, v62\n"
    "v_mfma_f32_16x16x32_bf16 v[36:39], a[208:211], a[64:67], v[36:39]\n"
    "v_exp_f32 v63, v63\n"
    "v_mfma_f32_16x16x32_bf16 v[36:39], a[212:215], a[68:71], v[36:39]\n"
    "v_exp_f32 v64, v64\n"
    "v_mfma_f32_16x16x32_bf16 v[40:43], a[212:215], a[92:95], v[40:43]\n"
    "ds_read_b128 a[212:215], v1 offset:30976\n"
    "v_cvt_pk_bf16_f32 v57, v58, v59\n"
    "v_mfma_f32_16x16x32_bf16 v[40:43], a[208:211], a[88:91], v[40:43]\n"
    "ds_read_b128 a[208:211], v0 offset:30976\n"
    "v_cvt_pk_bf16_f32 v54, v60, v61\n"
    "v_mfma_f32_16x16x32_bf16 v[40:43], a[204:207], a[84:87], v[40:43]\n"
    "s_waitcnt lgkmcnt(2)\n"
    "ds_read_b128 a[204:207], v1 offset:30848\n"
    "v_cvt_pk_bf16_f32 v55, v62, v63\n"
    "v_mfma_f32_16x16x32_bf16 v[68:71], a[192:195], a[0:3], 0\n"
    "v_exp_f32 v65, v65\n"
    "v_mfma_f32_16x16x32_bf16 v[68:71], a[196:199], a[4:7], v[68:71]\n"
    "v_exp_f32 v66, v66\n"
    "v_mfma_f32_16x16x32_bf16 v[68:71], a[200:203], a[8:11], v[68:71]\n"
    "v_exp_f32 v67, v67\n"
    "v_mfma_f32_16x16x32_bf16 v[72:75], a[200:203], a[32:35], 0\n"
    "v_cvt_pk_bf16_f32 v58, v64, v65\n"
    "v_cvt_pk_bf16_f32 v59, v66, v67\n"
    "v_mfma_f32_16x16x32_bf16 v[72:75], a[196:199], a[28:31], v[72:75]\n"
    "v_fma_f32 v28, v28, s52, v12\n"
    "v_fma_f32 v29, v29, s52, v12\n"
    "v_mfma_f32_16x16x32_bf16 v[72:75], a[192:195], a[24:27], v[72:75]\n"
    "v_exp_f32 v28, v28\n"
    "v_mfma_f32_16x16x32_bf16 v[76:79], a[192:195], a[48:51], 0\n"
    "v_exp_f32 v29, v29\n"
    "v_mfma_f32_16x16x32_bf16 v[76:79], a[196:199], a[52:55], v[76:79]\n"
    "buffer_load_dwordx4 v6, s[36:39], s55 offen offset:0 nt lds\n"
    "v_mfma_f32_16x16x32_bf16 v[76:79], a[200:203], a[56:59], v[76:79]\n"
    "v_fma_f32 v30, v30, s52, v12\n"
    "v_fma_f32 v31, v31, s52, v12\n"
    "v_mfma_f32_16x16x32_bf16 v[80:83], a[200:203], a[80:83], 0\n"
    "ds_read_b64_tr_b16 a[200:201], v4 offset:12288\n"
    "v_cvt_pk_bf16_f32 v28, v28, v29\n"
    "v_mfma_f32_16x16x32_bf16 v[80:83], a[196:199], a[76:79], v[80:83]\n"
    "ds_read_b64_tr_b16 a[196:197], v3 offset:12288\n"
    "v_fma_f32 v32, v32, s52, v13\n"
    "v_mfma_f32_16x16x32_bf16 v[80:83], a[192:195], a[72:75], v[80:83]\n"
    "s_waitcnt lgkmcnt(2)\n"
    "ds_read_b64_tr_b16 a[192:193], v2 offset:12288\n"
    "v_fma_f32 v33, v33, s52, v13\n"
    "v_mfma_f32_16x16x32_bf16 v[68:71], a[204:207], a[12:15], v[68:71]\n"
    "ds_read_b64_tr_b16 a[194:195], v2 offset:18432\n"
    "v_fma_f32 v34, v34, s52, v13\n"
    "v_mfma_f32_16x16x32_bf16 v[68:71], a[208:211], a[16:19], v[68:71]\n"
    "ds_read_b64_tr_b16 a[198:199], v3 offset:18432\n"
    "v_fma_f32 v35, v35, s52, v13\n"
    "v_mfma_f32_16x16x32_bf16 v[68:71], a[212:215], a[20:23], v[68:71]\n"
    "ds_read_b64_tr_b16 a[202:203], v4 offset:18432\n"
    "v_fma_f32 v36, v36, s52, v14\n"
    "v_mfma_f32_16x16x32_bf16 v[72:75], a[212:215], a[44:47], v[72:75]\n"
    "v_exp_f32 v30, v30\n"
    "v_mfma_f32_16x16x32_bf16 v[72:75], a[208:211], a[40:43], v[72:75]\n"
    "v_exp_f32 v31, v31\n"
    "v_mfma_f32_16x16x32_bf16 v[72:75], a[204:207], a[36:39], v[72:75]\n"
    "v_cvt_pk_bf16_f32 v29, v30, v31\n"
    "v_fma_f32 v68, v68, s52, v12\n"
    "v_mfma_f32_16x16x32_bf16 v[76:79], a[204:207], a[60:63], v[76:79]\n"
    "v_fma_f32 v69, v69, s52, v12\n"
    "v_fma_f32 v70, v70, s52, v12\n"
    "v_mfma_f32_16x16x32_bf16 v[76:79], a[208:211], a[64:67], v[76:79]\n"
    "v_fma_f32 v71, v71, s52, v12\n"
    "v_fma_f32 v37, v37, s52, v14\n"
    "v_mfma_f32_16x16x32_bf16 v[76:79], a[212:215], a[68:71], v[76:79]\n"
    "v_exp_f32 v68, v68\n"
    "v_mfma_f32_16x16x32_bf16 v[80:83], a[212:215], a[92:95], v[80:83]\n"
    "buffer_load_dwordx4 v7, s[36:39], s55 offen offset:1024 nt lds\n"
    "v_mfma_f32_16x16x32_bf16 v[80:83], a[208:211], a[88:91], v[80:83]\n"
    "ds_read_b64_tr_b16 a[208:209], v2 offset:12416\n"
    "v_fma_f32 v72, v72, s52, v13\n"
    "v_mfma_f32_16x16x32_bf16 v[80:83], a[204:207], a[84:87], v[80:83]\n"
    "ds_read_b64_tr_b16 a[204:205], v5 offset:12288\n"
    "v_fma_f32 v73, v73, s52, v13\n"
    "v_smfmac_f32_16x16x64_bf16 a[236:239], a[252:255], v[44:51], v23\n"
    "ds_read_b64_tr_b16 a[206:207], v5 offset:18432\n"
    "v_fma_f32 v74, v74, s52, v13\n"
    "v_smfmac_f32_16x16x64_bf16 a[240:243], a[252:255], v[52:59], v23\n"
    "s_waitcnt lgkmcnt(5)\n"
    "ds_read_b64_tr_b16 a[210:211], v2 offset:18560\n"
    "v_fma_f32 v75, v75, s52, v13\n"
    "v_mfma_f32_16x16x32_bf16 v[96:99], a[192:195], v[56:59], v[96:99]\n"
    "ds_read_b64_tr_b16 a[212:213], v3 offset:12416\n"
    "v_fma_f32 v40, v40, s52, v15\n"
    "v_mfma_f32_16x16x32_bf16 v[92:95], a[192:195], v[52:55], v[92:95]\n"
    "ds_read_b64_tr_b16 a[214:215], v3 offset:18560\n"
    "v_fma_f32 v41, v41, s52, v15\n"
    "v_mfma_f32_16x16x32_bf16 v[88:91], a[192:195], v[48:51], v[88:91]\n"
    "v_exp_f32 v69, v69\n"
    "v_mfma_f32_16x16x32_bf16 v[84:87], a[192:195], v[44:47], v[84:87]\n"
    "s_waitcnt lgkmcnt(7)\n"
    "ds_read_b64_tr_b16 a[192:193], v4 offset:12416\n"
    "v_cvt_pk_bf16_f32 v30, v68, v69\n"
    "v_mfma_f32_16x16x32_bf16 a[216:219], a[196:199], v[44:47], a[216:219]\n"
    "ds_read_b64_tr_b16 a[194:195], v4 offset:18560\n"
    "v_fma_f32 v38, v38, s52, v14\n"
    "v_mfma_f32_16x16x32_bf16 a[220:223], a[196:199], v[48:51], a[220:223]\n"
    "v_exp_f32 v70, v70\n"
    "v_mfma_f32_16x16x32_bf16 a[224:227], a[196:199], v[52:55], a[224:227]\n"
    "v_exp_f32 v71, v71\n"
    "v_mfma_f32_16x16x32_bf16 a[228:231], a[196:199], v[56:59], a[228:231]\n"
    "s_waitcnt lgkmcnt(8)\n"
    "ds_read_b64_tr_b16 a[196:197], v5 offset:12416\n"
    "v_cvt_pk_bf16_f32 v31, v70, v71\n"
    "v_mfma_f32_16x16x32_bf16 v[112:115], a[200:203], v[56:59], v[112:115]\n"
    "ds_read_b64_tr_b16 a[198:199], v5 offset:18560\n"
    "v_fma_f32 v39, v39, s52, v14\n"
    "v_mfma_f32_16x16x32_bf16 v[108:111], a[200:203], v[52:55], v[108:111]\n"
    "buffer_load_dwordx4 v8, s[36:39], s55 offen offset:2048 nt lds\n"
    "v_mfma_f32_16x16x32_bf16 v[104:107], a[200:203], v[48:51], v[104:107]\n"
    "s_add_u32 m0, s54, 98304\n"
    "v_exp_f32 v32, v32\n"
    "v_mfma_f32_16x16x32_bf16 v[100:103], a[200:203], v[44:47], v[100:103]\n"
    "s_waitcnt lgkmcnt(7)\n"
    "ds_read_b64_tr_b16 a[200:201], v2 offset:12544\n"
    "v_fma_f32 v42, v42, s52, v15\n"
    "v_mfma_f32_16x16x32_bf16 v[116:119], a[204:207], v[44:47], v[116:119]\n"
    "ds_read_b64_tr_b16 a[202:203], v2 offset:18688\n"
    "v_fma_f32 v43, v43, s52, v15\n"
    "v_mfma_f32_16x16x32_bf16 v[120:123], a[204:207], v[48:51], v[120:123]\n"
    "v_exp_f32 v33, v33\n"
    "v_mfma_f32_16x16x32_bf16 v[124:127], a[204:207], v[52:55], v[124:127]\n"
    "v_exp_f32 v34, v34\n"
    "v_mfma_f32_16x16x32_bf16 v[128:131], a[204:207], v[56:59], v[128:131]\n"
    "s_waitcnt lgkmcnt(8)\n"
    "ds_read_b64_tr_b16 a[204:205], v3 offset:12544\n"
    "v_cvt_pk_bf16_f32 v32, v32, v33\n"
    "v_mfma_f32_16x16x32_bf16 v[144:147], a[208:211], v[56:59], v[144:147]\n"
    "ds_read_b64_tr_b16 a[206:207], v3 offset:18688\n"
    "v_fma_f32 v76, v76, s52, v14\n"
    "v_mfma_f32_16x16x32_bf16 v[140:143], a[208:211], v[52:55], v[140:143]\n"
    "v_exp_f32 v35, v35\n"
    "v_mfma_f32_16x16x32_bf16 v[136:139], a[208:211], v[48:51], v[136:139]\n"
    "v_exp_f32 v72, v72\n"
    "v_mfma_f32_16x16x32_bf16 v[132:135], a[208:211], v[44:47], v[132:135]\n"
    "s_waitcnt lgkmcnt(8)\n"
    "ds_read_b64_tr_b16 a[208:209], v4 offset:12544\n"
    "v_cvt_pk_bf16_f32 v33, v34, v35\n"
    "v_mfma_f32_16x16x32_bf16 a[232:235], a[212:215], v[44:47], a[232:235]\n"
    "ds_read_b64_tr_b16 a[210:211], v4 offset:18688\n"
    "v_fma_f32 v77, v77, s52, v14\n"
    "v_mfma_f32_16x16x32_bf16 v[148:151], a[212:215], v[48:51], v[148:151]\n"
    "v_exp_f32 v73, v73\n"
    "v_mfma_f32_16x16x32_bf16 v[152:155], a[212:215], v[52:55], v[152:155]\n"
    "v_exp_f32 v74, v74\n"
    "v_mfma_f32_16x16x32_bf16 v[156:159], a[212:215], v[56:59], v[156:159]\n"
    "s_waitcnt lgkmcnt(8)\n"
    "ds_read_b64_tr_b16 a[212:213], v5 offset:12544\n"
    "v_cvt_pk_bf16_f32 v34, v72, v73\n"
    "v_mfma_f32_16x16x32_bf16 v[172:175], a[192:195], v[56:59], v[172:175]\n"
    "buffer_load_dwordx4 v6, s[40:43], s56 offen offset:0 nt lds\n"
    "v_mfma_f32_16x16x32_bf16 v[168:171], a[192:195], v[52:55], v[168:171]\n"
    "ds_read_b64_tr_b16 a[214:215], v5 offset:18688\n"
    "v_fma_f32 v78, v78, s52, v14\n"
    "v_mfma_f32_16x16x32_bf16 v[164:167], a[192:195], v[48:51], v[164:167]\n"
    "v_exp_f32 v75, v75\n"
    "v_mfma_f32_16x16x32_bf16 v[160:163], a[192:195], v[44:47], v[160:163]\n"
    "s_waitcnt lgkmcnt(8)\n"
    "v_cvt_pk_bf16_f32 v35, v74, v75\n"
    "v_fma_f32 v79, v79, s52, v14\n"
    "v_mfma_f32_16x16x32_bf16 v[176:179], a[196:199], v[44:47], v[176:179]\n"
    "v_exp_f32 v36, v36\n"
    "v_mfma_f32_16x16x32_bf16 v[180:183], a[196:199], v[48:51], v[180:183]\n"
    "v_exp_f32 v37, v37\n"
    "v_mfma_f32_16x16x32_bf16 v[184:187], a[196:199], v[52:55], v[184:187]\n"
    "v_exp_f32 v40, v40\n"
    "v_mfma_f32_16x16x32_bf16 v[188:191], a[196:199], v[56:59], v[188:191]\n"
    "s_waitcnt vmcnt(4) lgkmcnt(0)\n"
    "v_exp_f32 v41, v41\n"
    "v_mfma_f32_16x16x32_bf16 v[204:207], a[200:203], v[56:59], v[204:207]\n"
    "s_barrier\n"
    "ds_read_b128 a[192:195], v0 offset:36864\n"
    "v_fma_f32 v80, v80, s52, v15\n"
    "v_mfma_f32_16x16x32_bf16 v[200:203], a[200:203], v[52:55], v[200:203]\n"
    "ds_read_b128 a[196:199], v1 offset:36864\n"
    "v_fma_f32 v81, v81, s52, v15\n"
    "v_mfma_f32_16x16x32_bf16 v[196:199], a[200:203], v[48:51], v[196:199]\n"
    "v_exp_f32 v38, v38\n"
    "v_mfma_f32_16x16x32_bf16 v[192:195], a[200:203], v[44:47], v[192:195]\n"
    "ds_read_b128 a[200:203], v0 offset:36992\n"
    "v_fma_f32 v82, v82, s52, v15\n"
    "v_mfma_f32_16x16x32_bf16 v[208:211], a[204:207], v[44:47], v[208:211]\n"
    "v_exp_f32 v39, v39\n"
    "v_mfma_f32_16x16x32_bf16 v[212:215], a[204:207], v[48:51], v[212:215]\n"
    "v_exp_f32 v42, v42\n"
    "v_mfma_f32_16x16x32_bf16 v[216:219], a[204:207], v[52:55], v[216:219]\n"
    "buffer_load_dwordx4 v7, s[40:43], s56 offen offset:1024 nt lds\n"
    "v_mfma_f32_16x16x32_bf16 v[220:223], a[204:207], v[56:59], v[220:223]\n"
    "ds_read_b128 a[204:207], v1 offset:36992\n"
    "v_fma_f32 v83, v83, s52, v15\n"
    "v_mfma_f32_16x16x32_bf16 v[236:239], a[208:211], v[56:59], v[236:239]\n"
    "v_exp_f32 v43, v43\n"
    "v_mfma_f32_16x16x32_bf16 v[232:235], a[208:211], v[52:55], v[232:235]\n"
    "v_cvt_pk_bf16_f32 v36, v36, v37\n"
    "v_cvt_pk_bf16_f32 v40, v40, v41\n"
    "v_mfma_f32_16x16x32_bf16 v[228:231], a[208:211], v[48:51], v[228:231]\n"
    "v_exp_f32 v76, v76\n"
    "v_mfma_f32_16x16x32_bf16 v[224:227], a[208:211], v[44:47], v[224:227]\n"
    "ds_read_b128 a[208:211], v0 offset:37120\n"
    "v_cvt_pk_bf16_f32 v37, v38, v39\n"
    "v_mfma_f32_16x16x32_bf16 v[240:243], a[212:215], v[44:47], v[240:243]\n"
    "v_exp_f32 v77, v77\n"
    "v_mfma_f32_16x16x32_bf16 v[244:247], a[212:215], v[48:51], v[244:247]\n"
    "v_exp_f32 v78, v78\n"
    "v_mfma_f32_16x16x32_bf16 v[248:251], a[212:215], v[52:55], v[248:251]\n"
    "v_exp_f32 v79, v79\n"
    "v_mfma_f32_16x16x32_bf16 v[252:255], a[212:215], v[56:59], v[252:255]\n"
    "s_waitcnt lgkmcnt(4)\n"
    "ds_read_b128 a[212:215], v1 offset:37120\n"
    "v_cvt_pk_bf16_f32 v41, v42, v43\n"
    "v_mfma_f32_16x16x32_bf16 v[44:47], a[192:195], a[0:3], 0\n"
    "s_waitcnt lgkmcnt(4)\n"
    "v_exp_f32 v80, v80\n"
    "v_mfma_f32_16x16x32_bf16 v[44:47], a[196:199], a[4:7], v[44:47]\n"
    "s_waitcnt lgkmcnt(3)\n"
    "v_exp_f32 v81, v81\n"
    "v_mfma_f32_16x16x32_bf16 v[44:47], a[200:203], a[8:11], v[44:47]\n"
    "v_exp_f32 v82, v82\n"
    "v_mfma_f32_16x16x32_bf16 v[48:51], a[200:203], a[32:35], 0\n"
    "v_exp_f32 v83, v83\n"
    "v_mfma_f32_16x16x32_bf16 v[48:51], a[196:199], a[28:31], v[48:51]\n"
    "buffer_load_dwordx4 v8, s[40:43], s56 offen offset:2048 nt lds\n"
    "v_mfma_f32_16x16x32_bf16 v[48:51], a[192:195], a[24:27], v[48:51]\n"
    "s_add_u32 s56, s56, 24576\n"
    "v_cvt_pk_bf16_f32 v38, v76, v77\n"
    "v_cvt_pk_bf16_f32 v39, v78, v79\n"
    "v_mfma_f32_16x16x32_bf16 v[52:55], a[192:195], a[48:51], 0\n"
    "s_add_u32 m0, s54, 0\n"
    "v_cvt_pk_bf16_f32 v42, v80, v81\n"
    "v_cvt_pk_bf16_f32 v43, v82, v83\n"
    "v_mfma_f32_16x16x32_bf16 v[52:55], a[196:199], a[52:55], v[52:55]\n"
    "v_mfma_f32_16x16x32_bf16 v[52:55], a[200:203], a[56:59], v[52:55]\n"
    "v_mfma_f32_16x16x32_bf16 v[56:59], a[200:203], a[80:83], 0\n"
    "ds_read_b128 a[200:203], v0 offset:43136\n"
    "v_mfma_f32_16x16x32_bf16 v[56:59], a[196:199], a[76:79], v[56:59]\n"
    "ds_read_b128 a[196:199], v1 offset:43008\n"
    "v_mfma_f32_16x16x32_bf16 v[56:59], a[192:195], a[72:75], v[56:59]\n"
    "s_waitcnt lgkmcnt(4)\n"
    "ds_read_b128 a[192:195], v0 offset:43008\n"
    "v_mfma_f32_16x16x32_bf16 v[44:47], a[204:207], a[12:15], v[44:47]\n"
    "s_waitcnt lgkmcnt(4)\n"
    "v_mfma_f32_16x16x32_bf16 v[44:47], a[208:211], a[16:19], v[44:47]\n"
    "s_waitcnt lgkmcnt(3)\n"
    "v_mfma_f32_16x16x32_bf16 v[44:47], a[212:215], a[20:23], v[44:47]\n"
    "v_mfma_f32_16x16x32_bf16 v[48:51], a[212:215], a[44:47], v[48:51]\n"
    "v_mfma_f32_16x16x32_bf16 v[48:51], a[208:211], a[40:43], v[48:51]\n"
    "v_mfma_f32_16x16x32_bf16 v[48:51], a[204:207], a[36:39], v[48:51]\n"
    "v_mfma_f32_16x16x32_bf16 v[52:55], a[204:207], a[60:63], v[52:55]\n"
    "v_mfma_f32_16x16x32_bf16 v[52:55], a[208:211], a[64:67], v[52:55]\n"
    "buffer_load_dwordx4 v6, s[36:39], s56 offen offset:0 nt lds\n"
    "v_mfma_f32_16x16x32_bf16 v[52:55], a[212:215], a[68:71], v[52:55]\n"
    "v_mfma_f32_16x16x32_bf16 v[56:59], a[212:215], a[92:95], v[56:59]\n"
    "ds_read_b128 a[212:215], v1 offset:43264\n"
    "v_fma_f32 v44, v44, s52, v12\n"
    "v_mfma_f32_16x16x32_bf16 v[56:59], a[208:211], a[88:91], v[56:59]\n"
    "ds_read_b128 a[208:211], v0 offset:43264\n"
    "v_fma_f32 v45, v45, s52, v12\n"
    "v_mfma_f32_16x16x32_bf16 v[56:59], a[204:207], a[84:87], v[56:59]\n"
    "s_waitcnt lgkmcnt(2)\n"
    "ds_read_b128 a[204:207], v1 offset:43136\n"
    "v_fma_f32 v46, v46, s52, v12\n"
    "v_mfma_f32_16x16x32_bf16 v[60:63], a[192:195], a[0:3], 0\n"
    "v_exp_f32 v44, v44\n"
    "v_mfma_f32_16x16x32_bf16 v[60:63], a[196:199], a[4:7], v[60:63]\n"
    "v_exp_f32 v45, v45\n"
    "v_mfma_f32_16x16x32_bf16 v[60:63], a[200:203], a[8:11], v[60:63]\n"
    "v_fma_f32 v47, v47, s52, v12\n"
    "v_cvt_pk_bf16_f32 v44, v44, v45\n"
    "v_mfma_f32_16x16x32_bf16 v[64:67], a[200:203], a[32:35], 0\n"
    "v_exp_f32 v46, v46\n"
    "v_mfma_f32_16x16x32_bf16 v[64:67], a[196:199], a[28:31], v[64:67]\n"
    "v_exp_f32 v47, v47\n"
    "v_mfma_f32_16x16x32_bf16 v[64:67], a[192:195], a[24:27], v[64:67]\n"
    "v_cvt_pk_bf16_f32 v45, v46, v47\n"
    "v_fma_f32 v48, v48, s52, v13\n"
    "v_mfma_f32_16x16x32_bf16 v[68:71], a[192:195], a[48:51], 0\n"
    "v_fma_f32 v49, v49, s52, v13\n"
    "v_fma_f32 v50, v50, s52, v13\n"
    "v_mfma_f32_16x16x32_bf16 v[68:71], a[196:199], a[52:55], v[68:71]\n"
    "v_exp_f32 v48, v48\n"
    "v_mfma_f32_16x16x32_bf16 v[68:71], a[200:203], a[56:59], v[68:71]\n"
    "v_exp_f32 v49, v49\n"
    "v_mfma_f32_16x16x32_bf16 v[72:75], a[200:203], a[80:83], 0\n"
    "buffer_load_dwordx4 v7, s[36:39], s56 offen offset:1024 nt lds\n"
    "v_mfma_f32_16x16x32_bf16 v[72:75], a[196:199], a[76:79], v[72:75]\n"
    "ds_read_b64_tr_b16 a[196:197], v3 offset:24576\n"
    "v_fma_f32 v51, v51, s52, v13\n"
    "v_mfma_f32_16x16x32_bf16 v[72:75], a[192:195], a[72:75], v[72:75]\n"
    "s_waitcnt lgkmcnt(1)\n"
    "ds_read_b64_tr_b16 a[192:193], v2 offset:24576\n"
    "v_cvt_pk_bf16_f32 v48, v48, v49\n"
    "v_mfma_f32_16x16x32_bf16 v[60:63], a[204:207], a[12:15], v[60:63]\n"
    "ds_read_b64_tr_b16 a[194:195], v2 offset:30720\n"
    "v_fma_f32 v52, v52, s52, v14\n"
    "v_mfma_f32_16x16x32_bf16 v[60:63], a[208:211], a[16:19], v[60:63]\n"
    "ds_read_b64_tr_b16 a[198:199], v3 offset:30720\n"
    "v_fma_f32 v53, v53, s52, v14\n"
    "v_mfma_f32_16x16x32_bf16 v[60:63], a[212:215], a[20:23], v[60:63]\n"
    "ds_read_b64_tr_b16 a[200:201], v4 offset:24576\n"
    "v_fma_f32 v56, v56, s52, v15\n"
    "v_mfma_f32_16x16x32_bf16 v[64:67], a[212:215], a[44:47], v[64:67]\n"
    "ds_read_b64_tr_b16 a[202:203], v4 offset:30720\n"
    "v_fma_f32 v57, v57, s52, v15\n"
    "v_mfma_f32_16x16x32_bf16 v[64:67], a[208:211], a[40:43], v[64:67]\n"
    "v_exp_f32 v50, v50\n"
    "v_mfma_f32_16x16x32_bf16 v[64:67], a[204:207], a[36:39], v[64:67]\n"
    "v_fma_f32 v60, v60, s52, v12\n"
    "v_fma_f32 v61, v61, s52, v12\n"
    "v_mfma_f32_16x16x32_bf16 v[68:71], a[204:207], a[60:63], v[68:71]\n"
    "v_fma_f32 v62, v62, s52, v12\n"
    "v_fma_f32 v63, v63, s52, v12\n"
    "v_mfma_f32_16x16x32_bf16 v[68:71], a[208:211], a[64:67], v[68:71]\n"
    "v_exp_f32 v60, v60\n"
    "v_mfma_f32_16x16x32_bf16 v[68:71], a[212:215], a[68:71], v[68:71]\n"
    "v_exp_f32 v61, v61\n"
    "v_mfma_f32_16x16x32_bf16 v[72:75], a[212:215], a[92:95], v[72:75]\n"
    "ds_read_b64_tr_b16 a[212:213], v3 offset:24704\n"
    "v_cvt_pk_bf16_f32 v46, v60, v61\n"
    "v_mfma_f32_16x16x32_bf16 v[72:75], a[208:211], a[88:91], v[72:75]\n"
    "ds_read_b64_tr_b16 a[208:209], v2 offset:24704\n"
    "v_fma_f32 v64, v64, s52, v13\n"
    "v_mfma_f32_16x16x32_bf16 v[72:75], a[204:207], a[84:87], v[72:75]\n"
    "buffer_load_dwordx4 v8, s[36:39], s56 offen offset:2048 nt lds\n"
    "v_smfmac_f32_16x16x64_bf16 a[236:239], a[252:255], v[28:35], v23\n"
    "s_add_u32 m0, s54, 110592\n"
    "ds_read_b64_tr_b16 a[204:205], v5 offset:24576\n"
    "v_fma_f32 v65, v65, s52, v13\n"
    "v_smfmac_f32_16x16x64_bf16 a[240:243], a[252:255], v[36:43], v23\n"
    "s_waitcnt lgkmcnt(6)\n"
    "ds_read_b64_tr_b16 a[206:207], v5 offset:30720\n"
    "v_fma_f32 v66, v66, s52, v13\n"
    "v_mfma_f32_16x16x32_bf16 v[96:99], a[192:195], v[40:43], v[96:99]\n"
    "ds_read_b64_tr_b16 a[210:211], v2 offset:30848\n"
    "v_fma_f32 v67, v67, s52, v13\n"
    "v_mfma_f32_16x16x32_bf16 v[92:95], a[192:195], v[36:39], v[92:95]\n"
    "ds_read_b64_tr_b16 a[214:215], v3 offset:30848\n"
    "v_fma_f32 v54, v54, s52, v14\n"
    "v_mfma_f32_16x16x32_bf16 v[88:91], a[192:195], v[32:35], v[88:91]\n"
    "v_exp_f32 v62, v62\n"
    "v_mfma_f32_16x16x32_bf16 v[84:87], a[192:195], v[28:31], v[84:87]\n"
    "s_waitcnt lgkmcnt(8)\n"
    "ds_read_b64_tr_b16 a[192:193], v4 offset:24704\n"
    "v_fma_f32 v55, v55, s52, v14\n"
    "v_mfma_f32_16x16x32_bf16 a[216:219], a[196:199], v[28:31], a[216:219]\n"
    "ds_read_b64_tr_b16 a[194:195], v4 offset:30848\n"
    "v_fma_f32 v58, v58, s52, v15\n"
    "v_mfma_f32_16x16x32_bf16 a[220:223], a[196:199], v[32:35], a[220:223]\n"
    "v_exp_f32 v63, v63\n"
    "v_mfma_f32_16x16x32_bf16 a[224:227], a[196:199], v[36:39], a[224:227]\n"
    "v_cvt_pk_bf16_f32 v47, v62, v63\n"
    "v_fma_f32 v59, v59, s52, v15\n"
    "v_mfma_f32_16x16x32_bf16 a[228:231], a[196:199], v[40:43], a[228:231]\n"
    "s_waitcnt lgkmcnt(8)\n"
    "ds_read_b64_tr_b16 a[196:197], v5 offset:24704\n"
    "v_fma_f32 v68, v68, s52, v14\n"
    "v_mfma_f32_16x16x32_bf16 v[112:115], a[200:203], v[40:43], v[112:115]\n"
    "ds_read_b64_tr_b16 a[198:199], v5 offset:30848\n"
    "v_fma_f32 v69, v69, s52, v14\n"
    "v_mfma_f32_16x16x32_bf16 v[108:111], a[200:203], v[36:39], v[108:111]\n"
    "v_exp_f32 v51, v51\n"
    "v_mfma_f32_16x16x32_bf16 v[104:107], a[200:203], v[32:35], v[104:107]\n"
    "v_exp_f32 v64, v64\n"
    "v_mfma_f32_16x16x32_bf16 v[100:103], a[200:203], v[28:31], v[100:103]\n"
    "s_waitcnt lgkmcnt(6)\n"
    "ds_read_b64_tr_b16 a[200:201], v2 offset:24832\n"
    "v_cvt_pk_bf16_f32 v49, v50, v51\n"
    "v_mfma_f32_16x16x32_bf16 v[116:119], a[204:207], v[28:31], v[116:119]\n"
    "buffer_load_dwordx4 v6, s[40:43], s55 offen offset:0 nt lds\n"
    "v_mfma_f32_16x16x32_bf16 v[120:123], a[204:207], v[32:35], v[120:123]\n"
    "ds_read_b64_tr_b16 a[202:203], v2 offset:30976\n"
    "v_fma_f32 v70, v70, s52, v14\n"
    "v_mfma_f32_16x16x32_bf16 v[124:127], a[204:207], v[36:39], v[124:127]\n"
    "v_exp_f32 v65, v65\n"
    "v_mfma_f32_16x16x32_bf16 v[128:131], a[204:207], v[40:43], v[128:131]\n"
    "s_waitcnt lgkmcnt(7)\n"
    "ds_read_b64_tr_b16 a[204:205], v3 offset:24832\n"
    "v_cvt_pk_bf16_f32 v50, v64, v65\n"
    "v_mfma_f32_16x16x32_bf16 v[144:147], a[208:211], v[40:43], v[144:147]\n"
    "ds_read_b64_tr_b16 a[206:207], v3 offset:30976\n"
    "v_fma_f32 v71, v71, s52, v14\n"
    "v_mfma_f32_16x16x32_bf16 v[140:143], a[208:211], v[36:39], v[140:143]\n"
    "v_exp_f32 v66, v66\n"
    "v_mfma_f32_16x16x32_bf16 v[136:139], a[208:211], v[32:35], v[136:139]\n"
    "v_exp_f32 v67, v67\n"
    "v_mfma_f32_16x16x32_bf16 v[132:135], a[208:211], v[28:31], v[132:135]\n"
    "s_waitcnt lgkmcnt(8)\n"
    "ds_read_b64_tr_b16 a[208:209], v4 offset:24832\n"
    "v_cvt_pk_bf16_f32 v51, v66, v67\n"
    "v_mfma_f32_16x16x32_bf16 a[232:235], a[212:215], v[28:31], a[232:235]\n"
    "ds_read_b64_tr_b16 a[210:211], v4 offset:30976\n"
    "v_fma_f32 v72, v72, s52, v15\n"
    "v_mfma_f32_16x16x32_bf16 v[148:151], a[212:215], v[32:35], v[148:151]\n"
    "v_exp_f32 v52, v52\n"
    "v_mfma_f32_16x16x32_bf16 v[152:155], a[212:215], v[36:39], v[152:155]\n"
    "v_exp_f32 v53, v53\n"
    "v_mfma_f32_16x16x32_bf16 v[156:159], a[212:215], v[40:43], v[156:159]\n"
    "s_waitcnt lgkmcnt(8)\n"
    "ds_read_b64_tr_b16 a[212:213], v5 offset:24832\n"
    "v_fma_f32 v73, v73, s52, v15\n"
    "v_mfma_f32_16x16x32_bf16 v[172:175], a[192:195], v[40:43], v[172:175]\n"
    "ds_read_b64_tr_b16 a[214:215], v5 offset:30976\n"
    "v_fma_f32 v74, v74, s52, v15\n"
    "v_mfma_f32_16x16x32_bf16 v[168:171], a[192:195], v[36:39], v[168:171]\n"
    "v_exp_f32 v56, v56\n"
    "v_mfma_f32_16x16x32_bf16 v[164:167], a[192:195], v[32:35], v[164:167]\n"
    "buffer_load_dwordx4 v7, s[40:43], s55 offen offset:1024 nt lds\n"
    "v_mfma_f32_16x16x32_bf16 v[160:163], a[192:195], v[28:31], v[160:163]\n"
    "s_waitcnt lgkmcnt(8)\n"
    "v_exp_f32 v57, v57\n"
    "v_mfma_f32_16x16x32_bf16 v[176:179], a[196:199], v[28:31], v[176:179]\n"
    "v_exp_f32 v54, v54\n"
    "v_mfma_f32_16x16x32_bf16 v[180:183], a[196:199], v[32:35], v[180:183]\n"
    "v_exp_f32 v55, v55\n"
    "v_mfma_f32_16x16x32_bf16 v[184:187], a[196:199], v[36:39], v[184:187]\n"
    "s_waitcnt vmcnt(5) lgkmcnt(0)\n"
    "v_exp_f32 v58, v58\n"
    "v_mfma_f32_16x16x32_bf16 v[188:191], a[196:199], v[40:43], v[188:191]\n"
    "s_barrier\n"
    "ds_read_b128 a[192:195], v0 offset:49152\n"
    "v_fma_f32 v75, v75, s52, v15\n"
    "v_mfma_f32_16x16x32_bf16 v[204:207], a[200:203], v[40:43], v[204:207]\n"
    "ds_read_b128 a[196:199], v1 offset:49152\n"
    "v_cvt_pk_bf16_f32 v52, v52, v53\n"
    "v_mfma_f32_16x16x32_bf16 v[200:203], a[200:203], v[36:39], v[200:203]\n"
    "v_exp_f32 v59, v59\n"
    "v_mfma_f32_16x16x32_bf16 v[196:199], a[200:203], v[32:35], v[196:199]\n"
    "v_cvt_pk_bf16_f32 v56, v56, v57\n"
    "v_cvt_pk_bf16_f32 v53, v54, v55\n"
    "v_mfma_f32_16x16x32_bf16 v[192:195], a[200:203], v[28:31], v[192:195]\n"
    "ds_read_b128 a[200:203], v0 offset:49280\n"
    "v_cvt_pk_bf16_f32 v57, v58, v59\n"
    "v_mfma_f32_16x16x32_bf16 v[208:211], a[204:207], v[28:31], v[208:211]\n"
    "v_exp_f32 v68, v68\n"
    "v_mfma_f32_16x16x32_bf16 v[212:215], a[204:207], v[32:35], v[212:215]\n"
    "v_exp_f32 v69, v69\n"
    "v_mfma_f32_16x16x32_bf16 v[216:219], a[204:207], v[36:39], v[216:219]\n"
    "v_exp_f32 v70, v70\n"
    "v_mfma_f32_16x16x32_bf16 v[220:223], a[204:207], v[40:43], v[220:223]\n"
    "ds_read_b128 a[204:207], v1 offset:49280\n"
    "v_cvt_pk_bf16_f32 v54, v68, v69\n"
    "v_mfma_f32_16x16x32_bf16 v[236:239], a[208:211], v[40:43], v[236:239]\n"
    "buffer_load_dwordx4 v8, s[40:43], s55 offen offset:2048 nt lds\n"
    "v_mfma_f32_16x16x32_bf16 v[232:235], a[208:211], v[36:39], v[232:235]\n"
    "s_add_u32 s55, s55, 24576\n"
    "v_exp_f32 v71, v71\n"
    "v_mfma_f32_16x16x32_bf16 v[228:231], a[208:211], v[32:35], v[228:231]\n"
    "s_add_u32 m0, s54, 12288\n"
    "v_exp_f32 v72, v72\n"
    "v_mfma_f32_16x16x32_bf16 v[224:227], a[208:211], v[28:31], v[224:227]\n"
    "ds_read_b128 a[208:211], v0 offset:49408\n"
    "v_cvt_pk_bf16_f32 v55, v70, v71\n"
    "v_mfma_f32_16x16x32_bf16 v[240:243], a[212:215], v[28:31], v[240:243]\n"
    "v_exp_f32 v73, v73\n"
    "v_mfma_f32_16x16x32_bf16 v[244:247], a[212:215], v[32:35], v[244:247]\n"
    "v_exp_f32 v74, v74\n"
    "v_mfma_f32_16x16x32_bf16 v[248:251], a[212:215], v[36:39], v[248:251]\n"
    "v_exp_f32 v75, v75\n"
    "v_mfma_f32_16x16x32_bf16 v[252:255], a[212:215], v[40:43], v[252:255]\n"
    "s_waitcnt lgkmcnt(4)\n"
    "ds_read_b128 a[212:215], v1 offset:49408\n"
    "v_cvt_pk_bf16_f32 v58, v72, v73\n"
    "v_mfma_f32_16x16x32_bf16 v[28:31], a[192:195], a[0:3], 0\n"
    "s_waitcnt lgkmcnt(4)\n"
    "v_cvt_pk_bf16_f32 v59, v74, v75\n"
    "v_mfma_f32_16x16x32_bf16 v[28:31], a[196:199], a[4:7], v[28:31]\n"
    "s_waitcnt lgkmcnt(3)\n"
    "v_mfma_f32_16x16x32_bf16 v[28:31], a[200:203], a[8:11], v[28:31]\n"
    "v_mfma_f32_16x16x32_bf16 v[32:35], a[200:203], a[32:35], 0\n"
    "v_mfma_f32_16x16x32_bf16 v[32:35], a[196:199], a[28:31], v[32:35]\n"
    "v_mfma_f32_16x16x32_bf16 v[32:35], a[192:195], a[24:27], v[32:35]\n"
    "v_mfma_f32_16x16x32_bf16 v[36:39], a[192:195], a[48:51], 0\n"
    "v_mfma_f32_16x16x32_bf16 v[36:39], a[196:199], a[52:55], v[36:39]\n"
    "buffer_load_dwordx4 v6, s[36:39], s55 offen offset:0 nt lds\n"
    "v_mfma_f32_16x16x32_bf16 v[36:39], a[200:203], a[56:59], v[36:39]\n"
    "v_mfma_f32_16x16x32_bf16 v[40:43], a[200:203], a[80:83], 0\n"
    "ds_read_b128 a[200:203], v0 offset:55424\n"
    "v_mfma_f32_16x16x32_bf16 v[40:43], a[196:199], a[76:79], v[40:43]\n"
    "ds_read_b128 a[196:199], v1 offset:55296\n"
    "v_mfma_f32_16x16x32_bf16 v[40:43], a[192:195], a[72:75], v[40:43]\n"
    "s_waitcnt lgkmcnt(4)\n"
    "ds_read_b128 a[192:195], v0 offset:55296\n"
    "v_mfma_f32_16x16x32_bf16 v[28:31], a[204:207], a[12:15], v[28:31]\n"
    "s_waitcnt lgkmcnt(4)\n"
    "v_mfma_f32_16x16x32_bf16 v[28:31], a[208:211], a[16:19], v[28:31]\n"
    "s_waitcnt lgkmcnt(3)\n"
    "v_mfma_f32_16x16x32_bf16 v[28:31], a[212:215], a[20:23], v[28:31]\n"
    "v_mfma_f32_16x16x32_bf16 v[32:35], a[212:215], a[44:47], v[32:35]\n"
    "v_mfma_f32_16x16x32_bf16 v[32:35], a[208:211], a[40:43], v[32:35]\n"
    "v_mfma_f32_16x16x32_bf16 v[32:35], a[204:207], a[36:39], v[32:35]\n"
    "v_mfma_f32_16x16x32_bf16 v[36:39], a[204:207], a[60:63], v[36:39]\n"
    "v_mfma_f32_16x16x32_bf16 v[36:39], a[208:211], a[64:67], v[36:39]\n"
    "v_mfma_f32_16x16x32_bf16 v[36:39], a[212:215], a[68:71], v[36:39]\n"
    "v_mfma_f32_16x16x32_bf16 v[40:43], a[212:215], a[92:95], v[40:43]\n"
    "buffer_load_dwordx4 v7, s[36:39], s55 offen offset:1024 nt lds\n"
    "v_mfma_f32_16x16x32_bf16 v[40:43], a[208:211], a[88:91], v[40:43]\n"
    "ds_read_b128 a[208:211], v0 offset:55552\n"
    "v_fma_f32 v28, v28, s52, v12\n"
    "v_mfma_f32_16x16x32_bf16 v[40:43], a[204:207], a[84:87], v[40:43]\n"
    "s_waitcnt lgkmcnt(1)\n"
    "ds_read_b128 a[204:207], v1 offset:55424\n"
    "v_fma_f32 v29, v29, s52, v12\n"
    "v_mfma_f32_16x16x32_bf16 v[76:79], a[192:195], a[0:3], 0\n"
    "ds_read_b128 a[212:215], v1 offset:55552\n"
    "v_fma_f32 v30, v30, s52, v12\n"
    "v_mfma_f32_16x16x32_bf16 v[76:79], a[196:199], a[4:7], v[76:79]\n"
    "v_exp_f32 v28, v28\n"
    "v_mfma_f32_16x16x32_bf16 v[76:79], a[200:203], a[8:11], v[76:79]\n"
    "v_exp_f32 v29, v29\n"
    "v_mfma_f32_16x16x32_bf16 v[80:83], a[200:203], a[32:35], 0\n"
    "v_fma_f32 v31, v31, s52, v12\n"
    "v_cvt_pk_bf16_f32 v28, v28, v29\n"
    "v_mfma_f32_16x16x32_bf16 v[80:83], a[196:199], a[28:31], v[80:83]\n"
    "v_exp_f32 v30, v30\n"
    "v_mfma_f32_16x16x32_bf16 v[80:83], a[192:195], a[24:27], v[80:83]\n"
    "v_exp_f32 v31, v31\n"
    "v_mfma_f32_16x16x32_bf16 v[60:63], a[192:195], a[48:51], 0\n"
    "v_cvt_pk_bf16_f32 v29, v30, v31\n"
    "v_fma_f32 v32, v32, s52, v13\n"
    "v_mfma_f32_16x16x32_bf16 v[60:63], a[196:199], a[52:55], v[60:63]\n"
    "v_fma_f32 v33, v33, s52, v13\n"
    "v_fma_f32 v34, v34, s52, v13\n"
    "v_mfma_f32_16x16x32_bf16 v[60:63], a[200:203], a[56:59], v[60:63]\n"
    "v_exp_f32 v32, v32\n"
    "v_mfma_f32_16x16x32_bf16 v[64:67], a[200:203], a[80:83], 0\n"
    "ds_read_b64_tr_b16 a[200:201], v4 offset:36864\n"
    "v_fma_f32 v35, v35, s52, v13\n"
    "v_mfma_f32_16x16x32_bf16 v[64:67], a[196:199], a[76:79], v[64:67]\n"
    "ds_read_b64_tr_b16 a[196:197], v3 offset:36864\n"
    "v_fma_f32 v36, v36, s52, v14\n"
    "v_mfma_f32_16x16x32_bf16 v[64:67], a[192:195], a[72:75], v[64:67]\n"
    "s_waitcnt lgkmcnt(3)\n"
    "buffer_load_dwordx4 v8, s[36:39], s55 offen offset:2048 nt lds\n"
    "v_mfma_f32_16x16x32_bf16 v[76:79], a[204:207], a[12:15], v[76:79]\n"
    "s_add_u32 m0, s54, 61440\n"
    "ds_read_b64_tr_b16 a[192:193], v2 offset:36864\n"
    "v_fma_f32 v37, v37, s52, v14\n"
    "v_mfma_f32_16x16x32_bf16 v[76:79], a[208:211], a[16:19], v[76:79]\n"
    "s_waitcnt lgkmcnt(3)\n"
    "ds_read_b64_tr_b16 a[194:195], v2 offset:43008\n"
    "v_fma_f32 v40, v40, s52, v15\n"
    "v_mfma_f32_16x16x32_bf16 v[76:79], a[212:215], a[20:23], v[76:79]\n"
    "ds_read_b64_tr_b16 a[198:199], v3 offset:43008\n"
    "v_fma_f32 v41, v41, s52, v15\n"
    "v_mfma_f32_16x16x32_bf16 v[80:83], a[212:215], a[44:47], v[80:83]\n"
    "ds_read_b64_tr_b16 a[202:203], v4 offset:43008\n"
    "v_fma_f32 v38, v38, s52, v14\n"
    "v_mfma_f32_16x16x32_bf16 v[80:83], a[208:211], a[40:43], v[80:83]\n"
    "v_exp_f32 v33, v33\n"
    "v_mfma_f32_16x16x32_bf16 v[80:83], a[204:207], a[36:39], v[80:83]\n"
    "v_fma_f32 v76, v76, s52, v12\n"
    "v_fma_f32 v77, v77, s52, v12\n"
    "v_mfma_f32_16x16x32_bf16 v[60:63], a[204:207], a[60:63], v[60:63]\n"
    "v_fma_f32 v78, v78, s52, v12\n"
    "v_fma_f32 v79, v79, s52, v12\n"
    "v_mfma_f32_16x16x32_bf16 v[60:63], a[208:211], a[64:67], v[60:63]\n"
    "v_exp_f32 v76, v76\n"
    "v_mfma_f32_16x16x32_bf16 v[60:63], a[212:215], a[68:71], v[60:63]\n"
    "v_exp_f32 v77, v77\n"
    "v_mfma_f32_16x16x32_bf16 v[64:67], a[212:215], a[92:95], v[64:67]\n"
    "ds_read_b64_tr_b16 a[212:213], v3 offset:36992\n"
    "v_cvt_pk_bf16_f32 v30, v76, v77\n"
    "v_mfma_f32_16x16x32_bf16 v[64:67], a[208:211], a[88:91], v[64:67]\n"
    "ds_read_b64_tr_b16 a[208:209], v2 offset:36992\n"
    "v_fma_f32 v80, v80, s52, v13\n"
    "v_mfma_f32_16x16x32_bf16 v[64:67], a[204:207], a[84:87], v[64:67]\n"
    "ds_read_b64_tr_b16 a[204:205], v5 offset:36864\n"
    "v_fma_f32 v81, v81, s52, v13\n"
    "v_smfmac_f32_16x16x64_bf16 a[236:239], a[252:255], v[44:51], v23\n"
    "ds_read_b64_tr_b16 a[206:207], v5 offset:43008\n"
    "v_fma_f32 v82, v82, s52, v13\n"
    "v_smfmac_f32_16x16x64_bf16 a[240:243], a[252:255], v[52:59], v23\n"
    "s_waitcnt lgkmcnt(6)\n"
    "ds_read_b64_tr_b16 a[210:211], v2 offset:43136\n"
    "v_fma_f32 v83, v83, s52, v13\n"
    "v_mfma_f32_16x16x32_bf16 v[96:99], a[192:195], v[56:59], v[96:99]\n"
    "buffer_load_dwordx4 v6, s[40:43], s56 offen offset:0 nt lds\n"
    "v_mfma_f32_16x16x32_bf16 v[92:95], a[192:195], v[52:55], v[92:95]\n"
    "ds_read_b64_tr_b16 a[214:215], v3 offset:43136\n"
    "v_cvt_pk_bf16_f32 v32, v32, v33\n"
    "v_mfma_f32_16x16x32_bf16 v[88:91], a[192:195], v[48:51], v[88:91]\n"
    "v_exp_f32 v78, v78\n"
    "v_mfma_f32_16x16x32_bf16 v[84:87], a[192:195], v[44:47], v[84:87]\n"
    "s_waitcnt lgkmcnt(7)\n"
    "ds_read_b64_tr_b16 a[192:193], v4 offset:36992\n"
    "v_fma_f32 v39, v39, s52, v14\n"
    "v_mfma_f32_16x16x32_bf16 a[216:219], a[196:199], v[44:47], a[216:219]\n"
    "ds_read_b64_tr_b16 a[194:195], v4 offset:43136\n"
    "v_fma_f32 v42, v42, s52, v15\n"
    "v_mfma_f32_16x16x32_bf16 a[220:223], a[196:199], v[48:51], a[220:223]\n"
    "v_exp_f32 v79, v79\n"
    "v_mfma_f32_16x16x32_bf16 a[224:227], a[196:199], v[52:55], a[224:227]\n"
    "v_cvt_pk_bf16_f32 v31, v78, v79\n"
    "v_fma_f32 v43, v43, s52, v15\n"
    "v_mfma_f32_16x16x32_bf16 a[228:231], a[196:199], v[56:59], a[228:231]\n"
    "s_waitcnt lgkmcnt(8)\n"
    "ds_read_b64_tr_b16 a[196:197], v5 offset:36992\n"
    "v_fma_f32 v60, v60, s52, v14\n"
    "v_mfma_f32_16x16x32_bf16 v[112:115], a[200:203], v[56:59], v[112:115]\n"
    "ds_read_b64_tr_b16 a[198:199], v5 offset:43136\n"
    "v_fma_f32 v61, v61, s52, v14\n"
    "v_mfma_f32_16x16x32_bf16 v[108:111], a[200:203], v[52:55], v[108:111]\n"
    "v_exp_f32 v34, v34\n"
    "v_mfma_f32_16x16x32_bf16 v[104:107], a[200:203], v[48:51], v[104:107]\n"
    "v_exp_f32 v35, v35\n"
    "v_mfma_f32_16x16x32_bf16 v[100:103], a[200:203], v[44:47], v[100:103]\n"
    "s_waitcnt lgkmcnt(6)\n"
    "ds_read_b64_tr_b16 a[200:201], v2 offset:37120\n"
    "v_cvt_pk_bf16_f32 v33, v34, v35\n"
    "v_mfma_f32_16x16x32_bf16 v[116:119], a[204:207], v[44:47], v[116:119]\n"
    "ds_read_b64_tr_b16 a[202:203], v2 offset:43264\n"
    "v_fma_f32 v62, v62, s52, v14\n"
    "v_mfma_f32_16x16x32_bf16 v[120:123], a[204:207], v[48:51], v[120:123]\n"
    "v_exp_f32 v80, v80\n"
    "v_mfma_f32_16x16x32_bf16 v[124:127], a[204:207], v[52:55], v[124:127]\n"
    "buffer_load_dwordx4 v7, s[40:43], s56 offen offset:1024 nt lds\n"
    "v_mfma_f32_16x16x32_bf16 v[128:131], a[204:207], v[56:59], v[128:131]\n"
    "s_waitcnt lgkmcnt(7)\n"
    "ds_read_b64_tr_b16 a[204:205], v3 offset:37120\n"
    "v_fma_f32 v63, v63, s52, v14\n"
    "v_mfma_f32_16x16x32_bf16 v[144:147], a[208:211], v[56:59], v[144:147]\n"
    "ds_read_b64_tr_b16 a[206:207], v3 offset:43264\n"
    "v_fma_f32 v64, v64, s52, v15\n"
    "v_mfma_f32_16x16x32_bf16 v[140:143], a[208:211], v[52:55], v[140:143]\n"
    "v_exp_f32 v81, v81\n"
    "v_mfma_f32_16x16x32_bf16 v[136:139], a[208:211], v[48:51], v[136:139]\n"
    "v_exp_f32 v82, v82\n"
    "v_mfma_f32_16x16x32_bf16 v[132:135], a[208:211], v[44:47], v[132:135]\n"
    "s_waitcnt lgkmcnt(8)\n"
    "ds_read_b64_tr_b16 a[208:209], v4 offset:37120\n"
    "v_cvt_pk_bf16_f32 v34, v80, v81\n"
    "v_mfma_f32_16x16x32_bf16 a[232:235], a[212:215], v[44:47], a[232:235]\n"
    "ds_read_b64_tr_b16 a[210:211], v4 offset:43264\n"
    "v_fma_f32 v65, v65, s52, v15\n"
    "v_mfma_f32_16x16x32_bf16 v[148:151], a[212:215], v[48:51], v[148:151]\n"
    "v_exp_f32 v83, v83\n"
    "v_mfma_f32_16x16x32_bf16 v[152:155], a[212:215], v[52:55], v[152:155]\n"
    "v_cvt_pk_bf16_f32 v35, v82, v83\n"
    "v_fma_f32 v66, v66, s52, v15\n"
    "v_mfma_f32_16x16x32_bf16 v[156:159], a[212:215], v[56:59], v[156:159]\n"
    "s_waitcnt lgkmcnt(8)\n"
    "ds_read_b64_tr_b16 a[212:213], v5 offset:37120\n"
    "v_fma_f32 v67, v67, s52, v15\n"
    "v_mfma_f32_16x16x32_bf16 v[172:175], a[192:195], v[56:59], v[172:175]\n"
    "v_exp_f32 v36, v36\n"
    "v_mfma_f32_16x16x32_bf16 v[168:171], a[192:195], v[52:55], v[168:171]\n"
    "v_exp_f32 v37, v37\n"
    "v_mfma_f32_16x16x32_bf16 v[164:167], a[192:195], v[48:51], v[164:167]\n"
    "ds_read_b64_tr_b16 a[214:215], v5 offset:43264\n"
    "v_cvt_pk_bf16_f32 v36, v36, v37\n"
    "v_mfma_f32_16x16x32_bf16 v[160:163], a[192:195], v[44:47], v[160:163]\n"
    "s_waitcnt lgkmcnt(8)\n"
    "v_exp_f32 v40, v40\n"
    "v_mfma_f32_16x16x32_bf16 v[176:179], a[196:199], v[44:47], v[176:179]\n"
    "buffer_load_dwordx4 v8, s[40:43], s56 offen offset:2048 nt lds\n"
    "v_mfma_f32_16x16x32_bf16 v[180:183], a[196:199], v[48:51], v[180:183]\n"
    "s_add_u32 m0, s54, 24576\n"
    "v_exp_f32 v41, v41\n"
    "v_mfma_f32_16x16x32_bf16 v[184:187], a[196:199], v[52:55], v[184:187]\n"
    "s_add_u32 s56, s56, 24576\n"
    "v_exp_f32 v38, v38\n"
    "v_mfma_f32_16x16x32_bf16 v[188:191], a[196:199], v[56:59], v[188:191]\n"
    "s_waitcnt lgkmcnt(6)\n"
    "v_exp_f32 v39, v39\n"
    "v_mfma_f32_16x16x32_bf16 v[204:207], a[200:203], v[56:59], v[204:207]\n"
    "s_waitcnt vmcnt(6) lgkmcnt(0)\n"
    "v_exp_f32 v42, v42\n"
    "v_mfma_f32_16x16x32_bf16 v[200:203], a[200:203], v[52:55], v[200:203]\n"
    "s_barrier\n"
    "ds_read_b128 a[192:195], v0 offset:0\n"
    "v_cvt_pk_bf16_f32 v40, v40, v41\n"
    "v_mfma_f32_16x16x32_bf16 v[196:199], a[200:203], v[48:51], v[196:199]\n"
    "ds_read_b128 a[196:199], v1 offset:0\n"
    "v_cvt_pk_bf16_f32 v37, v38, v39\n"
    "v_mfma_f32_16x16x32_bf16 v[192:195], a[200:203], v[44:47], v[192:195]\n"
    "v_exp_f32 v43, v43\n"
    "v_mfma_f32_16x16x32_bf16 v[208:211], a[204:207], v[44:47], v[208:211]\n"
    "ds_read_b128 a[200:203], v0 offset:128\n"
    "v_cvt_pk_bf16_f32 v41, v42, v43\n"
    "v_mfma_f32_16x16x32_bf16 v[212:215], a[204:207], v[48:51], v[212:215]\n"
    "v_exp_f32 v60, v60\n"
    "v_mfma_f32_16x16x32_bf16 v[216:219], a[204:207], v[52:55], v[216:219]\n"
    "v_exp_f32 v61, v61\n"
    "v_mfma_f32_16x16x32_bf16 v[220:223], a[204:207], v[56:59], v[220:223]\n"
    "ds_read_b128 a[204:207], v1 offset:128\n"
    "v_cvt_pk_bf16_f32 v38, v60, v61\n"
    "v_mfma_f32_16x16x32_bf16 v[236:239], a[208:211], v[56:59], v[236:239]\n"
    "v_exp_f32 v62, v62\n"
    "v_mfma_f32_16x16x32_bf16 v[232:235], a[208:211], v[52:55], v[232:235]\n"
    "v_exp_f32 v63, v63\n"
    "v_mfma_f32_16x16x32_bf16 v[228:231], a[208:211], v[48:51], v[228:231]\n"
    "v_exp_f32 v64, v64\n"
    "v_mfma_f32_16x16x32_bf16 v[224:227], a[208:211], v[44:47], v[224:227]\n"
    "buffer_load_dwordx4 v6, s[36:39], s56 offen offset:0 nt lds\n"
    "v_mfma_f32_16x16x32_bf16 v[240:243], a[212:215], v[44:47], v[240:243]\n"
    "ds_read_b128 a[208:211], v0 offset:256\n"
    "v_cvt_pk_bf16_f32 v39, v62, v63\n"
    "v_mfma_f32_16x16x32_bf16 v[244:247], a[212:215], v[48:51], v[244:247]\n"
    "v_exp_f32 v65, v65\n"
    "v_mfma_f32_16x16x32_bf16 v[248:251], a[212:215], v[52:55], v[248:251]\n"
    "v_exp_f32 v66, v66\n"
    "v_mfma_f32_16x16x32_bf16 v[252:255], a[212:215], v[56:59], v[252:255]\n"
    "s_waitcnt lgkmcnt(4)\n"
    "ds_read_b128 a[212:215], v1 offset:256\n"
    "v_cvt_pk_bf16_f32 v42, v64, v65\n"
    "v_mfma_f32_16x16x32_bf16 v[44:47], a[192:195], a[0:3], 0\n"
    "s_waitcnt lgkmcnt(4)\n"
    "v_exp_f32 v67, v67\n"
    "v_mfma_f32_16x16x32_bf16 v[44:47], a[196:199], a[4:7], v[44:47]\n"
    "s_waitcnt lgkmcnt(3)\n"
    "v_cvt_pk_bf16_f32 v43, v66, v67\n"
    "v_mfma_f32_16x16x32_bf16 v[44:47], a[200:203], a[8:11], v[44:47]\n"
    "v_mfma_f32_16x16x32_bf16 v[48:51], a[200:203], a[32:35], 0\n"
    "v_mfma_f32_16x16x32_bf16 v[48:51], a[196:199], a[28:31], v[48:51]\n"
    "v_mfma_f32_16x16x32_bf16 v[48:51], a[192:195], a[24:27], v[48:51]\n"
    "v_mfma_f32_16x16x32_bf16 v[52:55], a[192:195], a[48:51], 0\n"
    "v_mfma_f32_16x16x32_bf16 v[52:55], a[196:199], a[52:55], v[52:55]\n"
    "v_mfma_f32_16x16x32_bf16 v[52:55], a[200:203], a[56:59], v[52:55]\n"
    "v_mfma_f32_16x16x32_bf16 v[56:59], a[200:203], a[80:83], 0\n"
    "buffer_load_dwordx4 v7, s[36:39], s56 offen offset:1024 nt lds\n"
    "v_mfma_f32_16x16x32_bf16 v[56:59], a[196:199], a[76:79], v[56:59]\n"
    "s_nop 0\n"
    "ds_read_b128 a[196:199], v1 offset:6144\n"
    "ds_read_b128 a[200:203], v0 offset:6272\n"
    "v_mfma_f32_16x16x32_bf16 v[56:59], a[192:195], a[72:75], v[56:59]\n"
    "s_waitcnt lgkmcnt(4)\n"
    "ds_read_b128 a[192:195], v0 offset:6144\n"
    "v_mfma_f32_16x16x32_bf16 v[44:47], a[204:207], a[12:15], v[44:47]\n"
    "s_waitcnt lgkmcnt(4)\n"
    "v_mfma_f32_16x16x32_bf16 v[44:47], a[208:211], a[16:19], v[44:47]\n"
    "s_waitcnt lgkmcnt(3)\n"
    "v_mfma_f32_16x16x32_bf16 v[44:47], a[212:215], a[20:23], v[44:47]\n"
    "v_mfma_f32_16x16x32_bf16 v[48:51], a[212:215], a[44:47], v[48:51]\n"
    "v_mfma_f32_16x16x32_bf16 v[48:51], a[208:211], a[40:43], v[48:51]\n"
    "v_mfma_f32_16x16x32_bf16 v[48:51], a[204:207], a[36:39], v[48:51]\n"
    "v_mfma_f32_16x16x32_bf16 v[52:55], a[204:207], a[60:63], v[52:55]\n"
    "v_mfma_f32_16x16x32_bf16 v[52:55], a[208:211], a[64:67], v[52:55]\n"
    "v_mfma_f32_16x16x32_bf16 v[52:55], a[212:215], a[68:71], v[52:55]\n"
    "v_mfma_f32_16x16x32_bf16 v[56:59], a[212:215], a[92:95], v[56:59]\n"
    "ds_read_b128 a[212:215], v1 offset:6400\n"
    "v_fma_f32 v44, v44, s52, v12\n"
    "v_mfma_f32_16x16x32_bf16 v[56:59], a[208:211], a[88:91], v[56:59]\n"
    "ds_read_b128 a[208:211], v0 offset:6400\n"
    "v_fma_f32 v45, v45, s52, v12\n"
    "v_mfma_f32_16x16x32_bf16 v[56:59], a[204:207], a[84:87], v[56:59]\n"
    "s_waitcnt lgkmcnt(2)\n"
    "buffer_load_dwordx4 v8, s[36:39], s56 offen offset:2048 nt lds\n"
    "v_mfma_f32_16x16x32_bf16 v[68:71], a[192:195], a[0:3], 0\n"
    "s_add_u32 m0, s54, 73728\n"
    "ds_read_b128 a[204:207], v1 offset:6272\n"
    "v_fma_f32 v46, v46, s52, v12\n"
    "v_mfma_f32_16x16x32_bf16 v[68:71], a[196:199], a[4:7], v[68:71]\n"
    "v_exp_f32 v44, v44\n"
    "v_mfma_f32_16x16x32_bf16 v[68:71], a[200:203], a[8:11], v[68:71]\n"
    "v_exp_f32 v45, v45\n"
    "v_mfma_f32_16x16x32_bf16 v[72:75], a[200:203], a[32:35], 0\n"
    "v_fma_f32 v47, v47, s52, v12\n"
    "v_cvt_pk_bf16_f32 v44, v44, v45\n"
    "v_mfma_f32_16x16x32_bf16 v[72:75], a[196:199], a[28:31], v[72:75]\n"
    "v_exp_f32 v46, v46\n"
    "v_mfma_f32_16x16x32_bf16 v[72:75], a[192:195], a[24:27], v[72:75]\n"
    "v_exp_f32 v47, v47\n"
    "v_mfma_f32_16x16x32_bf16 v[76:79], a[192:195], a[48:51], 0\n"
    "v_cvt_pk_bf16_f32 v45, v46, v47\n"
    "v_fma_f32 v48, v48, s52, v13\n"
    "v_mfma_f32_16x16x32_bf16 v[76:79], a[196:199], a[52:55], v[76:79]\n"
    "v_fma_f32 v49, v49, s52, v13\n"
    "v_fma_f32 v50, v50, s52, v13\n"
    "v_mfma_f32_16x16x32_bf16 v[76:79], a[200:203], a[56:59], v[76:79]\n"
    "v_exp_f32 v48, v48\n"
    "v_mfma_f32_16x16x32_bf16 v[80:83], a[200:203], a[80:83], 0\n"
    "ds_read_b64_tr_b16 a[200:201], v4 offset:49152\n"
    "v_fma_f32 v51, v51, s52, v13\n"
    "v_mfma_f32_16x16x32_bf16 v[80:83], a[196:199], a[76:79], v[80:83]\n"
    "ds_read_b64_tr_b16 a[196:197], v3 offset:49152\n"
    "v_fma_f32 v52, v52, s52, v14\n"
    "v_mfma_f32_16x16x32_bf16 v[80:83], a[192:195], a[72:75], v[80:83]\n"
    "s_waitcnt lgkmcnt(2)\n"
    "ds_read_b64_tr_b16 a[192:193], v2 offset:49152\n"
    "v_fma_f32 v53, v53, s52, v14\n"
    "v_mfma_f32_16x16x32_bf16 v[68:71], a[204:207], a[12:15], v[68:71]\n"
    "ds_read_b64_tr_b16 a[194:195], v2 offset:55296\n"
    "v_fma_f32 v56, v56, s52, v15\n"
    "v_mfma_f32_16x16x32_bf16 v[68:71], a[208:211], a[16:19], v[68:71]\n"
    "ds_read_b64_tr_b16 a[198:199], v3 offset:55296\n"
    "v_fma_f32 v57, v57, s52, v15\n"
    "v_mfma_f32_16x16x32_bf16 v[68:71], a[212:215], a[20:23], v[68:71]\n"
    "buffer_load_dwordx4 v6, s[40:43], s55 offen offset:0 nt lds\n"
    "v_mfma_f32_16x16x32_bf16 v[72:75], a[212:215], a[44:47], v[72:75]\n"
    "ds_read_b64_tr_b16 a[202:203], v4 offset:55296\n"
    "v_fma_f32 v54, v54, s52, v14\n"
    "v_mfma_f32_16x16x32_bf16 v[72:75], a[208:211], a[40:43], v[72:75]\n"
    "v_exp_f32 v49, v49\n"
    "v_mfma_f32_16x16x32_bf16 v[72:75], a[204:207], a[36:39], v[72:75]\n"
    "v_exp_f32 v50, v50\n"
    "v_mfma_f32_16x16x32_bf16 v[76:79], a[204:207], a[60:63], v[76:79]\n"
    "v_fma_f32 v68, v68, s52, v12\n"
    "v_fma_f32 v69, v69, s52, v12\n"
    "v_mfma_f32_16x16x32_bf16 v[76:79], a[208:211], a[64:67], v[76:79]\n"
    "v_fma_f32 v70, v70, s52, v12\n"
    "v_fma_f32 v71, v71, s52, v12\n"
    "v_mfma_f32_16x16x32_bf16 v[76:79], a[212:215], a[68:71], v[76:79]\n"
    "v_exp_f32 v68, v68\n"
    "v_mfma_f32_16x16x32_bf16 v[80:83], a[212:215], a[92:95], v[80:83]\n"
    "ds_read_b64_tr_b16 a[212:213], v3 offset:49280\n"
    "v_fma_f32 v72, v72, s52, v13\n"
    "v_mfma_f32_16x16x32_bf16 v[80:83], a[208:211], a[88:91], v[80:83]\n"
    "ds_read_b64_tr_b16 a[208:209], v2 offset:49280\n"
    "v_fma_f32 v73, v73, s52, v13\n"
    "v_mfma_f32_16x16x32_bf16 v[80:83], a[204:207], a[84:87], v[80:83]\n"
    "ds_read_b64_tr_b16 a[204:205], v5 offset:49152\n"
    "v_fma_f32 v74, v74, s52, v13\n"
    "v_smfmac_f32_16x16x64_bf16 a[236:239], a[252:255], v[28:35], v23\n"
    "ds_read_b64_tr_b16 a[206:207], v5 offset:55296\n"
    "v_fma_f32 v75, v75, s52, v13\n"
    "v_smfmac_f32_16x16x64_bf16 a[240:243], a[252:255], v[36:43], v23\n"
    "s_waitcnt lgkmcnt(6)\n"
    "ds_read_b64_tr_b16 a[210:211], v2 offset:55424\n"
    "v_cvt_pk_bf16_f32 v48, v48, v49\n"
    "v_mfma_f32_16x16x32_bf16 v[96:99], a[192:195], v[40:43], v[96:99]\n"
    "ds_read_b64_tr_b16 a[214:215], v3 offset:55424\n"
    "v_fma_f32 v55, v55, s52, v14\n"
    "v_mfma_f32_16x16x32_bf16 v[92:95], a[192:195], v[36:39], v[92:95]\n"
    "v_exp_f32 v69, v69\n"
    "v_mfma_f32_16x16x32_bf16 v[88:91], a[192:195], v[32:35], v[88:91]\n"
    "buffer_load_dwordx4 v7, s[40:43], s55 offen offset:1024 nt lds\n"
    "v_mfma_f32_16x16x32_bf16 v[84:87], a[192:195], v[28:31], v[84:87]\n"
    "s_waitcnt lgkmcnt(7)\n"
    "ds_read_b64_tr_b16 a[192:193], v4 offset:49280\n"
    "v_cvt_pk_bf16_f32 v46, v68, v69\n"
    "v_mfma_f32_16x16x32_bf16 a[216:219], a[196:199], v[28:31], a[216:219]\n"
    "ds_read_b64_tr_b16 a[194:195], v4 offset:55424\n"
    "v_fma_f32 v58, v58, s52, v15\n"
    "v_mfma_f32_16x16x32_bf16 a[220:223], a[196:199], v[32:35], a[220:223]\n"
    "v_exp_f32 v70, v70\n"
    "v_mfma_f32_16x16x32_bf16 a[224:227], a[196:199], v[36:39], a[224:227]\n"
    "v_exp_f32 v71, v71\n"
    "v_mfma_f32_16x16x32_bf16 a[228:231], a[196:199], v[40:43], a[228:231]\n"
    "s_waitcnt lgkmcnt(8)\n"
    "ds_read_b64_tr_b16 a[196:197], v5 offset:49280\n"
    "v_cvt_pk_bf16_f32 v47, v70, v71\n"
    "v_mfma_f32_16x16x32_bf16 v[112:115], a[200:203], v[40:43], v[112:115]\n"
    "ds_read_b64_tr_b16 a[198:199], v5 offset:55424\n"
    "v_fma_f32 v59, v59, s52, v15\n"
    "v_mfma_f32_16x16x32_bf16 v[108:111], a[200:203], v[36:39], v[108:111]\n"
    "v_exp_f32 v51, v51\n"
    "v_mfma_f32_16x16x32_bf16 v[104:107], a[200:203], v[32:35], v[104:107]\n"
    "v_exp_f32 v72, v72\n"
    "v_mfma_f32_16x16x32_bf16 v[100:103], a[200:203], v[28:31], v[100:103]\n"
    "s_waitcnt lgkmcnt(6)\n"
    "ds_read_b64_tr_b16 a[200:201], v2 offset:49408\n"
    "v_cvt_pk_bf16_f32 v49, v50, v51\n"
    "v_mfma_f32_16x16x32_bf16 v[116:119], a[204:207], v[28:31], v[116:119]\n"
    "ds_read_b64_tr_b16 a[202:203], v2 offset:55552\n"
    "v_fma_f32 v76, v76, s52, v14\n"
    "v_mfma_f32_16x16x32_bf16 v[120:123], a[204:207], v[32:35], v[120:123]\n"
    "v_exp_f32 v73, v73\n"
    "v_mfma_f32_16x16x32_bf16 v[124:127], a[204:207], v[36:39], v[124:127]\n"
    "v_exp_f32 v74, v74\n"
    "v_mfma_f32_16x16x32_bf16 v[128:131], a[204:207], v[40:43], v[128:131]\n"
    "s_waitcnt lgkmcnt(7)\n"
    "ds_read_b64_tr_b16 a[204:205], v3 offset:49408\n"
    "v_cvt_pk_bf16_f32 v50, v72, v73\n"
    "v_mfma_f32_16x16x32_bf16 v[144:147], a[208:211], v[40:43], v[144:147]\n"
    "buffer_load_dwordx4 v8, s[40:43], s55 offen offset:2048 nt lds\n"
    "v_mfma_f32_16x16x32_bf16 v[140:143], a[208:211], v[36:39], v[140:143]\n"
    "s_add_u32 m0, s57, 0\n"
    "ds_read_b64_tr_b16 a[206:207], v3 offset:55552\n"
    "v_fma_f32 v77, v77, s52, v14\n"
    "v_mfma_f32_16x16x32_bf16 v[136:139], a[208:211], v[32:35], v[136:139]\n"
    "s_add_u32 s55, s55, 24576\n"
    "buffer_load_dwordx4 v9, s[44:47], s63 offen offset:0 nt lds\n"
    "v_mfma_f32_16x16x32_bf16 v[132:135], a[208:211], v[28:31], v[132:135]\n"
    "s_waitcnt lgkmcnt(8)\n"
    "s_add_u32 s63, s62, 6144\n"
    "ds_read_b64_tr_b16 a[208:209], v4 offset:49408\n"
    "v_mfma_f32_16x16x32_bf16 a[232:235], a[212:215], v[28:31], a[232:235]\n"
    "buffer_load_dwordx4 v9, s[44:47], s64 offen offset:1024 nt lds\n"
    "v_mfma_f32_16x16x32_bf16 v[148:151], a[212:215], v[32:35], v[148:151]\n"
    "s_add_u32 m0, s57, 2048\n"
    "s_add_u32 s64, s62, 8192\n"
    "ds_read_b64_tr_b16 a[210:211], v4 offset:55552\n"
    "v_mfma_f32_16x16x32_bf16 v[152:155], a[212:215], v[36:39], v[152:155]\n"
    "buffer_load_dwordx4 v9, s[44:47], s63 offen offset:0 nt lds\n"
    "v_mfma_f32_16x16x32_bf16 v[156:159], a[212:215], v[40:43], v[156:159]\n"
    "s_waitcnt lgkmcnt(8)\n"
    "s_add_u32 s63, s62, 12288\n"
    "ds_read_b64_tr_b16 a[212:213], v5 offset:49408\n"
    "v_mfma_f32_16x16x32_bf16 v[172:175], a[192:195], v[40:43], v[172:175]\n"
    "buffer_load_dwordx4 v9, s[44:47], s64 offen offset:1024 nt lds\n"
    "v_mfma_f32_16x16x32_bf16 v[168:171], a[192:195], v[36:39], v[168:171]\n"
    "s_add_u32 m0, s54, 36864\n"
    "ds_read_b64_tr_b16 a[214:215], v5 offset:55552\n"
    "v_fma_f32 v78, v78, s52, v14\n"
    "v_mfma_f32_16x16x32_bf16 v[164:167], a[192:195], v[32:35], v[164:167]\n"
    "s_add_u32 s64, s62, 14336\n"
    "v_exp_f32 v75, v75\n"
    "v_mfma_f32_16x16x32_bf16 v[160:163], a[192:195], v[28:31], v[160:163]\n"
    "s_waitcnt lgkmcnt(8)\n"
    "v_cvt_pk_bf16_f32 v51, v74, v75\n"
    "v_fma_f32 v79, v79, s52, v14\n"
    "v_mfma_f32_16x16x32_bf16 v[176:179], a[196:199], v[28:31], v[176:179]\n"
    "v_exp_f32 v52, v52\n"
    "v_mfma_f32_16x16x32_bf16 v[180:183], a[196:199], v[32:35], v[180:183]\n"
    "v_exp_f32 v53, v53\n"
    "v_mfma_f32_16x16x32_bf16 v[184:187], a[196:199], v[36:39], v[184:187]\n"
    "v_exp_f32 v56, v56\n"
    "v_mfma_f32_16x16x32_bf16 v[188:191], a[196:199], v[40:43], v[188:191]\n"
    "s_waitcnt vmcnt(10) lgkmcnt(0)\n"
    "buffer_load_dwordx4 v6, s[36:39], s55 offen offset:0 nt lds\n"
    "v_mfma_f32_16x16x32_bf16 v[204:207], a[200:203], v[40:43], v[204:207]\n"
    "s_barrier\n"
    "ds_read_b128 a[192:195], v0 offset:12288\n"
    "v_fma_f32 v80, v80, s52, v15\n"
    "v_mfma_f32_16x16x32_bf16 v[200:203], a[200:203], v[36:39], v[200:203]\n"
    "ds_read_b128 a[196:199], v1 offset:12288\n"
    "v_fma_f32 v81, v81, s52, v15\n"
    "v_mfma_f32_16x16x32_bf16 v[196:199], a[200:203], v[32:35], v[196:199]\n"
    "v_exp_f32 v57, v57\n"
    "v_mfma_f32_16x16x32_bf16 v[192:195], a[200:203], v[28:31], v[192:195]\n"
    "ds_read_b128 a[200:203], v0 offset:12416\n"
    "v_fma_f32 v82, v82, s52, v15\n"
    "v_mfma_f32_16x16x32_bf16 v[208:211], a[204:207], v[28:31], v[208:211]\n"
    "v_exp_f32 v54, v54\n"
    "v_mfma_f32_16x16x32_bf16 v[212:215], a[204:207], v[32:35], v[212:215]\n"
    "v_exp_f32 v55, v55\n"
    "v_mfma_f32_16x16x32_bf16 v[216:219], a[204:207], v[36:39], v[216:219]\n"
    "v_exp_f32 v58, v58\n"
    "v_mfma_f32_16x16x32_bf16 v[220:223], a[204:207], v[40:43], v[220:223]\n"
    "ds_read_b128 a[204:207], v1 offset:12416\n"
    "v_fma_f32 v83, v83, s52, v15\n"
    "v_mfma_f32_16x16x32_bf16 v[236:239], a[208:211], v[40:43], v[236:239]\n"
    "v_exp_f32 v59, v59\n"
    "v_mfma_f32_16x16x32_bf16 v[232:235], a[208:211], v[36:39], v[232:235]\n"
    "v_cvt_pk_bf16_f32 v52, v52, v53\n"
    "v_cvt_pk_bf16_f32 v56, v56, v57\n"
    "v_mfma_f32_16x16x32_bf16 v[228:231], a[208:211], v[32:35], v[228:231]\n"
    "v_exp_f32 v76, v76\n"
    "v_mfma_f32_16x16x32_bf16 v[224:227], a[208:211], v[28:31], v[224:227]\n"
    "ds_read_b128 a[208:211], v0 offset:12544\n"
    "v_cvt_pk_bf16_f32 v53, v54, v55\n"
    "v_mfma_f32_16x16x32_bf16 v[240:243], a[212:215], v[28:31], v[240:243]\n"
    "v_exp_f32 v77, v77\n"
    "v_mfma_f32_16x16x32_bf16 v[244:247], a[212:215], v[32:35], v[244:247]\n"
    "buffer_load_dwordx4 v7, s[36:39], s55 offen offset:1024 nt lds\n"
    "v_mfma_f32_16x16x32_bf16 v[248:251], a[212:215], v[36:39], v[248:251]\n"
    "v_exp_f32 v78, v78\n"
    "v_mfma_f32_16x16x32_bf16 v[252:255], a[212:215], v[40:43], v[252:255]\n"
    "s_waitcnt lgkmcnt(4)\n"
    "ds_read_b128 a[212:215], v1 offset:12544\n"
    "v_cvt_pk_bf16_f32 v57, v58, v59\n"
    "v_mfma_f32_16x16x32_bf16 v[28:31], a[192:195], a[0:3], 0\n"
    "s_waitcnt lgkmcnt(4)\n"
    "v_exp_f32 v79, v79\n"
    "v_mfma_f32_16x16x32_bf16 v[28:31], a[196:199], a[4:7], v[28:31]\n"
    "s_waitcnt lgkmcnt(3)\n"
    "v_exp_f32 v80, v80\n"
    "v_mfma_f32_16x16x32_bf16 v[28:31], a[200:203], a[8:11], v[28:31]\n"
    "v_exp_f32 v81, v81\n"
    "v_mfma_f32_16x16x32_bf16 v[32:35], a[200:203], a[32:35], 0\n"
    "v_exp_f32 v82, v82\n"
    "v_mfma_f32_16x16x32_bf16 v[32:35], a[196:199], a[28:31], v[32:35]\n"
    "v_exp_f32 v83, v83\n"
    "v_mfma_f32_16x16x32_bf16 v[32:35], a[192:195], a[24:27], v[32:35]\n"
    "v_cvt_pk_bf16_f32 v54, v76, v77\n"
    "v_cvt_pk_bf16_f32 v55, v78, v79\n"
    "v_mfma_f32_16x16x32_bf16 v[36:39], a[192:195], a[48:51], 0\n"
    "v_cvt_pk_bf16_f32 v58, v80, v81\n"
    "v_cvt_pk_bf16_f32 v59, v82, v83\n"
    "v_mfma_f32_16x16x32_bf16 v[36:39], a[196:199], a[52:55], v[36:39]\n"
    "v_mfma_f32_16x16x32_bf16 v[36:39], a[200:203], a[56:59], v[36:39]\n"
    "v_mfma_f32_16x16x32_bf16 v[40:43], a[200:203], a[80:83], 0\n"
    "ds_read_b128 a[200:203], v0 offset:18560\n"
    "v_mfma_f32_16x16x32_bf16 v[40:43], a[196:199], a[76:79], v[40:43]\n"
    "ds_read_b128 a[196:199], v1 offset:18432\n"
    "v_mfma_f32_16x16x32_bf16 v[40:43], a[192:195], a[72:75], v[40:43]\n"
    "s_waitcnt lgkmcnt(4)\n"
    "buffer_load_dwordx4 v8, s[36:39], s55 offen offset:2048 nt lds\n"
    "v_mfma_f32_16x16x32_bf16 v[28:31], a[204:207], a[12:15], v[28:31]\n"
    "s_waitcnt lgkmcnt(3)\n"
    "s_add_u32 m0, s54, 86016\n"
    "ds_read_b128 a[192:195], v0 offset:18432\n"
    "v_mfma_f32_16x16x32_bf16 v[28:31], a[208:211], a[16:19], v[28:31]\n"
    "s_waitcnt lgkmcnt(3)\n"
    "v_mfma_f32_16x16x32_bf16 v[28:31], a[212:215], a[20:23], v[28:31]\n"
    "v_mfma_f32_16x16x32_bf16 v[32:35], a[212:215], a[44:47], v[32:35]\n"
    "v_mfma_f32_16x16x32_bf16 v[32:35], a[208:211], a[40:43], v[32:35]\n"
    "v_mfma_f32_16x16x32_bf16 v[32:35], a[204:207], a[36:39], v[32:35]\n"
    "v_mfma_f32_16x16x32_bf16 v[36:39], a[204:207], a[60:63], v[36:39]\n"
    "v_mfma_f32_16x16x32_bf16 v[36:39], a[208:211], a[64:67], v[36:39]\n"
    "v_mfma_f32_16x16x32_bf16 v[36:39], a[212:215], a[68:71], v[36:39]\n"
    "v_mfma_f32_16x16x32_bf16 v[40:43], a[212:215], a[92:95], v[40:43]\n"
    "ds_read_b128 a[212:215], v1 offset:18688\n"
    "v_fma_f32 v28, v28, s52, v12\n"
    "v_mfma_f32_16x16x32_bf16 v[40:43], a[208:211], a[88:91], v[40:43]\n"
    "ds_read_b128 a[208:211], v0 offset:18688\n"
    "v_fma_f32 v29, v29, s52, v12\n"
    "v_mfma_f32_16x16x32_bf16 v[40:43], a[204:207], a[84:87], v[40:43]\n"
    "s_waitcnt lgkmcnt(2)\n"
    "ds_read_b128 a[204:207], v1 offset:18560\n"
    "v_fma_f32 v30, v30, s52, v12\n"
    "v_mfma_f32_16x16x32_bf16 v[60:63], a[192:195], a[0:3], 0\n"
    "v_exp_f32 v28, v28\n"
    "v_mfma_f32_16x16x32_bf16 v[60:63], a[196:199], a[4:7], v[60:63]\n"
    "v_exp_f32 v29, v29\n"
    "v_mfma_f32_16x16x32_bf16 v[60:63], a[200:203], a[8:11], v[60:63]\n"
    "buffer_load_dwordx4 v6, s[40:43], s56 offen offset:0 nt lds\n"
    "v_mfma_f32_16x16x32_bf16 v[64:67], a[200:203], a[32:35], 0\n"
    "v_fma_f32 v31, v31, s52, v12\n"
    "v_cvt_pk_bf16_f32 v28, v28, v29\n"
    "v_mfma_f32_16x16x32_bf16 v[64:67], a[196:199], a[28:31], v[64:67]\n"
    "v_exp_f32 v30, v30\n"
    "v_mfma_f32_16x16x32_bf16 v[64:67], a[192:195], a[24:27], v[64:67]\n"
    "v_exp_f32 v31, v31\n"
    "v_mfma_f32_16x16x32_bf16 v[68:71], a[192:195], a[48:51], 0\n"
    "v_cvt_pk_bf16_f32 v29, v30, v31\n"
    "v_fma_f32 v32, v32, s52, v13\n"
    "v_mfma_f32_16x16x32_bf16 v[68:71], a[196:199], a[52:55], v[68:71]\n"
    "v_fma_f32 v33, v33, s52, v13\n"
    "v_fma_f32 v34, v34, s52, v13\n"
    "v_mfma_f32_16x16x32_bf16 v[68:71], a[200:203], a[56:59], v[68:71]\n"
    "v_exp_f32 v32, v32\n"
    "v_mfma_f32_16x16x32_bf16 v[72:75], a[200:203], a[80:83], 0\n"
    "ds_read_b64_tr_b16 a[200:201], v4 offset:0\n"
    "v_fma_f32 v35, v35, s52, v13\n"
    "v_mfma_f32_16x16x32_bf16 v[72:75], a[196:199], a[76:79], v[72:75]\n"
    "ds_read_b64_tr_b16 a[196:197], v3 offset:0\n"
    "v_fma_f32 v36, v36, s52, v14\n"
    "v_mfma_f32_16x16x32_bf16 v[72:75], a[192:195], a[72:75], v[72:75]\n"
    "s_waitcnt lgkmcnt(2)\n"
    "ds_read_b64_tr_b16 a[192:193], v2 offset:0\n"
    "v_fma_f32 v37, v37, s52, v14\n"
    "v_mfma_f32_16x16x32_bf16 v[60:63], a[204:207], a[12:15], v[60:63]\n"
    "ds_read_b64_tr_b16 a[194:195], v2 offset:6144\n"
    "v_fma_f32 v40, v40, s52, v15\n"
    "v_mfma_f32_16x16x32_bf16 v[60:63], a[208:211], a[16:19], v[60:63]\n"
    "ds_read_b64_tr_b16 a[198:199], v3 offset:6144\n"
    "v_fma_f32 v41, v41, s52, v15\n"
    "v_mfma_f32_16x16x32_bf16 v[60:63], a[212:215], a[20:23], v[60:63]\n"
    "ds_read_b64_tr_b16 a[202:203], v4 offset:6144\n"
    "v_fma_f32 v38, v38, s52, v14\n"
    "v_mfma_f32_16x16x32_bf16 v[64:67], a[212:215], a[44:47], v[64:67]\n"
    "v_exp_f32 v33, v33\n"
    "v_mfma_f32_16x16x32_bf16 v[64:67], a[208:211], a[40:43], v[64:67]\n"
    "buffer_load_dwordx4 v7, s[40:43], s56 offen offset:1024 nt lds\n"
    "v_mfma_f32_16x16x32_bf16 v[64:67], a[204:207], a[36:39], v[64:67]\n"
    "v_exp_f32 v34, v34\n"
    "v_mfma_f32_16x16x32_bf16 v[68:71], a[204:207], a[60:63], v[68:71]\n"
    "v_fma_f32 v60, v60, s52, v12\n"
    "v_fma_f32 v61, v61, s52, v12\n"
    "v_mfma_f32_16x16x32_bf16 v[68:71], a[208:211], a[64:67], v[68:71]\n"
    "v_fma_f32 v62, v62, s52, v12\n"
    "v_fma_f32 v63, v63, s52, v12\n"
    "v_mfma_f32_16x16x32_bf16 v[68:71], a[212:215], a[68:71], v[68:71]\n"
    "v_exp_f32 v60, v60\n"
    "v_mfma_f32_16x16x32_bf16 v[72:75], a[212:215], a[92:95], v[72:75]\n"
    "ds_read_b64_tr_b16 a[212:213], v3 offset:128\n"
    "v_fma_f32 v64, v64, s52, v13\n"
    "v_mfma_f32_16x16x32_bf16 v[72:75], a[208:211], a[88:91], v[72:75]\n"
    "ds_read_b64_tr_b16 a[208:209], v2 offset:128\n"
    "v_fma_f32 v65, v65, s52, v13\n"
    "v_mfma_f32_16x16x32_bf16 v[72:75], a[204:207], a[84:87], v[72:75]\n"
    "ds_read_b64_tr_b16 a[204:205], v5 offset:0\n"
    "v_fma_f32 v66, v66, s52, v13\n"
    "v_smfmac_f32_16x16x64_bf16 a[236:239], a[252:255], v[44:51], v23\n"
    "ds_read_b64_tr_b16 a[206:207], v5 offset:6144\n"
    "v_fma_f32 v67, v67, s52, v13\n"
    "v_smfmac_f32_16x16x64_bf16 a[240:243], a[252:255], v[52:59], v23\n"
    "s_waitcnt lgkmcnt(6)\n"
    "ds_read_b64_tr_b16 a[210:211], v2 offset:6272\n"
    "v_cvt_pk_bf16_f32 v32, v32, v33\n"
    "v_mfma_f32_16x16x32_bf16 v[96:99], a[192:195], v[56:59], v[96:99]\n"
    "ds_read_b64_tr_b16 a[214:215], v3 offset:6272\n"
    "v_fma_f32 v39, v39, s52, v14\n"
    "v_mfma_f32_16x16x32_bf16 v[92:95], a[192:195], v[52:55], v[92:95]\n"
    "v_exp_f32 v61, v61\n"
    "v_mfma_f32_16x16x32_bf16 v[88:91], a[192:195], v[48:51], v[88:91]\n"
    "v_exp_f32 v62, v62\n"
    "v_mfma_f32_16x16x32_bf16 v[84:87], a[192:195], v[44:47], v[84:87]\n"
    "s_waitcnt lgkmcnt(7)\n"
    "ds_read_b64_tr_b16 a[192:193], v4 offset:128\n"
    "v_cvt_pk_bf16_f32 v30, v60, v61\n"
    "v_mfma_f32_16x16x32_bf16 a[216:219], a[196:199], v[44:47], a[216:219]\n"
    "buffer_load_dwordx4 v8, s[40:43], s56 offen offset:2048 nt lds\n"
    "v_mfma_f32_16x16x32_bf16 a[220:223], a[196:199], v[48:51], a[220:223]\n"
    "s_add_u32 m0, s57, 4096\n"
    "ds_read_b64_tr_b16 a[194:195], v4 offset:6272\n"
    "v_fma_f32 v42, v42, s52, v15\n"
    "v_mfma_f32_16x16x32_bf16 a[224:227], a[196:199], v[52:55], a[224:227]\n"
    "s_add_u32 s56, s56, 24576\n"
    "buffer_load_dwordx4 v9, s[44:47], s63 offen offset:0 nt lds\n"
    "v_mfma_f32_16x16x32_bf16 a[228:231], a[196:199], v[56:59], a[228:231]\n"
    "s_waitcnt lgkmcnt(8)\n"
    "s_add_u32 s63, s62, 18432\n"
    "ds_read_b64_tr_b16 a[196:197], v5 offset:128\n"
    "v_mfma_f32_16x16x32_bf16 v[112:115], a[200:203], v[56:59], v[112:115]\n"
    "buffer_load_dwordx4 v9, s[44:47], s64 offen offset:1024 nt lds\n"
    "v_mfma_f32_16x16x32_bf16 v[108:111], a[200:203], v[52:55], v[108:111]\n"
    "s_add_u32 m0, s54, 49152\n"
    "ds_read_b64_tr_b16 a[198:199], v5 offset:6272\n"
    "v_fma_f32 v43, v43, s52, v15\n"
    "v_mfma_f32_16x16x32_bf16 v[104:107], a[200:203], v[48:51], v[104:107]\n"
    "s_add_u32 s64, s62, 20480\n"
    "v_exp_f32 v63, v63\n"
    "v_mfma_f32_16x16x32_bf16 v[100:103], a[200:203], v[44:47], v[100:103]\n"
    "s_waitcnt lgkmcnt(6)\n"
    "ds_read_b64_tr_b16 a[200:201], v2 offset:256\n"
    "v_cvt_pk_bf16_f32 v31, v62, v63\n"
    "v_mfma_f32_16x16x32_bf16 v[116:119], a[204:207], v[44:47], v[116:119]\n"
    "ds_read_b64_tr_b16 a[202:203], v2 offset:6400\n"
    "v_fma_f32 v68, v68, s52, v14\n"
    "v_mfma_f32_16x16x32_bf16 v[120:123], a[204:207], v[48:51], v[120:123]\n"
    "v_exp_f32 v35, v35\n"
    "v_mfma_f32_16x16x32_bf16 v[124:127], a[204:207], v[52:55], v[124:127]\n"
    "v_exp_f32 v64, v64\n"
    "v_mfma_f32_16x16x32_bf16 v[128:131], a[204:207], v[56:59], v[128:131]\n"
    "s_waitcnt lgkmcnt(7)\n"
    "ds_read_b64_tr_b16 a[204:205], v3 offset:256\n"
    "v_cvt_pk_bf16_f32 v33, v34, v35\n"
    "v_mfma_f32_16x16x32_bf16 v[144:147], a[208:211], v[56:59], v[144:147]\n"
    "ds_read_b64_tr_b16 a[206:207], v3 offset:6400\n"
    "v_fma_f32 v69, v69, s52, v14\n"
    "v_mfma_f32_16x16x32_bf16 v[140:143], a[208:211], v[52:55], v[140:143]\n"
    "v_exp_f32 v65, v65\n"
    "v_mfma_f32_16x16x32_bf16 v[136:139], a[208:211], v[48:51], v[136:139]\n"
    "v_exp_f32 v66, v66\n"
    "v_mfma_f32_16x16x32_bf16 v[132:135], a[208:211], v[44:47], v[132:135]\n"
    "s_waitcnt lgkmcnt(8)\n"
    "buffer_load_dwordx4 v6, s[36:39], s56 offen offset:0 nt lds\n"
    "v_mfma_f32_16x16x32_bf16 a[232:235], a[212:215], v[44:47], a[232:235]\n"
    "ds_read_b64_tr_b16 a[208:209], v4 offset:256\n"
    "v_cvt_pk_bf16_f32 v34, v64, v65\n"
    "v_mfma_f32_16x16x32_bf16 v[148:151], a[212:215], v[48:51], v[148:151]\n"
    "ds_read_b64_tr_b16 a[210:211], v4 offset:6400\n"
    "v_fma_f32 v70, v70, s52, v14\n"
    "v_mfma_f32_16x16x32_bf16 v[152:155], a[212:215], v[52:55], v[152:155]\n"
    "v_exp_f32 v67, v67\n"
    "v_mfma_f32_16x16x32_bf16 v[156:159], a[212:215], v[56:59], v[156:159]\n"
    "s_waitcnt lgkmcnt(8)\n"
    "ds_read_b64_tr_b16 a[212:213], v5 offset:256\n"
    "v_cvt_pk_bf16_f32 v35, v66, v67\n"
    "v_mfma_f32_16x16x32_bf16 v[172:175], a[192:195], v[56:59], v[172:175]\n"
    "ds_read_b64_tr_b16 a[214:215], v5 offset:6400\n"
    "v_fma_f32 v71, v71, s52, v14\n"
    "v_mfma_f32_16x16x32_bf16 v[168:171], a[192:195], v[52:55], v[168:171]\n"
    "v_exp_f32 v36, v36\n"
    "v_mfma_f32_16x16x32_bf16 v[164:167], a[192:195], v[48:51], v[164:167]\n"
    "v_exp_f32 v37, v37\n"
    "v_mfma_f32_16x16x32_bf16 v[160:163], a[192:195], v[44:47], v[160:163]\n"
    "s_waitcnt lgkmcnt(8)\n"
    "v_exp_f32 v40, v40\n"
    "v_mfma_f32_16x16x32_bf16 v[176:179], a[196:199], v[44:47], v[176:179]\n"
    "v_exp_f32 v41, v41\n"
    "v_mfma_f32_16x16x32_bf16 v[180:183], a[196:199], v[48:51], v[180:183]\n"
    "v_exp_f32 v38, v38\n"
    "v_mfma_f32_16x16x32_bf16 v[184:187], a[196:199], v[52:55], v[184:187]\n"
    "s_waitcnt vmcnt(13) lgkmcnt(0)\n"
    "v_exp_f32 v39, v39\n"
    "v_mfma_f32_16x16x32_bf16 v[188:191], a[196:199], v[56:59], v[188:191]\n"
    "s_barrier\n"
    "ds_read_b128 a[192:195], v0 offset:24576\n"
    "v_fma_f32 v72, v72, s52, v15\n"
    "v_mfma_f32_16x16x32_bf16 v[204:207], a[200:203], v[56:59], v[204:207]\n"
    "ds_read_b128 a[196:199], v1 offset:24576\n"
    "v_fma_f32 v73, v73, s52, v15\n"
    "v_mfma_f32_16x16x32_bf16 v[200:203], a[200:203], v[52:55], v[200:203]\n"
    "buffer_load_dwordx4 v7, s[36:39], s56 offen offset:1024 nt lds\n"
    "v_mfma_f32_16x16x32_bf16 v[196:199], a[200:203], v[48:51], v[196:199]\n"
    "v_exp_f32 v42, v42\n"
    "v_mfma_f32_16x16x32_bf16 v[192:195], a[200:203], v[44:47], v[192:195]\n"
    "ds_read_b128 a[200:203], v0 offset:24704\n"
    "v_fma_f32 v74, v74, s52, v15\n"
    "v_mfma_f32_16x16x32_bf16 v[208:211], a[204:207], v[44:47], v[208:211]\n"
    "v_exp_f32 v43, v43\n"
    "v_mfma_f32_16x16x32_bf16 v[212:215], a[204:207], v[48:51], v[212:215]\n"
    "v_fma_f32 v75, v75, s52, v15\n"
    "v_cvt_pk_bf16_f32 v36, v36, v37\n"
    "v_mfma_f32_16x16x32_bf16 v[216:219], a[204:207], v[52:55], v[216:219]\n"
    "v_cvt_pk_bf16_f32 v40, v40, v41\n"
    "v_cvt_pk_bf16_f32 v37, v38, v39\n"
    "v_mfma_f32_16x16x32_bf16 v[220:223], a[204:207], v[56:59], v[220:223]\n"
    "ds_read_b128 a[204:207], v1 offset:24704\n"
    "v_cvt_pk_bf16_f32 v41, v42, v43\n"
    "v_mfma_f32_16x16x32_bf16 v[236:239], a[208:211], v[56:59], v[236:239]\n"
    "v_exp_f32 v68, v68\n"
    "v_mfma_f32_16x16x32_bf16 v[232:235], a[208:211], v[52:55], v[232:235]\n"
    "v_exp_f32 v69, v69\n"
    "v_mfma_f32_16x16x32_bf16 v[228:231], a[208:211], v[48:51], v[228:231]\n"
    "v_exp_f32 v70, v70\n"
    "v_mfma_f32_16x16x32_bf16 v[224:227], a[208:211], v[44:47], v[224:227]\n"
    "ds_read_b128 a[208:211], v0 offset:24832\n"
    "v_cvt_pk_bf16_f32 v38, v68, v69\n"
    "v_mfma_f32_16x16x32_bf16 v[240:243], a[212:215], v[44:47], v[240:243]\n"
    "v_exp_f32 v71, v71\n"
    "v_mfma_f32_16x16x32_bf16 v[244:247], a[212:215], v[48:51], v[244:247]\n"
    "v_exp_f32 v72, v72\n"
    "v_mfma_f32_16x16x32_bf16 v[248:251], a[212:215], v[52:55], v[248:251]\n"
    "v_exp_f32 v73, v73\n"
    "v_mfma_f32_16x16x32_bf16 v[252:255], a[212:215], v[56:59], v[252:255]\n"
    "s_waitcnt lgkmcnt(4)\n"
    "buffer_load_dwordx4 v8, s[36:39], s56 offen offset:2048 nt lds\n"
    "v_mfma_f32_16x16x32_bf16 v[44:47], a[192:195], a[0:3], 0\n"
    "s_waitcnt lgkmcnt(3)\n"
    "s_add_u32 m0, s54, 98304\n"
    "ds_read_b128 a[212:215], v1 offset:24832\n"
    "v_mfma_f32_16x16x32_bf16 v[44:47], a[196:199], a[4:7], v[44:47]\n"
    "s_waitcnt vmcnt(13) lgkmcnt(3)\n"
    "v_exp_f32 v74, v74\n"
    "v_mfma_f32_16x16x32_bf16 v[44:47], a[200:203], a[8:11], v[44:47]\n"
    "ds_read_b128 a[96:99], v10 offset:0\n"
    "v_cvt_pk_bf16_f32 v39, v70, v71\n"
    "v_mfma_f32_16x16x32_bf16 v[48:51], a[200:203], a[32:35], 0\n"
    "ds_read_b128 a[100:103], v11 offset:0\n"
    "v_cvt_pk_bf16_f32 v42, v72, v73\n"
    "v_mfma_f32_16x16x32_bf16 v[48:51], a[196:199], a[28:31], v[48:51]\n"
    "v_exp_f32 v75, v75\n"
    "v_mfma_f32_16x16x32_bf16 v[48:51], a[192:195], a[24:27], v[48:51]\n"
    "v_cvt_pk_bf16_f32 v43, v74, v75\n"
    "v_mfma_f32_16x16x32_bf16 v[52:55], a[192:195], a[48:51], 0\n"
    "v_mfma_f32_16x16x32_bf16 v[52:55], a[196:199], a[52:55], v[52:55]\n"
    "v_mfma_f32_16x16x32_bf16 v[52:55], a[200:203], a[56:59], v[52:55]\n"
    "v_mfma_f32_16x16x32_bf16 v[56:59], a[200:203], a[80:83], 0\n"
    "ds_read_b128 a[200:203], v0 offset:30848\n"
    "v_mfma_f32_16x16x32_bf16 v[56:59], a[196:199], a[76:79], v[56:59]\n"
    "ds_read_b128 a[196:199], v1 offset:30720\n"
    "v_mfma_f32_16x16x32_bf16 v[56:59], a[192:195], a[72:75], v[56:59]\n"
    "s_waitcnt lgkmcnt(6)\n"
    "ds_read_b128 a[192:195], v0 offset:30720\n"
    "v_mfma_f32_16x16x32_bf16 v[44:47], a[204:207], a[12:15], v[44:47]\n"
    "s_waitcnt lgkmcnt(6)\n"
    "v_mfma_f32_16x16x32_bf16 v[44:47], a[208:211], a[16:19], v[44:47]\n"
    "s_waitcnt lgkmcnt(5)\n"
    "v_mfma_f32_16x16x32_bf16 v[44:47], a[212:215], a[20:23], v[44:47]\n"
    "buffer_load_dwordx4 v6, s[40:43], s55 offen offset:0 nt lds\n"
    "v_mfma_f32_16x16x32_bf16 v[48:51], a[212:215], a[44:47], v[48:51]\n"
    "v_mfma_f32_16x16x32_bf16 v[48:51], a[208:211], a[40:43], v[48:51]\n"
    "v_mfma_f32_16x16x32_bf16 v[48:51], a[204:207], a[36:39], v[48:51]\n"
    "v_mfma_f32_16x16x32_bf16 v[52:55], a[204:207], a[60:63], v[52:55]\n"
    "v_mfma_f32_16x16x32_bf16 v[52:55], a[208:211], a[64:67], v[52:55]\n"
    "v_mfma_f32_16x16x32_bf16 v[52:55], a[212:215], a[68:71], v[52:55]\n"
    "v_mfma_f32_16x16x32_bf16 v[56:59], a[212:215], a[92:95], v[56:59]\n"
    "ds_read_b128 a[212:215], v1 offset:30976\n"
    "v_fma_f32 v44, v44, s52, v12\n"
    "v_mfma_f32_16x16x32_bf16 v[56:59], a[208:211], a[88:91], v[56:59]\n"
    "ds_read_b128 a[208:211], v0 offset:30976\n"
    "v_fma_f32 v45, v45, s52, v12\n"
    "v_mfma_f32_16x16x32_bf16 v[56:59], a[204:207], a[84:87], v[56:59]\n"
    "s_waitcnt lgkmcnt(2)\n"
    "ds_read_b128 a[204:207], v1 offset:30848\n"
    "v_fma_f32 v46, v46, s52, v12\n"
    "v_mfma_f32_16x16x32_bf16 v[76:79], a[192:195], a[0:3], 0\n"
    "v_exp_f32 v44, v44\n"
    "v_mfma_f32_16x16x32_bf16 v[76:79], a[196:199], a[4:7], v[76:79]\n"
    "v_exp_f32 v45, v45\n"
    "v_mfma_f32_16x16x32_bf16 v[76:79], a[200:203], a[8:11], v[76:79]\n"
    "v_fma_f32 v47, v47, s52, v12\n"
    "v_cvt_pk_bf16_f32 v44, v44, v45\n"
    "v_mfma_f32_16x16x32_bf16 v[80:83], a[200:203], a[32:35], 0\n"
    "v_exp_f32 v46, v46\n"
    "v_mfma_f32_16x16x32_bf16 v[80:83], a[196:199], a[28:31], v[80:83]\n"
    "buffer_load_dwordx4 v7, s[40:43], s55 offen offset:1024 nt lds\n"
    "v_mfma_f32_16x16x32_bf16 v[80:83], a[192:195], a[24:27], v[80:83]\n"
    "v_exp_f32 v47, v47\n"
    "v_mfma_f32_16x16x32_bf16 v[60:63], a[192:195], a[48:51], 0\n"
    "v_cvt_pk_bf16_f32 v45, v46, v47\n"
    "v_fma_f32 v48, v48, s52, v13\n"
    "v_mfma_f32_16x16x32_bf16 v[60:63], a[196:199], a[52:55], v[60:63]\n"
    "v_fma_f32 v49, v49, s52, v13\n"
    "v_fma_f32 v50, v50, s52, v13\n"
    "v_mfma_f32_16x16x32_bf16 v[60:63], a[200:203], a[56:59], v[60:63]\n"
    "v_exp_f32 v48, v48\n"
    "v_mfma_f32_16x16x32_bf16 v[64:67], a[200:203], a[80:83], 0\n"
    "ds_read_b64_tr_b16 a[200:201], v4 offset:12288\n"
    "v_fma_f32 v51, v51, s52, v13\n"
    "v_mfma_f32_16x16x32_bf16 v[64:67], a[196:199], a[76:79], v[64:67]\n"
    "ds_read_b64_tr_b16 a[196:197], v3 offset:12288\n"
    "v_fma_f32 v52, v52, s52, v14\n"
    "v_mfma_f32_16x16x32_bf16 v[64:67], a[192:195], a[72:75], v[64:67]\n"
    "s_waitcnt lgkmcnt(2)\n"
    "ds_read_b64_tr_b16 a[192:193], v2 offset:12288\n"
    "v_fma_f32 v53, v53, s52, v14\n"
    "v_mfma_f32_16x16x32_bf16 v[76:79], a[204:207], a[12:15], v[76:79]\n"
    "ds_read_b64_tr_b16 a[194:195], v2 offset:18432\n"
    "v_fma_f32 v56, v56, s52, v15\n"
    "v_mfma_f32_16x16x32_bf16 v[76:79], a[208:211], a[16:19], v[76:79]\n"
    "ds_read_b64_tr_b16 a[198:199], v3 offset:18432\n"
    "v_fma_f32 v57, v57, s52, v15\n"
    "v_mfma_f32_16x16x32_bf16 v[76:79], a[212:215], a[20:23], v[76:79]\n"
    "ds_read_b64_tr_b16 a[202:203], v4 offset:18432\n"
    "v_fma_f32 v54, v54, s52, v14\n"
    "v_mfma_f32_16x16x32_bf16 v[80:83], a[212:215], a[44:47], v[80:83]\n"
    "v_exp_f32 v49, v49\n"
    "v_mfma_f32_16x16x32_bf16 v[80:83], a[208:211], a[40:43], v[80:83]\n"
    "v_exp_f32 v50, v50\n"
    "v_mfma_f32_16x16x32_bf16 v[80:83], a[204:207], a[36:39], v[80:83]\n"
    "v_exp_f32 v51, v51\n"
    "v_mfma_f32_16x16x32_bf16 v[60:63], a[204:207], a[60:63], v[60:63]\n"
    "buffer_load_dwordx4 v8, s[40:43], s55 offen offset:2048 nt lds\n"
    "v_mfma_f32_16x16x32_bf16 v[60:63], a[208:211], a[64:67], v[60:63]\n"
    "s_add_u32 m0, s57, 6144\n"
    "v_fma_f32 v76, v76, s52, v12\n"
    "v_fma_f32 v77, v77, s52, v12\n"
    "v_mfma_f32_16x16x32_bf16 v[60:63], a[212:215], a[68:71], v[60:63]\n"
    "s_add_u32 s55, s55, 24576\n"
    "buffer_load_dwordx4 v9, s[44:47], s63 offen offset:0 nt lds\n"
    "v_mfma_f32_16x16x32_bf16 v[64:67], a[212:215], a[92:95], v[64:67]\n"
    "s_add_u32 s63, s62, 128\n"
    "buffer_load_dwordx4 v9, s[44:47], s64 offen offset:1024 nt lds\n"
    "v_mfma_f32_16x16x32_bf16 v[64:67], a[208:211], a[88:91], v[64:67]\n"
    "s_add_u32 m0, s54, 0\n"
    "s_add_u32 s64, s62, 2176\n"
    "ds_read_b64_tr_b16 a[208:209], v2 offset:12416\n"
    "v_mfma_f32_16x16x32_bf16 v[64:67], a[204:207], a[84:87], v[64:67]\n"
    "ds_read_b64_tr_b16 a[204:205], v5 offset:12288\n"
    "v_fma_f32 v78, v78, s52, v12\n"
    "v_smfmac_f32_16x16x64_bf16 a[236:239], a[252:255], v[28:35], v23\n"
    "ds_read_b64_tr_b16 a[206:207], v5 offset:18432\n"
    "v_fma_f32 v79, v79, s52, v12\n"
    "v_smfmac_f32_16x16x64_bf16 a[240:243], a[252:255], v[36:43], v23\n"
    "s_waitcnt lgkmcnt(5)\n"
    "ds_read_b64_tr_b16 a[210:211], v2 offset:18560\n"
    "v_fma_f32 v80, v80, s52, v13\n"
    "v_mfma_f32_16x16x32_bf16 v[96:99], a[192:195], v[40:43], v[96:99]\n"
    "ds_read_b64_tr_b16 a[212:213], v3 offset:12416\n"
    "v_fma_f32 v81, v81, s52, v13\n"
    "v_mfma_f32_16x16x32_bf16 v[92:95], a[192:195], v[36:39], v[92:95]\n"
    "ds_read_b64_tr_b16 a[214:215], v3 offset:18560\n"
    "v_fma_f32 v82, v82, s52, v13\n"
    "v_mfma_f32_16x16x32_bf16 v[88:91], a[192:195], v[32:35], v[88:91]\n"
    "v_exp_f32 v76, v76\n"
    "v_mfma_f32_16x16x32_bf16 v[84:87], a[192:195], v[28:31], v[84:87]\n"
    "s_waitcnt lgkmcnt(7)\n"
    "ds_read_b64_tr_b16 a[192:193], v4 offset:12416\n"
    "v_fma_f32 v83, v83, s52, v13\n"
    "v_mfma_f32_16x16x32_bf16 a[216:219], a[196:199], v[28:31], a[216:219]\n"
    "ds_read_b64_tr_b16 a[194:195], v4 offset:18560\n"
    "v_cvt_pk_bf16_f32 v48, v48, v49\n"
    "v_mfma_f32_16x16x32_bf16 a[220:223], a[196:199], v[32:35], a[220:223]\n"
    "v_exp_f32 v77, v77\n"
    "v_mfma_f32_16x16x32_bf16 a[224:227], a[196:199], v[36:39], a[224:227]\n"
    "v_exp_f32 v78, v78\n"
    "v_mfma_f32_16x16x32_bf16 a[228:231], a[196:199], v[40:43], a[228:231]\n"
    "s_waitcnt lgkmcnt(8)\n"
    "buffer_load_dwordx4 v6, s[36:39], s55 offen offset:0 nt lds\n"
    "v_mfma_f32_16x16x32_bf16 v[112:115], a[200:203], v[40:43], v[112:115]\n"
    "ds_read_b64_tr_b16 a[196:197], v5 offset:12416\n"
    "v_cvt_pk_bf16_f32 v46, v76, v77\n"
    "v_mfma_f32_16x16x32_bf16 v[108:111], a[200:203], v[36:39], v[108:111]\n"
    "ds_read_b64_tr_b16 a[198:199], v5 offset:18560\n"
    "v_cvt_pk_bf16_f32 v49, v50, v51\n"
    "v_mfma_f32_16x16x32_bf16 v[104:107], a[200:203], v[32:35], v[104:107]\n"
    "v_exp_f32 v79, v79\n"
    "v_mfma_f32_16x16x32_bf16 v[100:103], a[200:203], v[28:31], v[100:103]\n"
    "s_waitcnt lgkmcnt(7)\n"
    "ds_read_b64_tr_b16 a[200:201], v2 offset:12544\n"
    "v_cvt_pk_bf16_f32 v47, v78, v79\n"
    "v_mfma_f32_16x16x32_bf16 v[116:119], a[204:207], v[28:31], v[116:119]\n"
    "ds_read_b64_tr_b16 a[202:203], v2 offset:18688\n"
    "v_fma_f32 v55, v55, s52, v14\n"
    "v_mfma_f32_16x16x32_bf16 v[120:123], a[204:207], v[32:35], v[120:123]\n"
    "v_exp_f32 v80, v80\n"
    "v_mfma_f32_16x16x32_bf16 v[124:127], a[204:207], v[36:39], v[124:127]\n"
    "v_exp_f32 v81, v81\n"
    "v_mfma_f32_16x16x32_bf16 v[128:131], a[204:207], v[40:43], v[128:131]\n"
    "s_waitcnt lgkmcnt(8)\n"
    "ds_read_b64_tr_b16 a[204:205], v3 offset:12544\n"
    "v_cvt_pk_bf16_f32 v50, v80, v81\n"
    "v_mfma_f32_16x16x32_bf16 v[144:147], a[208:211], v[40:43], v[144:147]\n"
    "ds_read_b64_tr_b16 a[206:207], v3 offset:18688\n"
    "v_fma_f32 v58, v58, s52, v15\n"
    "v_mfma_f32_16x16x32_bf16 v[140:143], a[208:211], v[36:39], v[140:143]\n"
    "v_exp_f32 v82, v82\n"
    "v_mfma_f32_16x16x32_bf16 v[136:139], a[208:211], v[32:35], v[136:139]\n"
    "v_exp_f32 v83, v83\n"
    "v_mfma_f32_16x16x32_bf16 v[132:135], a[208:211], v[28:31], v[132:135]\n"
    "s_waitcnt lgkmcnt(8)\n"
    "ds_read_b64_tr_b16 a[208:209], v4 offset:12544\n"
    "v_cvt_pk_bf16_f32 v51, v82, v83\n"
    "v_mfma_f32_16x16x32_bf16 a[232:235], a[212:215], v[28:31], a[232:235]\n"
    "ds_read_b64_tr_b16 a[210:211], v4 offset:18688\n"
    "v_fma_f32 v59, v59, s52, v15\n"
    "v_mfma_f32_16x16x32_bf16 v[148:151], a[212:215], v[32:35], v[148:151]\n"
    "buffer_load_dwordx4 v7, s[36:39], s55 offen offset:1024 nt lds\n"
    "v_mfma_f32_16x16x32_bf16 v[152:155], a[212:215], v[36:39], v[152:155]\n"
    "v_exp_f32 v52, v52\n"
    "v_mfma_f32_16x16x32_bf16 v[156:159], a[212:215], v[40:43], v[156:159]\n"
    "s_waitcnt lgkmcnt(8)\n"
    "ds_read_b64_tr_b16 a[212:213], v5 offset:12544\n"
    "v_fma_f32 v60, v60, s52, v14\n"
    "v_mfma_f32_16x16x32_bf16 v[172:175], a[192:195], v[40:43], v[172:175]\n"
    "ds_read_b64_tr_b16 a[214:215], v5 offset:18688\n"
    "v_fma_f32 v61, v61, s52, v14\n"
    "v_mfma_f32_16x16x32_bf16 v[168:171], a[192:195], v[36:39], v[168:171]\n"
    "v_exp_f32 v53, v53\n"
    "v_mfma_f32_16x16x32_bf16 v[164:167], a[192:195], v[32:35], v[164:167]\n"
    "v_exp_f32 v56, v56\n"
    "v_mfma_f32_16x16x32_bf16 v[160:163], a[192:195], v[28:31], v[160:163]\n"
    "s_waitcnt lgkmcnt(8)\n"
    "v_exp_f32 v57, v57\n"
    "v_mfma_f32_16x16x32_bf16 v[176:179], a[196:199], v[28:31], v[176:179]\n"
    "v_exp_f32 v54, v54\n"
    "v_mfma_f32_16x16x32_bf16 v[180:183], a[196:199], v[32:35], v[180:183]\n"
    "s_waitcnt vmcnt(18)\n"
    "v_exp_f32 v55, v55\n"
    "v_mfma_f32_16x16x32_bf16 v[184:187], a[196:199], v[36:39], v[184:187]\n"
    "s_waitcnt vmcnt(12) lgkmcnt(0)\n"
    "ds_read_b128 a[120:123], v10 offset:2048\n"
    "v_fma_f32 v62, v62, s52, v14\n"
    "v_mfma_f32_16x16x32_bf16 v[188:191], a[196:199], v[40:43], v[188:191]\n"
    "s_barrier\n"
    "ds_read_b128 a[192:195], v0 offset:36864\n"
    "v_fma_f32 v63, v63, s52, v14\n"
    "v_mfma_f32_16x16x32_bf16 v[204:207], a[200:203], v[40:43], v[204:207]\n"
    "ds_read_b128 a[196:199], v1 offset:36864\n"
    "v_fma_f32 v64, v64, s52, v15\n"
    "v_mfma_f32_16x16x32_bf16 v[200:203], a[200:203], v[36:39], v[200:203]\n"
    "ds_read_b128 a[124:127], v11 offset:2048\n"
    "v_fma_f32 v65, v65, s52, v15\n"
    "v_mfma_f32_16x16x32_bf16 v[196:199], a[200:203], v[32:35], v[196:199]\n"
    "v_exp_f32 v58, v58\n"
    "v_mfma_f32_16x16x32_bf16 v[192:195], a[200:203], v[28:31], v[192:195]\n"
    "buffer_load_dwordx4 v8, s[36:39], s55 offen offset:2048 nt lds\n"
    "v_mfma_f32_16x16x32_bf16 v[208:211], a[204:207], v[28:31], v[208:211]\n"
    "s_add_u32 m0, s54, 110592\n"
    "ds_read_b128 a[200:203], v0 offset:36992\n"
    "v_fma_f32 v66, v66, s52, v15\n"
    "v_mfma_f32_16x16x32_bf16 v[212:215], a[204:207], v[32:35], v[212:215]\n"
    "v_exp_f32 v59, v59\n"
    "v_mfma_f32_16x16x32_bf16 v[216:219], a[204:207], v[36:39], v[216:219]\n"
    "v_fma_f32 v67, v67, s52, v15\n"
    "v_cvt_pk_bf16_f32 v52, v52, v53\n"
    "v_mfma_f32_16x16x32_bf16 v[220:223], a[204:207], v[40:43], v[220:223]\n"
    "ds_read_b128 a[204:207], v1 offset:36992\n"
    "v_cvt_pk_bf16_f32 v56, v56, v57\n"
    "v_mfma_f32_16x16x32_bf16 v[236:239], a[208:211], v[40:43], v[236:239]\n"
    "v_exp_f32 v60, v60\n"
    "v_mfma_f32_16x16x32_bf16 v[232:235], a[208:211], v[36:39], v[232:235]\n"
    "v_exp_f32 v61, v61\n"
    "v_mfma_f32_16x16x32_bf16 v[228:231], a[208:211], v[32:35], v[228:231]\n"
    "v_exp_f32 v62, v62\n"
    "v_mfma_f32_16x16x32_bf16 v[224:227], a[208:211], v[28:31], v[224:227]\n"
    "ds_read_b128 a[208:211], v0 offset:37120\n"
    "v_cvt_pk_bf16_f32 v53, v54, v55\n"
    "v_mfma_f32_16x16x32_bf16 v[240:243], a[212:215], v[28:31], v[240:243]\n"
    "v_exp_f32 v63, v63\n"
    "v_mfma_f32_16x16x32_bf16 v[244:247], a[212:215], v[32:35], v[244:247]\n"
    "v_exp_f32 v64, v64\n"
    "v_mfma_f32_16x16x32_bf16 v[248:251], a[212:215], v[36:39], v[248:251]\n"
    "v_exp_f32 v65, v65\n"
    "v_mfma_f32_16x16x32_bf16 v[252:255], a[212:215], v[40:43], v[252:255]\n"
    "s_waitcnt lgkmcnt(5)\n"
    "ds_read_b128 a[212:215], v1 offset:37120\n"
    "v_cvt_pk_bf16_f32 v57, v58, v59\n"
    "v_mfma_f32_16x16x32_bf16 v[28:31], a[192:195], a[0:3], 0\n"
    "s_waitcnt lgkmcnt(5)\n"
    "v_exp_f32 v66, v66\n"
    "v_mfma_f32_16x16x32_bf16 v[28:31], a[196:199], a[4:7], v[28:31]\n"
    "s_waitcnt lgkmcnt(3)\n"
    "v_exp_f32 v67, v67\n"
    "v_mfma_f32_16x16x32_bf16 v[28:31], a[200:203], a[8:11], v[28:31]\n"
    "buffer_load_dwordx4 v6, s[40:43], s56 offen offset:0 nt lds\n"
    "v_mfma_f32_16x16x32_bf16 v[32:35], a[200:203], a[32:35], 0\n"
    "v_cvt_pk_bf16_f32 v54, v60, v61\n"
    "v_cvt_pk_bf16_f32 v55, v62, v63\n"
    "v_mfma_f32_16x16x32_bf16 v[32:35], a[196:199], a[28:31], v[32:35]\n"
    "v_cvt_pk_bf16_f32 v58, v64, v65\n"
    "v_cvt_pk_bf16_f32 v59, v66, v67\n"
    "v_mfma_f32_16x16x32_bf16 v[32:35], a[192:195], a[24:27], v[32:35]\n"
    "v_mfma_f32_16x16x32_bf16 v[36:39], a[192:195], a[48:51], 0\n"
    "v_mfma_f32_16x16x32_bf16 v[36:39], a[196:199], a[52:55], v[36:39]\n"
    "v_mfma_f32_16x16x32_bf16 v[36:39], a[200:203], a[56:59], v[36:39]\n"
    "v_mfma_f32_16x16x32_bf16 v[40:43], a[200:203], a[80:83], 0\n"
    "ds_read_b128 a[200:203], v0 offset:43136\n"
    "v_mfma_f32_16x16x32_bf16 v[40:43], a[196:199], a[76:79], v[40:43]\n"
    "ds_read_b128 a[196:199], v1 offset:43008\n"
    "v_mfma_f32_16x16x32_bf16 v[40:43], a[192:195], a[72:75], v[40:43]\n"
    "s_waitcnt lgkmcnt(4)\n"
    "ds_read_b128 a[192:195], v0 offset:43008\n"
    "v_mfma_f32_16x16x32_bf16 v[28:31], a[204:207], a[12:15], v[28:31]\n"
    "s_waitcnt lgkmcnt(4)\n"
    "v_mfma_f32_16x16x32_bf16 v[28:31], a[208:211], a[16:19], v[28:31]\n"
    "s_waitcnt lgkmcnt(3)\n"
    "v_mfma_f32_16x16x32_bf16 v[28:31], a[212:215], a[20:23], v[28:31]\n"
    "v_mfma_f32_16x16x32_bf16 v[32:35], a[212:215], a[44:47], v[32:35]\n"
    "v_mfma_f32_16x16x32_bf16 v[32:35], a[208:211], a[40:43], v[32:35]\n"
    "buffer_load_dwordx4 v7, s[40:43], s56 offen offset:1024 nt lds\n"
    "v_mfma_f32_16x16x32_bf16 v[32:35], a[204:207], a[36:39], v[32:35]\n"
    "v_mfma_f32_16x16x32_bf16 v[36:39], a[204:207], a[60:63], v[36:39]\n"
    "v_mfma_f32_16x16x32_bf16 v[36:39], a[208:211], a[64:67], v[36:39]\n"
    "v_mfma_f32_16x16x32_bf16 v[36:39], a[212:215], a[68:71], v[36:39]\n"
    "v_mfma_f32_16x16x32_bf16 v[40:43], a[212:215], a[92:95], v[40:43]\n"
    "ds_read_b128 a[212:215], v1 offset:43264\n"
    "v_fma_f32 v28, v28, s52, v12\n"
    "v_mfma_f32_16x16x32_bf16 v[40:43], a[208:211], a[88:91], v[40:43]\n"
    "ds_read_b128 a[208:211], v0 offset:43264\n"
    "v_fma_f32 v29, v29, s52, v12\n"
    "v_mfma_f32_16x16x32_bf16 v[40:43], a[204:207], a[84:87], v[40:43]\n"
    "s_waitcnt lgkmcnt(2)\n"
    "ds_read_b128 a[204:207], v1 offset:43136\n"
    "v_fma_f32 v30, v30, s52, v12\n"
    "v_mfma_f32_16x16x32_bf16 v[68:71], a[192:195], a[0:3], 0\n"
    "v_exp_f32 v28, v28\n"
    "v_mfma_f32_16x16x32_bf16 v[68:71], a[196:199], a[4:7], v[68:71]\n"
    "v_exp_f32 v29, v29\n"
    "v_mfma_f32_16x16x32_bf16 v[68:71], a[200:203], a[8:11], v[68:71]\n"
    "v_fma_f32 v31, v31, s52, v12\n"
    "v_cvt_pk_bf16_f32 v28, v28, v29\n"
    "v_mfma_f32_16x16x32_bf16 v[72:75], a[200:203], a[32:35], 0\n"
    "v_exp_f32 v30, v30\n"
    "v_mfma_f32_16x16x32_bf16 v[72:75], a[196:199], a[28:31], v[72:75]\n"
    "v_exp_f32 v31, v31\n"
    "v_mfma_f32_16x16x32_bf16 v[72:75], a[192:195], a[24:27], v[72:75]\n"
    "v_cvt_pk_bf16_f32 v29, v30, v31\n"
    "v_fma_f32 v32, v32, s52, v13\n"
    "v_mfma_f32_16x16x32_bf16 v[76:79], a[192:195], a[48:51], 0\n"
    "buffer_load_dwordx4 v8, s[40:43], s56 offen offset:2048 nt lds\n"
    "v_mfma_f32_16x16x32_bf16 v[76:79], a[196:199], a[52:55], v[76:79]\n"
    "s_add_u32 m0, s57, 8192\n"
    "v_fma_f32 v33, v33, s52, v13\n"
    "v_fma_f32 v34, v34, s52, v13\n"
    "v_mfma_f32_16x16x32_bf16 v[76:79], a[200:203], a[56:59], v[76:79]\n"
    "s_add_u32 s56, s56, 24576\n"
    "buffer_load_dwordx4 v9, s[44:47], s63 offen offset:0 nt lds\n"
    "v_mfma_f32_16x16x32_bf16 v[80:83], a[200:203], a[80:83], 0\n"
    "s_add_u32 s63, s62, 6272\n"
    "buffer_load_dwordx4 v9, s[44:47], s64 offen offset:1024 nt lds\n"
    "v_mfma_f32_16x16x32_bf16 v[80:83], a[196:199], a[76:79], v[80:83]\n"
    "s_add_u32 m0, s54, 12288\n"
    "s_add_u32 s64, s62, 8320\n"
    "ds_read_b64_tr_b16 a[196:197], v3 offset:24576\n"
    "v_mfma_f32_16x16x32_bf16 v[80:83], a[192:195], a[72:75], v[80:83]\n"
    "s_waitcnt lgkmcnt(1)\n"
    "ds_read_b64_tr_b16 a[192:193], v2 offset:24576\n"
    "v_fma_f32 v35, v35, s52, v13\n"
    "v_mfma_f32_16x16x32_bf16 v[68:71], a[204:207], a[12:15], v[68:71]\n"
    "ds_read_b64_tr_b16 a[194:195], v2 offset:30720\n"
    "v_fma_f32 v36, v36, s52, v14\n"
    "v_mfma_f32_16x16x32_bf16 v[68:71], a[208:211], a[16:19], v[68:71]\n"
    "ds_read_b64_tr_b16 a[198:199], v3 offset:30720\n"
    "v_fma_f32 v37, v37, s52, v14\n"
    "v_mfma_f32_16x16x32_bf16 v[68:71], a[212:215], a[20:23], v[68:71]\n"
    "ds_read_b64_tr_b16 a[200:201], v4 offset:24576\n"
    "v_fma_f32 v40, v40, s52, v15\n"
    "v_mfma_f32_16x16x32_bf16 v[72:75], a[212:215], a[44:47], v[72:75]\n"
    "ds_read_b64_tr_b16 a[202:203], v4 offset:30720\n"
    "v_fma_f32 v41, v41, s52, v15\n"
    "v_mfma_f32_16x16x32_bf16 v[72:75], a[208:211], a[40:43], v[72:75]\n"
    "v_exp_f32 v32, v32\n"
    "v_mfma_f32_16x16x32_bf16 v[72:75], a[204:207], a[36:39], v[72:75]\n"
    "v_fma_f32 v68, v68, s52, v12\n"
    "v_fma_f32 v69, v69, s52, v12\n"
    "v_mfma_f32_16x16x32_bf16 v[76:79], a[204:207], a[60:63], v[76:79]\n"
    "v_fma_f32 v70, v70, s52, v12\n"
    "v_fma_f32 v71, v71, s52, v12\n"
    "v_mfma_f32_16x16x32_bf16 v[76:79], a[208:211], a[64:67], v[76:79]\n"
    "v_exp_f32 v68, v68\n"
    "v_mfma_f32_16x16x32_bf16 v[76:79], a[212:215], a[68:71], v[76:79]\n"
    "v_exp_f32 v69, v69\n"
    "v_mfma_f32_16x16x32_bf16 v[80:83], a[212:215], a[92:95], v[80:83]\n"
    "buffer_load_dwordx4 v6, s[36:39], s56 offen offset:0 nt lds\n"
    "v_mfma_f32_16x16x32_bf16 v[80:83], a[208:211], a[88:91], v[80:83]\n"
    "ds_read_b64_tr_b16 a[208:209], v2 offset:24704\n"
    "v_cvt_pk_bf16_f32 v30, v68, v69\n"
    "v_mfma_f32_16x16x32_bf16 v[80:83], a[204:207], a[84:87], v[80:83]\n"
    "ds_read_b64_tr_b16 a[204:205], v5 offset:24576\n"
    "v_fma_f32 v72, v72, s52, v13\n"
    "v_smfmac_f32_16x16x64_bf16 a[236:239], a[252:255], v[44:51], v23\n"
    "ds_read_b64_tr_b16 a[206:207], v5 offset:30720\n"
    "v_fma_f32 v73, v73, s52, v13\n"
    "v_smfmac_f32_16x16x64_bf16 a[240:243], a[252:255], v[52:59], v23\n"
    "s_waitcnt lgkmcnt(6)\n"
    "ds_read_b64_tr_b16 a[210:211], v2 offset:30848\n"
    "v_fma_f32 v74, v74, s52, v13\n"
    "v_mfma_f32_16x16x32_bf16 v[96:99], a[192:195], v[56:59], v[96:99]\n"
    "ds_read_b64_tr_b16 a[212:213], v3 offset:24704\n"
    "v_fma_f32 v75, v75, s52, v13\n"
    "v_mfma_f32_16x16x32_bf16 v[92:95], a[192:195], v[52:55], v[92:95]\n"
    "ds_read_b64_tr_b16 a[214:215], v3 offset:30848\n"
    "v_fma_f32 v38, v38, s52, v14\n"
    "v_mfma_f32_16x16x32_bf16 v[88:91], a[192:195], v[48:51], v[88:91]\n"
    "v_exp_f32 v70, v70\n"
    "v_mfma_f32_16x16x32_bf16 v[84:87], a[192:195], v[44:47], v[84:87]\n"
    "s_waitcnt lgkmcnt(8)\n"
    "ds_read_b64_tr_b16 a[192:193], v4 offset:24704\n"
    "v_fma_f32 v39, v39, s52, v14\n"
    "v_mfma_f32_16x16x32_bf16 a[216:219], a[196:199], v[44:47], a[216:219]\n"
    "ds_read_b64_tr_b16 a[194:195], v4 offset:30848\n"
    "v_fma_f32 v42, v42, s52, v15\n"
    "v_mfma_f32_16x16x32_bf16 a[220:223], a[196:199], v[48:51], a[220:223]\n"
    "v_exp_f32 v71, v71\n"
    "v_mfma_f32_16x16x32_bf16 a[224:227], a[196:199], v[52:55], a[224:227]\n"
    "v_cvt_pk_bf16_f32 v31, v70, v71\n"
    "v_fma_f32 v43, v43, s52, v15\n"
    "v_mfma_f32_16x16x32_bf16 a[228:231], a[196:199], v[56:59], a[228:231]\n"
    "s_waitcnt lgkmcnt(8)\n"
    "ds_read_b64_tr_b16 a[196:197], v5 offset:24704\n"
    "v_fma_f32 v76, v76, s52, v14\n"
    "v_mfma_f32_16x16x32_bf16 v[112:115], a[200:203], v[56:59], v[112:115]\n"
    "ds_read_b64_tr_b16 a[198:199], v5 offset:30848\n"
    "v_fma_f32 v77, v77, s52, v14\n"
    "v_mfma_f32_16x16x32_bf16 v[108:111], a[200:203], v[52:55], v[108:111]\n"
    "s_waitcnt vmcnt(17)\n"
    "buffer_load_dwordx4 v7, s[36:39], s56 offen offset:1024 nt lds\n"
    "v_mfma_f32_16x16x32_bf16 v[104:107], a[200:203], v[48:51], v[104:107]\n"
    "ds_read_b128 a[144:147], v10 offset:4096\n"
    "v_fma_f32 v78, v78, s52, v14\n"
    "v_mfma_f32_16x16x32_bf16 v[100:103], a[200:203], v[44:47], v[100:103]\n"
    "s_waitcnt lgkmcnt(8)\n"
    "ds_read_b64_tr_b16 a[200:201], v2 offset:24832\n"
    "v_fma_f32 v79, v79, s52, v14\n"
    "v_mfma_f32_16x16x32_bf16 v[116:119], a[204:207], v[44:47], v[116:119]\n"
    "ds_read_b64_tr_b16 a[202:203], v2 offset:30976\n"
    "v_fma_f32 v80, v80, s52, v15\n"
    "v_mfma_f32_16x16x32_bf16 v[120:123], a[204:207], v[48:51], v[120:123]\n"
    "ds_read_b128 a[148:151], v11 offset:4096\n"
    "v_fma_f32 v81, v81, s52, v15\n"
    "v_mfma_f32_16x16x32_bf16 v[124:127], a[204:207], v[52:55], v[124:127]\n"
    "v_exp_f32 v33, v33\n"
    "v_mfma_f32_16x16x32_bf16 v[128:131], a[204:207], v[56:59], v[128:131]\n"
    "s_waitcnt lgkmcnt(10)\n"
    "ds_read_b64_tr_b16 a[204:205], v3 offset:24832\n"
    "v_cvt_pk_bf16_f32 v32, v32, v33\n"
    "v_mfma_f32_16x16x32_bf16 v[144:147], a[208:211], v[56:59], v[144:147]\n"
    "ds_read_b64_tr_b16 a[206:207], v3 offset:30976\n"
    "v_fma_f32 v82, v82, s52, v15\n"
    "v_mfma_f32_16x16x32_bf16 v[140:143], a[208:211], v[52:55], v[140:143]\n"
    "v_exp_f32 v34, v34\n"
    "v_mfma_f32_16x16x32_bf16 v[136:139], a[208:211], v[48:51], v[136:139]\n"
    "v_exp_f32 v35, v35\n"
    "v_mfma_f32_16x16x32_bf16 v[132:135], a[208:211], v[44:47], v[132:135]\n"
    "s_waitcnt lgkmcnt(10)\n"
    "ds_read_b64_tr_b16 a[208:209], v4 offset:24832\n"
    "v_cvt_pk_bf16_f32 v33, v34, v35\n"
    "v_mfma_f32_16x16x32_bf16 a[232:235], a[212:215], v[44:47], a[232:235]\n"
    "ds_read_b64_tr_b16 a[210:211], v4 offset:30976\n"
    "v_fma_f32 v83, v83, s52, v15\n"
    "v_mfma_f32_16x16x32_bf16 v[148:151], a[212:215], v[48:51], v[148:151]\n"
    "v_exp_f32 v72, v72\n"
    "v_mfma_f32_16x16x32_bf16 v[152:155], a[212:215], v[52:55], v[152:155]\n"
    "v_exp_f32 v73, v73\n"
    "v_mfma_f32_16x16x32_bf16 v[156:159], a[212:215], v[56:59], v[156:159]\n"
    "s_waitcnt lgkmcnt(10)\n"
    "buffer_load_dwordx4 v8, s[36:39], s56 offen offset:2048 nt lds\n"
    "v_mfma_f32_16x16x32_bf16 v[172:175], a[192:195], v[56:59], v[172:175]\n"
    "s_add_u32 m0, s54, 61440\n"
    "ds_read_b64_tr_b16 a[212:213], v5 offset:24832\n"
    "v_cvt_pk_bf16_f32 v34, v72, v73\n"
    "v_mfma_f32_16x16x32_bf16 v[168:171], a[192:195], v[52:55], v[168:171]\n"
    "v_exp_f32 v74, v74\n"
    "v_mfma_f32_16x16x32_bf16 v[164:167], a[192:195], v[48:51], v[164:167]\n"
    "v_exp_f32 v75, v75\n"
    "v_mfma_f32_16x16x32_bf16 v[160:163], a[192:195], v[44:47], v[160:163]\n"
    "s_waitcnt lgkmcnt(9)\n"
    "ds_read_b64_tr_b16 a[214:215], v5 offset:30976\n"
    "v_cvt_pk_bf16_f32 v35, v74, v75\n"
    "v_mfma_f32_16x16x32_bf16 v[176:179], a[196:199], v[44:47], v[176:179]\n"
    "v_exp_f32 v36, v36\n"
    "v_mfma_f32_16x16x32_bf16 v[180:183], a[196:199], v[48:51], v[180:183]\n"
    "v_exp_f32 v37, v37\n"
    "v_mfma_f32_16x16x32_bf16 v[184:187], a[196:199], v[52:55], v[184:187]\n"
    "v_exp_f32 v40, v40\n"
    "v_mfma_f32_16x16x32_bf16 v[188:191], a[196:199], v[56:59], v[188:191]\n"
    "s_waitcnt lgkmcnt(7)\n"
    "v_exp_f32 v41, v41\n"
    "v_mfma_f32_16x16x32_bf16 v[204:207], a[200:203], v[56:59], v[204:207]\n"
    "v_exp_f32 v38, v38\n"
    "v_mfma_f32_16x16x32_bf16 v[200:203], a[200:203], v[52:55], v[200:203]\n"
    "s_waitcnt vmcnt(13) lgkmcnt(0)\n"
    "v_exp_f32 v39, v39\n"
    "v_mfma_f32_16x16x32_bf16 v[196:199], a[200:203], v[48:51], v[196:199]\n"
    "s_barrier\n"
    "ds_read_b128 a[192:195], v0 offset:49152\n"
    "v_cvt_pk_bf16_f32 v36, v36, v37\n"
    "v_mfma_f32_16x16x32_bf16 v[192:195], a[200:203], v[44:47], v[192:195]\n"
    "ds_read_b128 a[196:199], v1 offset:49152\n"
    "v_cvt_pk_bf16_f32 v40, v40, v41\n"
    "v_mfma_f32_16x16x32_bf16 v[208:211], a[204:207], v[44:47], v[208:211]\n"
    "ds_read_b128 a[200:203], v0 offset:49280\n"
    "v_cvt_pk_bf16_f32 v37, v38, v39\n"
    "v_mfma_f32_16x16x32_bf16 v[212:215], a[204:207], v[48:51], v[212:215]\n"
    "v_exp_f32 v42, v42\n"
    "v_mfma_f32_16x16x32_bf16 v[216:219], a[204:207], v[52:55], v[216:219]\n"
    "buffer_load_dwordx4 v6, s[40:43], s55 offen offset:0 nt lds\n"
    "v_mfma_f32_16x16x32_bf16 v[220:223], a[204:207], v[56:59], v[220:223]\n"
    "v_exp_f32 v43, v43\n"
    "v_mfma_f32_16x16x32_bf16 v[236:239], a[208:211], v[56:59], v[236:239]\n"
    "ds_read_b128 a[204:207], v1 offset:49280\n"
    "v_cvt_pk_bf16_f32 v41, v42, v43\n"
    "v_mfma_f32_16x16x32_bf16 v[232:235], a[208:211], v[52:55], v[232:235]\n"
    "v_exp_f32 v76, v76\n"
    "v_mfma_f32_16x16x32_bf16 v[228:231], a[208:211], v[48:51], v[228:231]\n"
    "v_exp_f32 v77, v77\n"
    "v_mfma_f32_16x16x32_bf16 v[224:227], a[208:211], v[44:47], v[224:227]\n"
    "ds_read_b128 a[208:211], v0 offset:49408\n"
    "v_cvt_pk_bf16_f32 v38, v76, v77\n"
    "v_mfma_f32_16x16x32_bf16 v[240:243], a[212:215], v[44:47], v[240:243]\n"
    "v_exp_f32 v78, v78\n"
    "v_mfma_f32_16x16x32_bf16 v[244:247], a[212:215], v[48:51], v[244:247]\n"
    "v_exp_f32 v79, v79\n"
    "v_mfma_f32_16x16x32_bf16 v[248:251], a[212:215], v[52:55], v[248:251]\n"
    "v_exp_f32 v80, v80\n"
    "v_mfma_f32_16x16x32_bf16 v[252:255], a[212:215], v[56:59], v[252:255]\n"
    "s_waitcnt lgkmcnt(4)\n"
    "ds_read_b128 a[212:215], v1 offset:49408\n"
    "v_cvt_pk_bf16_f32 v39, v78, v79\n"
    "v_mfma_f32_16x16x32_bf16 v[44:47], a[192:195], a[0:3], 0\n"
    "s_waitcnt lgkmcnt(4)\n"
    "v_exp_f32 v81, v81\n"
    "v_mfma_f32_16x16x32_bf16 v[44:47], a[196:199], a[4:7], v[44:47]\n"
    "s_waitcnt lgkmcnt(3)\n"
    "v_exp_f32 v82, v82\n"
    "v_mfma_f32_16x16x32_bf16 v[44:47], a[200:203], a[8:11], v[44:47]\n"
    "v_exp_f32 v83, v83\n"
    "v_mfma_f32_16x16x32_bf16 v[48:51], a[200:203], a[32:35], 0\n"
    "v_cvt_pk_bf16_f32 v42, v80, v81\n"
    "v_cvt_pk_bf16_f32 v43, v82, v83\n"
    "v_mfma_f32_16x16x32_bf16 v[48:51], a[196:199], a[28:31], v[48:51]\n"
    "buffer_load_dwordx4 v7, s[40:43], s55 offen offset:1024 nt lds\n"
    "v_mfma_f32_16x16x32_bf16 v[48:51], a[192:195], a[24:27], v[48:51]\n"
    "v_mfma_f32_16x16x32_bf16 v[52:55], a[192:195], a[48:51], 0\n"
    "v_mfma_f32_16x16x32_bf16 v[52:55], a[196:199], a[52:55], v[52:55]\n"
    "v_mfma_f32_16x16x32_bf16 v[52:55], a[200:203], a[56:59], v[52:55]\n"
    "v_mfma_f32_16x16x32_bf16 v[56:59], a[200:203], a[80:83], 0\n"
    "ds_read_b128 a[200:203], v0 offset:55424\n"
    "v_mfma_f32_16x16x32_bf16 v[56:59], a[196:199], a[76:79], v[56:59]\n"
    "ds_read_b128 a[196:199], v1 offset:55296\n"
    "v_mfma_f32_16x16x32_bf16 v[56:59], a[192:195], a[72:75], v[56:59]\n"
    "s_waitcnt lgkmcnt(4)\n"
    "ds_read_b128 a[192:195], v0 offset:55296\n"
    "v_mfma_f32_16x16x32_bf16 v[44:47], a[204:207], a[12:15], v[44:47]\n"
    "s_waitcnt lgkmcnt(4)\n"
    "v_mfma_f32_16x16x32_bf16 v[44:47], a[208:211], a[16:19], v[44:47]\n"
    "s_waitcnt lgkmcnt(3)\n"
    "v_mfma_f32_16x16x32_bf16 v[44:47], a[212:215], a[20:23], v[44:47]\n"
    "v_mfma_f32_16x16x32_bf16 v[48:51], a[212:215], a[44:47], v[48:51]\n"
    "v_mfma_f32_16x16x32_bf16 v[48:51], a[208:211], a[40:43], v[48:51]\n"
    "v_mfma_f32_16x16x32_bf16 v[48:51], a[204:207], a[36:39], v[48:51]\n"
    "v_mfma_f32_16x16x32_bf16 v[52:55], a[204:207], a[60:63], v[52:55]\n"
    "buffer_load_dwordx4 v8, s[40:43], s55 offen offset:2048 nt lds\n"
    "v_mfma_f32_16x16x32_bf16 v[52:55], a[208:211], a[64:67], v[52:55]\n"
    "s_add_u32 m0, s57, 0\n"
    "v_mfma_f32_16x16x32_bf16 v[52:55], a[212:215], a[68:71], v[52:55]\n"
    "s_add_u32 s55, s55, 24576\n"
    "buffer_load_dwordx4 v9, s[44:47], s63 offen offset:0 nt lds\n"
    "v_mfma_f32_16x16x32_bf16 v[56:59], a[212:215], a[92:95], v[56:59]\n"
    "s_add_u32 s63, s62, 12416\n"
    "buffer_load_dwordx4 v9, s[44:47], s64 offen offset:1024 nt lds\n"
    "v_mfma_f32_16x16x32_bf16 v[56:59], a[208:211], a[88:91], v[56:59]\n"
    "s_add_u32 m0, s57, 2048\n"
    "s_add_u32 s64, s62, 14464\n"
    "ds_read_b128 a[208:211], v0 offset:55552\n"
    "v_mfma_f32_16x16x32_bf16 v[56:59], a[204:207], a[84:87], v[56:59]\n"
    "s_waitcnt lgkmcnt(1)\n"
    "buffer_load_dwordx4 v9, s[44:47], s63 offen offset:0 nt lds\n"
    "v_mfma_f32_16x16x32_bf16 v[60:63], a[192:195], a[0:3], 0\n"
    "s_add_u32 s63, s62, 18560\n"
    "buffer_load_dwordx4 v9, s[44:47], s64 offen offset:1024 nt lds\n"
    "v_mfma_f32_16x16x32_bf16 v[60:63], a[196:199], a[4:7], v[60:63]\n"
    "s_add_u32 m0, s54, 24576\n"
    "ds_read_b128 a[204:207], v1 offset:55424\n"
    "v_fma_f32 v44, v44, s52, v12\n"
    "v_mfma_f32_16x16x32_bf16 v[60:63], a[200:203], a[8:11], v[60:63]\n"
    "s_add_u32 s64, s62, 20608\n"
    "ds_read_b128 a[212:215], v1 offset:55552\n"
    "v_fma_f32 v45, v45, s52, v12\n"
    "v_mfma_f32_16x16x32_bf16 v[64:67], a[200:203], a[32:35], 0\n"
    "v_exp_f32 v44, v44\n"
    "v_mfma_f32_16x16x32_bf16 v[64:67], a[196:199], a[28:31], v[64:67]\n"
    "v_exp_f32 v45, v45\n"
    "v_mfma_f32_16x16x32_bf16 v[64:67], a[192:195], a[24:27], v[64:67]\n"
    "v_fma_f32 v46, v46, s52, v12\n"
    "v_fma_f32 v47, v47, s52, v12\n"
    "v_mfma_f32_16x16x32_bf16 v[68:71], a[192:195], a[48:51], 0\n"
    "v_exp_f32 v46, v46\n"
    "v_mfma_f32_16x16x32_bf16 v[68:71], a[196:199], a[52:55], v[68:71]\n"
    "v_exp_f32 v47, v47\n"
    "v_mfma_f32_16x16x32_bf16 v[68:71], a[200:203], a[56:59], v[68:71]\n"
    "v_cvt_pk_bf16_f32 v44, v44, v45\n"
    "v_fma_f32 v48, v48, s52, v13\n"
    "v_mfma_f32_16x16x32_bf16 v[72:75], a[200:203], a[80:83], 0\n"
    "buffer_load_dwordx4 v6, s[36:39], s55 offen offset:0 nt lds\n"
    "v_mfma_f32_16x16x32_bf16 v[72:75], a[196:199], a[76:79], v[72:75]\n"
    "ds_read_b64_tr_b16 a[196:197], v3 offset:36864\n"
    "v_cvt_pk_bf16_f32 v45, v46, v47\n"
    "v_mfma_f32_16x16x32_bf16 v[72:75], a[192:195], a[72:75], v[72:75]\n"
    "s_waitcnt lgkmcnt(2)\n"
    "ds_read_b64_tr_b16 a[192:193], v2 offset:36864\n"
    "v_fma_f32 v49, v49, s52, v13\n"
    "v_mfma_f32_16x16x32_bf16 v[60:63], a[204:207], a[12:15], v[60:63]\n"
    "ds_read_b64_tr_b16 a[194:195], v2 offset:43008\n"
    "v_fma_f32 v50, v50, s52, v13\n"
    "v_mfma_f32_16x16x32_bf16 v[60:63], a[208:211], a[16:19], v[60:63]\n"
    "s_waitcnt lgkmcnt(3)\n"
    "ds_read_b64_tr_b16 a[198:199], v3 offset:43008\n"
    "v_fma_f32 v51, v51, s52, v13\n"
    "v_mfma_f32_16x16x32_bf16 v[60:63], a[212:215], a[20:23], v[60:63]\n"
    "ds_read_b64_tr_b16 a[200:201], v4 offset:36864\n"
    "v_fma_f32 v52, v52, s52, v14\n"
    "v_mfma_f32_16x16x32_bf16 v[64:67], a[212:215], a[44:47], v[64:67]\n"
    "s_waitcnt vmcnt(19)\n"
    "ds_read_b64_tr_b16 a[202:203], v4 offset:43008\n"
    "v_fma_f32 v53, v53, s52, v14\n"
    "v_mfma_f32_16x16x32_bf16 v[64:67], a[208:211], a[40:43], v[64:67]\n"
    "ds_read_b128 a[168:171], v10 offset:6144\n"
    "v_fma_f32 v60, v60, s52, v12\n"
    "v_mfma_f32_16x16x32_bf16 v[64:67], a[204:207], a[36:39], v[64:67]\n"
    "ds_read_b128 a[172:175], v11 offset:6144\n"
    "v_fma_f32 v61, v61, s52, v12\n"
    "v_mfma_f32_16x16x32_bf16 v[68:71], a[204:207], a[60:63], v[68:71]\n"
    "v_fma_f32 v62, v62, s52, v12\n"
    "v_fma_f32 v63, v63, s52, v12\n"
    "v_mfma_f32_16x16x32_bf16 v[68:71], a[208:211], a[64:67], v[68:71]\n"
    "v_exp_f32 v60, v60\n"
    "v_mfma_f32_16x16x32_bf16 v[68:71], a[212:215], a[68:71], v[68:71]\n"
    "v_exp_f32 v61, v61\n"
    "v_mfma_f32_16x16x32_bf16 v[72:75], a[212:215], a[92:95], v[72:75]\n"
    "ds_read_b64_tr_b16 a[212:213], v3 offset:36992\n"
    "v_cvt_pk_bf16_f32 v46, v60, v61\n"
    "v_mfma_f32_16x16x32_bf16 v[72:75], a[208:211], a[88:91], v[72:75]\n"
    "ds_read_b64_tr_b16 a[208:209], v2 offset:36992\n"
    "v_fma_f32 v64, v64, s52, v13\n"
    "v_mfma_f32_16x16x32_bf16 v[72:75], a[204:207], a[84:87], v[72:75]\n"
    "buffer_load_dwordx4 v7, s[36:39], s55 offen offset:1024 nt lds\n"
    "v_smfmac_f32_16x16x64_bf16 a[236:239], a[252:255], v[28:35], v23\n"
    "ds_read_b64_tr_b16 a[204:205], v5 offset:36864\n"
    "v_fma_f32 v65, v65, s52, v13\n"
    "v_smfmac_f32_16x16x64_bf16 a[240:243], a[252:255], v[36:43], v23\n"
    "s_waitcnt lgkmcnt(8)\n"
    "ds_read_b64_tr_b16 a[206:207], v5 offset:43008\n"
    "v_fma_f32 v66, v66, s52, v13\n"
    "v_mfma_f32_16x16x32_bf16 v[96:99], a[192:195], v[40:43], v[96:99]\n"
    "ds_read_b64_tr_b16 a[210:211], v2 offset:43136\n"
    "v_fma_f32 v67, v67, s52, v13\n"
    "v_mfma_f32_16x16x32_bf16 v[92:95], a[192:195], v[36:39], v[92:95]\n"
    "ds_read_b64_tr_b16 a[214:215], v3 offset:43136\n"
    "v_fma_f32 v56, v56, s52, v15\n"
    "v_mfma_f32_16x16x32_bf16 v[88:91], a[192:195], v[32:35], v[88:91]\n"
    "v_exp_f32 v62, v62\n"
    "v_mfma_f32_16x16x32_bf16 v[84:87], a[192:195], v[28:31], v[84:87]\n"
    "s_waitcnt lgkmcnt(10)\n"
    "ds_read_b64_tr_b16 a[192:193], v4 offset:36992\n"
    "v_fma_f32 v57, v57, s52, v15\n"
    "v_mfma_f32_16x16x32_bf16 a[216:219], a[196:199], v[28:31], a[216:219]\n"
    "ds_read_b64_tr_b16 a[194:195], v4 offset:43136\n"
    "v_fma_f32 v54, v54, s52, v14\n"
    "v_mfma_f32_16x16x32_bf16 a[220:223], a[196:199], v[32:35], a[220:223]\n"
    "v_exp_f32 v63, v63\n"
    "v_mfma_f32_16x16x32_bf16 a[224:227], a[196:199], v[36:39], a[224:227]\n"
    "v_cvt_pk_bf16_f32 v47, v62, v63\n"
    "v_fma_f32 v55, v55, s52, v14\n"
    "v_mfma_f32_16x16x32_bf16 a[228:231], a[196:199], v[40:43], a[228:231]\n"
    "s_waitcnt lgkmcnt(10)\n"
    "ds_read_b64_tr_b16 a[196:197], v5 offset:36992\n"
    "v_fma_f32 v58, v58, s52, v15\n"
    "v_mfma_f32_16x16x32_bf16 v[112:115], a[200:203], v[40:43], v[112:115]\n"
    "ds_read_b64_tr_b16 a[198:199], v5 offset:43136\n"
    "v_fma_f32 v59, v59, s52, v15\n"
    "v_mfma_f32_16x16x32_bf16 v[108:111], a[200:203], v[36:39], v[108:111]\n"
    "v_exp_f32 v48, v48\n"
    "v_mfma_f32_16x16x32_bf16 v[104:107], a[200:203], v[32:35], v[104:107]\n"
    "v_exp_f32 v49, v49\n"
    "v_mfma_f32_16x16x32_bf16 v[100:103], a[200:203], v[28:31], v[100:103]\n"
    "s_waitcnt lgkmcnt(6)\n"
    "buffer_load_dwordx4 v8, s[36:39], s55 offen offset:2048 nt lds\n"
    "v_mfma_f32_16x16x32_bf16 v[116:119], a[204:207], v[28:31], v[116:119]\n"
    "s_add_u32 m0, s54, 73728\n"
    "ds_read_b64_tr_b16 a[200:201], v2 offset:37120\n"
    "v_cvt_pk_bf16_f32 v48, v48, v49\n"
    "v_mfma_f32_16x16x32_bf16 v[120:123], a[204:207], v[32:35], v[120:123]\n"
    "ds_read_b64_tr_b16 a[202:203], v2 offset:43264\n"
    "v_fma_f32 v68, v68, s52, v14\n"
    "v_mfma_f32_16x16x32_bf16 v[124:127], a[204:207], v[36:39], v[124:127]\n"
    "v_exp_f32 v50, v50\n"
    "v_mfma_f32_16x16x32_bf16 v[128:131], a[204:207], v[40:43], v[128:131]\n"
    "s_waitcnt lgkmcnt(7)\n"
    "ds_read_b64_tr_b16 a[204:205], v3 offset:37120\n"
    "v_fma_f32 v69, v69, s52, v14\n"
    "v_mfma_f32_16x16x32_bf16 v[144:147], a[208:211], v[40:43], v[144:147]\n"
    "ds_read_b64_tr_b16 a[206:207], v3 offset:43264\n"
    "v_fma_f32 v70, v70, s52, v14\n"
    "v_mfma_f32_16x16x32_bf16 v[140:143], a[208:211], v[36:39], v[140:143]\n"
    "v_exp_f32 v51, v51\n"
    "v_mfma_f32_16x16x32_bf16 v[136:139], a[208:211], v[32:35], v[136:139]\n"
    "v_exp_f32 v64, v64\n"
    "v_mfma_f32_16x16x32_bf16 v[132:135], a[208:211], v[28:31], v[132:135]\n"
    "s_waitcnt lgkmcnt(8)\n"
    "ds_read_b64_tr_b16 a[208:209], v4 offset:37120\n"
    "v_cvt_pk_bf16_f32 v49, v50, v51\n"
    "v_mfma_f32_16x16x32_bf16 a[232:235], a[212:215], v[28:31], a[232:235]\n"
    "ds_read_b64_tr_b16 a[210:211], v4 offset:43264\n"
    "v_fma_f32 v71, v71, s52, v14\n"
    "v_mfma_f32_16x16x32_bf16 v[148:151], a[212:215], v[32:35], v[148:151]\n"
    "v_exp_f32 v65, v65\n"
    "v_mfma_f32_16x16x32_bf16 v[152:155], a[212:215], v[36:39], v[152:155]\n"
    "v_exp_f32 v66, v66\n"
    "v_mfma_f32_16x16x32_bf16 v[156:159], a[212:215], v[40:43], v[156:159]\n"
    "s_waitcnt lgkmcnt(8)\n"
    "ds_read_b64_tr_b16 a[212:213], v5 offset:37120\n"
    "v_cvt_pk_bf16_f32 v50, v64, v65\n"
    "v_mfma_f32_16x16x32_bf16 v[172:175], a[192:195], v[40:43], v[172:175]\n"
    "ds_read_b64_tr_b16 a[214:215], v5 offset:43264\n"
    "v_fma_f32 v72, v72, s52, v15\n"
    "v_mfma_f32_16x16x32_bf16 v[168:171], a[192:195], v[36:39], v[168:171]\n"
    "v_exp_f32 v67, v67\n"
    "v_mfma_f32_16x16x32_bf16 v[164:167], a[192:195], v[32:35], v[164:167]\n"
    "buffer_load_dwordx4 v6, s[40:43], s56 offen offset:0 nt lds\n"
    "v_mfma_f32_16x16x32_bf16 v[160:163], a[192:195], v[28:31], v[160:163]\n"
    "s_waitcnt lgkmcnt(8)\n"
    "v_cvt_pk_bf16_f32 v51, v66, v67\n"
    "v_fma_f32 v73, v73, s52, v15\n"
    "v_mfma_f32_16x16x32_bf16 v[176:179], a[196:199], v[28:31], v[176:179]\n"
    "v_exp_f32 v52, v52\n"
    "v_mfma_f32_16x16x32_bf16 v[180:183], a[196:199], v[32:35], v[180:183]\n"
    "v_exp_f32 v53, v53\n"
    "v_mfma_f32_16x16x32_bf16 v[184:187], a[196:199], v[36:39], v[184:187]\n"
    "s_waitcnt vmcnt(16) lgkmcnt(0)\n"
    "v_exp_f32 v56, v56\n"
    "v_mfma_f32_16x16x32_bf16 v[188:191], a[196:199], v[40:43], v[188:191]\n"
    "s_barrier\n"
    "ds_read_b128 a[192:195], v0 offset:0\n"
    "v_fma_f32 v74, v74, s52, v15\n"
    "v_mfma_f32_16x16x32_bf16 v[204:207], a[200:203], v[40:43], v[204:207]\n"
    "ds_read_b128 a[196:199], v1 offset:0\n"
    "v_fma_f32 v75, v75, s52, v15\n"
    "v_mfma_f32_16x16x32_bf16 v[200:203], a[200:203], v[36:39], v[200:203]\n"
    "v_exp_f32 v57, v57\n"
    "v_mfma_f32_16x16x32_bf16 v[196:199], a[200:203], v[32:35], v[196:199]\n"
    "v_exp_f32 v54, v54\n"
    "v_mfma_f32_16x16x32_bf16 v[192:195], a[200:203], v[28:31], v[192:195]\n"
    "ds_read_b128 a[200:203], v0 offset:128\n"
    "v_cvt_pk_bf16_f32 v52, v52, v53\n"
    "v_mfma_f32_16x16x32_bf16 v[208:211], a[204:207], v[28:31], v[208:211]\n"
    "v_exp_f32 v55, v55\n"
    "v_mfma_f32_16x16x32_bf16 v[212:215], a[204:207], v[32:35], v[212:215]\n"
    "v_exp_f32 v58, v58\n"
    "v_mfma_f32_16x16x32_bf16 v[216:219], a[204:207], v[36:39], v[216:219]\n"
    "v_exp_f32 v59, v59\n"
    "v_mfma_f32_16x16x32_bf16 v[220:223], a[204:207], v[40:43], v[220:223]\n"
    "ds_read_b128 a[204:207], v1 offset:128\n"
    "v_cvt_pk_bf16_f32 v56, v56, v57\n"
    "v_mfma_f32_16x16x32_bf16 v[236:239], a[208:211], v[40:43], v[236:239]\n"
    "buffer_load_dwordx4 v7, s[40:43], s56 offen offset:1024 nt lds\n"
    "v_mfma_f32_16x16x32_bf16 v[232:235], a[208:211], v[36:39], v[232:235]\n"
    "v_exp_f32 v68, v68\n"
    "v_mfma_f32_16x16x32_bf16 v[228:231], a[208:211], v[32:35], v[228:231]\n"
    "v_exp_f32 v69, v69\n"
    "v_mfma_f32_16x16x32_bf16 v[224:227], a[208:211], v[28:31], v[224:227]\n"
    "ds_read_b128 a[208:211], v0 offset:256\n"
    "v_cvt_pk_bf16_f32 v53, v54, v55\n"
    "v_mfma_f32_16x16x32_bf16 v[240:243], a[212:215], v[28:31], v[240:243]\n"
    "v_exp_f32 v70, v70\n"
    "v_mfma_f32_16x16x32_bf16 v[244:247], a[212:215], v[32:35], v[244:247]\n"
    "v_exp_f32 v71, v71\n"
    "v_mfma_f32_16x16x32_bf16 v[248:251], a[212:215], v[36:39], v[248:251]\n"
    "v_exp_f32 v72, v72\n"
    "v_mfma_f32_16x16x32_bf16 v[252:255], a[212:215], v[40:43], v[252:255]\n"
    "s_waitcnt lgkmcnt(4)\n"
    "ds_read_b128 a[212:215], v1 offset:256\n"
    "v_cvt_pk_bf16_f32 v57, v58, v59\n"
    "v_mfma_f32_16x16x32_bf16 v[28:31], a[192:195], a[0:3], 0\n"
    "s_waitcnt lgkmcnt(4)\n"
    "v_exp_f32 v73, v73\n"
    "v_mfma_f32_16x16x32_bf16 v[28:31], a[196:199], a[4:7], v[28:31]\n"
    "s_waitcnt lgkmcnt(3)\n"
    "v_exp_f32 v74, v74\n"
    "v_mfma_f32_16x16x32_bf16 v[28:31], a[200:203], a[8:11], v[28:31]\n"
    "v_exp_f32 v75, v75\n"
    "v_mfma_f32_16x16x32_bf16 v[32:35], a[200:203], a[32:35], 0\n"
    "v_cvt_pk_bf16_f32 v54, v68, v69\n"
    "v_cvt_pk_bf16_f32 v55, v70, v71\n"
    "v_mfma_f32_16x16x32_bf16 v[32:35], a[196:199], a[28:31], v[32:35]\n"
    "v_cvt_pk_bf16_f32 v58, v72, v73\n"
    "v_cvt_pk_bf16_f32 v59, v74, v75\n"
    "v_mfma_f32_16x16x32_bf16 v[32:35], a[192:195], a[24:27], v[32:35]\n"
    "v_mfma_f32_16x16x32_bf16 v[36:39], a[192:195], a[48:51], 0\n"
    "buffer_load_dwordx4 v8, s[40:43], s56 offen offset:2048 nt lds\n"
    "v_mfma_f32_16x16x32_bf16 v[36:39], a[196:199], a[52:55], v[36:39]\n"
    "s_add_u32 m0, s57, 4096\n"
    "v_mfma_f32_16x16x32_bf16 v[36:39], a[200:203], a[56:59], v[36:39]\n"
    "s_add_u32 s56, s56, 24576\n"
    "buffer_load_dwordx4 v9, s[44:47], s63 offen offset:0 nt lds\n"
    "v_mfma_f32_16x16x32_bf16 v[40:43], a[200:203], a[80:83], 0\n"
    "s_add_u32 s63, s62, 256\n"
    "buffer_load_dwordx4 v9, s[44:47], s64 offen offset:1024 nt lds\n"
    "v_mfma_f32_16x16x32_bf16 v[40:43], a[196:199], a[76:79], v[40:43]\n"
    "s_add_u32 m0, s54, 36864\n"
    "ds_read_b128 a[196:199], v1 offset:6144\n"
    "ds_read_b128 a[200:203], v0 offset:6272\n"
    "v_mfma_f32_16x16x32_bf16 v[40:43], a[192:195], a[72:75], v[40:43]\n"
    "s_waitcnt lgkmcnt(4)\n"
    "ds_read_b128 a[192:195], v0 offset:6144\n"
    "v_mfma_f32_16x16x32_bf16 v[28:31], a[204:207], a[12:15], v[28:31]\n"
    "s_waitcnt lgkmcnt(4)\n"
    "v_mfma_f32_16x16x32_bf16 v[28:31], a[208:211], a[16:19], v[28:31]\n"
    "s_waitcnt lgkmcnt(3)\n"
    "v_mfma_f32_16x16x32_bf16 v[28:31], a[212:215], a[20:23], v[28:31]\n"
    "s_add_u32 s64, s62, 2304\n"
    "v_mfma_f32_16x16x32_bf16 v[32:35], a[212:215], a[44:47], v[32:35]\n"
    "v_mfma_f32_16x16x32_bf16 v[32:35], a[208:211], a[40:43], v[32:35]\n"
    "v_mfma_f32_16x16x32_bf16 v[32:35], a[204:207], a[36:39], v[32:35]\n"
    "v_mfma_f32_16x16x32_bf16 v[36:39], a[204:207], a[60:63], v[36:39]\n"
    "v_mfma_f32_16x16x32_bf16 v[36:39], a[208:211], a[64:67], v[36:39]\n"
    "s_waitcnt vmcnt(18)\n"
    "v_mfma_f32_16x16x32_bf16 v[36:39], a[212:215], a[68:71], v[36:39]\n"
    "ds_read_b128 a[104:107], v10 offset:8192\n"
    "v_fma_f32 v28, v28, s52, v12\n"
    "v_mfma_f32_16x16x32_bf16 v[40:43], a[212:215], a[92:95], v[40:43]\n"
    "buffer_load_dwordx4 v6, s[36:39], s56 offen offset:0 nt lds\n"
    "v_mfma_f32_16x16x32_bf16 v[40:43], a[208:211], a[88:91], v[40:43]\n"
    "ds_read_b128 a[208:211], v0 offset:6400\n"
    "v_fma_f32 v29, v29, s52, v12\n"
    "v_mfma_f32_16x16x32_bf16 v[40:43], a[204:207], a[84:87], v[40:43]\n"
    "s_waitcnt lgkmcnt(2)\n"
    "ds_read_b128 a[204:207], v1 offset:6272\n"
    "v_fma_f32 v30, v30, s52, v12\n"
    "v_mfma_f32_16x16x32_bf16 v[76:79], a[192:195], a[0:3], 0\n"
    "ds_read_b128 a[212:215], v1 offset:6400\n"
    "v_fma_f32 v31, v31, s52, v12\n"
    "v_mfma_f32_16x16x32_bf16 v[76:79], a[196:199], a[4:7], v[76:79]\n"
    "ds_read_b128 a[108:111], v11 offset:8192\n"
    "v_fma_f32 v32, v32, s52, v13\n"
    "v_mfma_f32_16x16x32_bf16 v[76:79], a[200:203], a[8:11], v[76:79]\n"
    "v_exp_f32 v28, v28\n"
    "v_mfma_f32_16x16x32_bf16 v[80:83], a[200:203], a[32:35], 0\n"
    "v_exp_f32 v29, v29\n"
    "v_mfma_f32_16x16x32_bf16 v[80:83], a[196:199], a[28:31], v[80:83]\n"
    "v_exp_f32 v30, v30\n"
    "v_mfma_f32_16x16x32_bf16 v[80:83], a[192:195], a[24:27], v[80:83]\n"
    "v_exp_f32 v31, v31\n"
    "v_mfma_f32_16x16x32_bf16 v[60:63], a[192:195], a[48:51], 0\n"
    "v_cvt_pk_bf16_f32 v28, v28, v29\n"
    "v_fma_f32 v33, v33, s52, v13\n"
    "v_mfma_f32_16x16x32_bf16 v[60:63], a[196:199], a[52:55], v[60:63]\n"
    "v_cvt_pk_bf16_f32 v29, v30, v31\n"
    "v_fma_f32 v34, v34, s52, v13\n"
    "v_mfma_f32_16x16x32_bf16 v[60:63], a[200:203], a[56:59], v[60:63]\n"
    "v_exp_f32 v32, v32\n"
    "v_mfma_f32_16x16x32_bf16 v[64:67], a[200:203], a[80:83], 0\n"
    "ds_read_b64_tr_b16 a[200:201], v4 offset:49152\n"
    "v_fma_f32 v35, v35, s52, v13\n"
    "v_mfma_f32_16x16x32_bf16 v[64:67], a[196:199], a[76:79], v[64:67]\n"
    "ds_read_b64_tr_b16 a[196:197], v3 offset:49152\n"
    "v_fma_f32 v36, v36, s52, v14\n"
    "v_mfma_f32_16x16x32_bf16 v[64:67], a[192:195], a[72:75], v[64:67]\n"
    "s_waitcnt lgkmcnt(4)\n"
    "buffer_load_dwordx4 v7, s[36:39], s56 offen offset:1024 nt lds\n"
    "v_mfma_f32_16x16x32_bf16 v[76:79], a[204:207], a[12:15], v[76:79]\n"
    "ds_read_b64_tr_b16 a[192:193], v2 offset:49152\n"
    "v_fma_f32 v37, v37, s52, v14\n"
    "v_mfma_f32_16x16x32_bf16 v[76:79], a[208:211], a[16:19], v[76:79]\n"
    "s_waitcnt lgkmcnt(4)\n"
    "ds_read_b64_tr_b16 a[194:195], v2 offset:55296\n"
    "v_fma_f32 v40, v40, s52, v15\n"
    "v_mfma_f32_16x16x32_bf16 v[76:79], a[212:215], a[20:23], v[76:79]\n"
    "ds_read_b64_tr_b16 a[198:199], v3 offset:55296\n"
    "v_fma_f32 v41, v41, s52, v15\n"
    "v_mfma_f32_16x16x32_bf16 v[80:83], a[212:215], a[44:47], v[80:83]\n"
    "ds_read_b64_tr_b16 a[202:203], v4 offset:55296\n"
    "v_fma_f32 v38, v38, s52, v14\n"
    "v_mfma_f32_16x16x32_bf16 v[80:83], a[208:211], a[40:43], v[80:83]\n"
    "v_exp_f32 v33, v33\n"
    "v_mfma_f32_16x16x32_bf16 v[80:83], a[204:207], a[36:39], v[80:83]\n"
    "v_fma_f32 v76, v76, s52, v12\n"
    "v_fma_f32 v77, v77, s52, v12\n"
    "v_mfma_f32_16x16x32_bf16 v[60:63], a[204:207], a[60:63], v[60:63]\n"
    "v_fma_f32 v78, v78, s52, v12\n"
    "v_fma_f32 v79, v79, s52, v12\n"
    "v_mfma_f32_16x16x32_bf16 v[60:63], a[208:211], a[64:67], v[60:63]\n"
    "v_exp_f32 v76, v76\n"
    "v_mfma_f32_16x16x32_bf16 v[60:63], a[212:215], a[68:71], v[60:63]\n"
    "v_exp_f32 v77, v77\n"
    "v_mfma_f32_16x16x32_bf16 v[64:67], a[212:215], a[92:95], v[64:67]\n"
    "ds_read_b64_tr_b16 a[212:213], v3 offset:49280\n"
    "v_cvt_pk_bf16_f32 v30, v76, v77\n"
    "v_mfma_f32_16x16x32_bf16 v[64:67], a[208:211], a[88:91], v[64:67]\n"
    "ds_read_b64_tr_b16 a[208:209], v2 offset:49280\n"
    "v_fma_f32 v80, v80, s52, v13\n"
    "v_mfma_f32_16x16x32_bf16 v[64:67], a[204:207], a[84:87], v[64:67]\n"
    "ds_read_b64_tr_b16 a[204:205], v5 offset:49152\n"
    "v_fma_f32 v81, v81, s52, v13\n"
    "v_smfmac_f32_16x16x64_bf16 a[236:239], a[252:255], v[44:51], v23\n"
    "ds_read_b64_tr_b16 a[206:207], v5 offset:55296\n"
    "v_fma_f32 v82, v82, s52, v13\n"
    "v_smfmac_f32_16x16x64_bf16 a[240:243], a[252:255], v[52:59], v23\n"
    "s_waitcnt lgkmcnt(6)\n"
    "buffer_load_dwordx4 v8, s[36:39], s56 offen offset:2048 nt lds\n"
    "v_mfma_f32_16x16x32_bf16 v[96:99], a[192:195], v[56:59], v[96:99]\n"
    "s_add_u32 m0, s54, 86016\n"
    "ds_read_b64_tr_b16 a[210:211], v2 offset:55424\n"
    "v_fma_f32 v83, v83, s52, v13\n"
    "v_mfma_f32_16x16x32_bf16 v[92:95], a[192:195], v[52:55], v[92:95]\n"
    "ds_read_b64_tr_b16 a[214:215], v3 offset:55424\n"
    "v_cvt_pk_bf16_f32 v32, v32, v33\n"
    "v_mfma_f32_16x16x32_bf16 v[88:91], a[192:195], v[48:51], v[88:91]\n"
    "v_exp_f32 v78, v78\n"
    "v_mfma_f32_16x16x32_bf16 v[84:87], a[192:195], v[44:47], v[84:87]\n"
    "s_waitcnt lgkmcnt(7)\n"
    "ds_read_b64_tr_b16 a[192:193], v4 offset:49280\n"
    "v_fma_f32 v39, v39, s52, v14\n"
    "v_mfma_f32_16x16x32_bf16 a[216:219], a[196:199], v[44:47], a[216:219]\n"
    "ds_read_b64_tr_b16 a[194:195], v4 offset:55424\n"
    "v_fma_f32 v42, v42, s52, v15\n"
    "v_mfma_f32_16x16x32_bf16 a[220:223], a[196:199], v[48:51], a[220:223]\n"
    "v_exp_f32 v79, v79\n"
    "v_mfma_f32_16x16x32_bf16 a[224:227], a[196:199], v[52:55], a[224:227]\n"
    "v_cvt_pk_bf16_f32 v31, v78, v79\n"
    "v_fma_f32 v43, v43, s52, v15\n"
    "v_mfma_f32_16x16x32_bf16 a[228:231], a[196:199], v[56:59], a[228:231]\n"
    "s_waitcnt lgkmcnt(8)\n"
    "ds_read_b64_tr_b16 a[196:197], v5 offset:49280\n"
    "v_fma_f32 v60, v60, s52, v14\n"
    "v_mfma_f32_16x16x32_bf16 v[112:115], a[200:203], v[56:59], v[112:115]\n"
    "ds_read_b64_tr_b16 a[198:199], v5 offset:55424\n"
    "v_fma_f32 v61, v61, s52, v14\n"
    "v_mfma_f32_16x16x32_bf16 v[108:111], a[200:203], v[52:55], v[108:111]\n"
    "v_exp_f32 v34, v34\n"
    "v_mfma_f32_16x16x32_bf16 v[104:107], a[200:203], v[48:51], v[104:107]\n"
    "v_exp_f32 v35, v35\n"
    "v_mfma_f32_16x16x32_bf16 v[100:103], a[200:203], v[44:47], v[100:103]\n"
    "s_waitcnt lgkmcnt(6)\n"
    "ds_read_b64_tr_b16 a[200:201], v2 offset:49408\n"
    "v_cvt_pk_bf16_f32 v33, v34, v35\n"
    "v_mfma_f32_16x16x32_bf16 v[116:119], a[204:207], v[44:47], v[116:119]\n"
    "ds_read_b64_tr_b16 a[202:203], v2 offset:55552\n"
    "v_fma_f32 v62, v62, s52, v14\n"
    "v_mfma_f32_16x16x32_bf16 v[120:123], a[204:207], v[48:51], v[120:123]\n"
    "v_exp_f32 v80, v80\n"
    "v_mfma_f32_16x16x32_bf16 v[124:127], a[204:207], v[52:55], v[124:127]\n"
    "buffer_load_dwordx4 v6, s[40:43], s55 offen offset:0 nt lds\n"
    "v_mfma_f32_16x16x32_bf16 v[128:131], a[204:207], v[56:59], v[128:131]\n"
    "s_waitcnt lgkmcnt(7)\n"
    "ds_read_b64_tr_b16 a[204:205], v3 offset:49408\n"
    "v_fma_f32 v63, v63, s52, v14\n"
    "v_mfma_f32_16x16x32_bf16 v[144:147], a[208:211], v[56:59], v[144:147]\n"
    "ds_read_b64_tr_b16 a[206:207], v3 offset:55552\n"
    "v_fma_f32 v64, v64, s52, v15\n"
    "v_mfma_f32_16x16x32_bf16 v[140:143], a[208:211], v[52:55], v[140:143]\n"
    "v_exp_f32 v81, v81\n"
    "v_mfma_f32_16x16x32_bf16 v[136:139], a[208:211], v[48:51], v[136:139]\n"
    "v_exp_f32 v82, v82\n"
    "v_mfma_f32_16x16x32_bf16 v[132:135], a[208:211], v[44:47], v[132:135]\n"
    "s_waitcnt lgkmcnt(8)\n"
    "ds_read_b64_tr_b16 a[208:209], v4 offset:49408\n"
    "v_cvt_pk_bf16_f32 v34, v80, v81\n"
    "v_mfma_f32_16x16x32_bf16 a[232:235], a[212:215], v[44:47], a[232:235]\n"
    "ds_read_b64_tr_b16 a[210:211], v4 offset:55552\n"
    "v_fma_f32 v65, v65, s52, v15\n"
    "v_mfma_f32_16x16x32_bf16 v[148:151], a[212:215], v[48:51], v[148:151]\n"
    "v_exp_f32 v83, v83\n"
    "v_mfma_f32_16x16x32_bf16 v[152:155], a[212:215], v[52:55], v[152:155]\n"
    "v_cvt_pk_bf16_f32 v35, v82, v83\n"
    "v_fma_f32 v66, v66, s52, v15\n"
    "v_mfma_f32_16x16x32_bf16 v[156:159], a[212:215], v[56:59], v[156:159]\n"
    "s_waitcnt lgkmcnt(8)\n"
    "ds_read_b64_tr_b16 a[212:213], v5 offset:49408\n"
    "v_fma_f32 v67, v67, s52, v15\n"
    "v_mfma_f32_16x16x32_bf16 v[172:175], a[192:195], v[56:59], v[172:175]\n"
    "v_exp_f32 v36, v36\n"
    "v_mfma_f32_16x16x32_bf16 v[168:171], a[192:195], v[52:55], v[168:171]\n"
    "v_exp_f32 v37, v37\n"
    "v_mfma_f32_16x16x32_bf16 v[164:167], a[192:195], v[48:51], v[164:167]\n"
    "ds_read_b64_tr_b16 a[214:215], v5 offset:55552\n"
    "v_cvt_pk_bf16_f32 v36, v36, v37\n"
    "v_mfma_f32_16x16x32_bf16 v[160:163], a[192:195], v[44:47], v[160:163]\n"
    "s_waitcnt lgkmcnt(8)\n"
    "v_exp_f32 v40, v40\n"
    "v_mfma_f32_16x16x32_bf16 v[176:179], a[196:199], v[44:47], v[176:179]\n"
    "buffer_load_dwordx4 v7, s[40:43], s55 offen offset:1024 nt lds\n"
    "v_mfma_f32_16x16x32_bf16 v[180:183], a[196:199], v[48:51], v[180:183]\n"
    "v_exp_f32 v41, v41\n"
    "v_mfma_f32_16x16x32_bf16 v[184:187], a[196:199], v[52:55], v[184:187]\n"
    "v_exp_f32 v38, v38\n"
    "v_mfma_f32_16x16x32_bf16 v[188:191], a[196:199], v[56:59], v[188:191]\n"
    "s_waitcnt lgkmcnt(6)\n"
    "v_exp_f32 v39, v39\n"
    "v_mfma_f32_16x16x32_bf16 v[204:207], a[200:203], v[56:59], v[204:207]\n"
    "s_waitcnt vmcnt(17) lgkmcnt(0)\n"
    "v_exp_f32 v42, v42\n"
    "v_mfma_f32_16x16x32_bf16 v[200:203], a[200:203], v[52:55], v[200:203]\n"
    "s_barrier\n"
    "ds_read_b128 a[192:195], v0 offset:12288\n"
    "v_cvt_pk_bf16_f32 v40, v40, v41\n"
    "v_mfma_f32_16x16x32_bf16 v[196:199], a[200:203], v[48:51], v[196:199]\n"
    "ds_read_b128 a[196:199], v1 offset:12288\n"
    "v_cvt_pk_bf16_f32 v37, v38, v39\n"
    "v_mfma_f32_16x16x32_bf16 v[192:195], a[200:203], v[44:47], v[192:195]\n"
    "v_exp_f32 v43, v43\n"
    "v_mfma_f32_16x16x32_bf16 v[208:211], a[204:207], v[44:47], v[208:211]\n"
    "ds_read_b128 a[200:203], v0 offset:12416\n"
    "v_cvt_pk_bf16_f32 v41, v42, v43\n"
    "v_mfma_f32_16x16x32_bf16 v[212:215], a[204:207], v[48:51], v[212:215]\n"
    "v_exp_f32 v60, v60\n"
    "v_mfma_f32_16x16x32_bf16 v[216:219], a[204:207], v[52:55], v[216:219]\n"
    "v_exp_f32 v61, v61\n"
    "v_mfma_f32_16x16x32_bf16 v[220:223], a[204:207], v[56:59], v[220:223]\n"
    "ds_read_b128 a[204:207], v1 offset:12416\n"
    "v_cvt_pk_bf16_f32 v38, v60, v61\n"
    "v_mfma_f32_16x16x32_bf16 v[236:239], a[208:211], v[56:59], v[236:239]\n"
    "v_exp_f32 v62, v62\n"
    "v_mfma_f32_16x16x32_bf16 v[232:235], a[208:211], v[52:55], v[232:235]\n"
    "v_exp_f32 v63, v63\n"
    "v_mfma_f32_16x16x32_bf16 v[228:231], a[208:211], v[48:51], v[228:231]\n"
    "buffer_load_dwordx4 v8, s[40:43], s55 offen offset:2048 nt lds\n"
    "v_mfma_f32_16x16x32_bf16 v[224:227], a[208:211], v[44:47], v[224:227]\n"
    "s_add_u32 m0, s57, 6144\n"
    "ds_read_b128 a[208:211], v0 offset:12544\n"
    "v_cvt_pk_bf16_f32 v39, v62, v63\n"
    "v_mfma_f32_16x16x32_bf16 v[240:243], a[212:215], v[44:47], v[240:243]\n"
    "s_add_u32 s55, s55, 24576\n"
    "buffer_load_dwordx4 v9, s[44:47], s63 offen offset:0 nt lds\n"
    "v_mfma_f32_16x16x32_bf16 v[244:247], a[212:215], v[48:51], v[244:247]\n"
    "s_add_u32 s63, s62, 6400\n"
    "buffer_load_dwordx4 v9, s[44:47], s64 offen offset:1024 nt lds\n"
    "v_mfma_f32_16x16x32_bf16 v[248:251], a[212:215], v[52:55], v[248:251]\n"
    "s_add_u32 m0, s54, 49152\n"
    "v_exp_f32 v64, v64\n"
    "v_mfma_f32_16x16x32_bf16 v[252:255], a[212:215], v[56:59], v[252:255]\n"
    "s_waitcnt vmcnt(18) lgkmcnt(4)\n"
    "v_exp_f32 v65, v65\n"
    "v_mfma_f32_16x16x32_bf16 v[44:47], a[192:195], a[0:3], 0\n"
    "s_waitcnt lgkmcnt(3)\n"
    "ds_read_b128 a[212:215], v1 offset:12544\n"
    "v_cvt_pk_bf16_f32 v42, v64, v65\n"
    "v_mfma_f32_16x16x32_bf16 v[44:47], a[196:199], a[4:7], v[44:47]\n"
    "s_waitcnt lgkmcnt(3)\n"
    "ds_read_b128 a[128:131], v10 offset:0\n"
    "ds_read_b128 a[132:135], v11 offset:0\n"
    "v_mfma_f32_16x16x32_bf16 v[44:47], a[200:203], a[8:11], v[44:47]\n"
    "s_add_u32 s64, s62, 8448\n"
    "v_exp_f32 v66, v66\n"
    "v_mfma_f32_16x16x32_bf16 v[48:51], a[200:203], a[32:35], 0\n"
    "v_exp_f32 v67, v67\n"
    "v_mfma_f32_16x16x32_bf16 v[48:51], a[196:199], a[28:31], v[48:51]\n"
    "v_cvt_pk_bf16_f32 v43, v66, v67\n"
    "v_mfma_f32_16x16x32_bf16 v[48:51], a[192:195], a[24:27], v[48:51]\n"
    "v_mfma_f32_16x16x32_bf16 v[52:55], a[192:195], a[48:51], 0\n"
    "v_mfma_f32_16x16x32_bf16 v[52:55], a[196:199], a[52:55], v[52:55]\n"
    "v_mfma_f32_16x16x32_bf16 v[52:55], a[200:203], a[56:59], v[52:55]\n"
    "v_mfma_f32_16x16x32_bf16 v[56:59], a[200:203], a[80:83], 0\n"
    "buffer_load_dwordx4 v6, s[36:39], s55 offen offset:0 nt lds\n"
    "v_mfma_f32_16x16x32_bf16 v[56:59], a[196:199], a[76:79], v[56:59]\n"
    "s_nop 0\n"
    "ds_read_b128 a[196:199], v1 offset:18432\n"
    "ds_read_b128 a[200:203], v0 offset:18560\n"
    "v_mfma_f32_16x16x32_bf16 v[56:59], a[192:195], a[72:75], v[56:59]\n"
    "s_waitcnt lgkmcnt(6)\n"
    "ds_read_b128 a[192:195], v0 offset:18432\n"
    "v_mfma_f32_16x16x32_bf16 v[44:47], a[204:207], a[12:15], v[44:47]\n"
    "s_waitcnt lgkmcnt(6)\n"
    "v_mfma_f32_16x16x32_bf16 v[44:47], a[208:211], a[16:19], v[44:47]\n"
    "s_waitcnt lgkmcnt(5)\n"
    "v_mfma_f32_16x16x32_bf16 v[44:47], a[212:215], a[20:23], v[44:47]\n"
    "v_mfma_f32_16x16x32_bf16 v[48:51], a[212:215], a[44:47], v[48:51]\n"
    "v_mfma_f32_16x16x32_bf16 v[48:51], a[208:211], a[40:43], v[48:51]\n"
    "v_mfma_f32_16x16x32_bf16 v[48:51], a[204:207], a[36:39], v[48:51]\n"
    "v_mfma_f32_16x16x32_bf16 v[52:55], a[204:207], a[60:63], v[52:55]\n"
    "v_mfma_f32_16x16x32_bf16 v[52:55], a[208:211], a[64:67], v[52:55]\n"
    "v_mfma_f32_16x16x32_bf16 v[52:55], a[212:215], a[68:71], v[52:55]\n"
    "v_mfma_f32_16x16x32_bf16 v[56:59], a[212:215], a[92:95], v[56:59]\n"
    "ds_read_b128 a[212:215], v1 offset:18688\n"
    "v_fma_f32 v44, v44, s52, v12\n"
    "v_mfma_f32_16x16x32_bf16 v[56:59], a[208:211], a[88:91], v[56:59]\n"
    "ds_read_b128 a[208:211], v0 offset:18688\n"
    "v_fma_f32 v45, v45, s52, v12\n"
    "v_mfma_f32_16x16x32_bf16 v[56:59], a[204:207], a[84:87], v[56:59]\n"
    "s_waitcnt lgkmcnt(2)\n"
    "buffer_load_dwordx4 v7, s[36:39], s55 offen offset:1024 nt lds\n"
    "v_mfma_f32_16x16x32_bf16 v[68:71], a[192:195], a[0:3], 0\n"
    "ds_read_b128 a[204:207], v1 offset:18560\n"
    "v_fma_f32 v46, v46, s52, v12\n"
    "v_mfma_f32_16x16x32_bf16 v[68:71], a[196:199], a[4:7], v[68:71]\n"
    "v_exp_f32 v44, v44\n"
    "v_mfma_f32_16x16x32_bf16 v[68:71], a[200:203], a[8:11], v[68:71]\n"
    "v_exp_f32 v45, v45\n"
    "v_mfma_f32_16x16x32_bf16 v[72:75], a[200:203], a[32:35], 0\n"
    "v_fma_f32 v47, v47, s52, v12\n"
    "v_cvt_pk_bf16_f32 v44, v44, v45\n"
    "v_mfma_f32_16x16x32_bf16 v[72:75], a[196:199], a[28:31], v[72:75]\n"
    "v_exp_f32 v46, v46\n"
    "v_mfma_f32_16x16x32_bf16 v[72:75], a[192:195], a[24:27], v[72:75]\n"
    "v_exp_f32 v47, v47\n"
    "v_mfma_f32_16x16x32_bf16 v[76:79], a[192:195], a[48:51], 0\n"
    "v_cvt_pk_bf16_f32 v45, v46, v47\n"
    "v_fma_f32 v48, v48, s52, v13\n"
    "v_mfma_f32_16x16x32_bf16 v[76:79], a[196:199], a[52:55], v[76:79]\n"
    "v_fma_f32 v49, v49, s52, v13\n"
    "v_fma_f32 v50, v50, s52, v13\n"
    "v_mfma_f32_16x16x32_bf16 v[76:79], a[200:203], a[56:59], v[76:79]\n"
    "v_exp_f32 v48, v48\n"
    "v_mfma_f32_16x16x32_bf16 v[80:83], a[200:203], a[80:83], 0\n"
    "ds_read_b64_tr_b16 a[200:201], v4 offset:0\n"
    "v_fma_f32 v51, v51, s52, v13\n"
    "v_mfma_f32_16x16x32_bf16 v[80:83], a[196:199], a[76:79], v[80:83]\n"
    "ds_read_b64_tr_b16 a[196:197], v3 offset:0\n"
    "v_fma_f32 v52, v52, s52, v14\n"
    "v_mfma_f32_16x16x32_bf16 v[80:83], a[192:195], a[72:75], v[80:83]\n"
    "s_waitcnt lgkmcnt(2)\n"
    "ds_read_b64_tr_b16 a[192:193], v2 offset:0\n"
    "v_fma_f32 v53, v53, s52, v14\n"
    "v_mfma_f32_16x16x32_bf16 v[68:71], a[204:207], a[12:15], v[68:71]\n"
    "ds_read_b64_tr_b16 a[194:195], v2 offset:6144\n"
    "v_fma_f32 v56, v56, s52, v15\n"
    "v_mfma_f32_16x16x32_bf16 v[68:71], a[208:211], a[16:19], v[68:71]\n"
    "buffer_load_dwordx4 v8, s[36:39], s55 offen offset:2048 nt lds\n"
    "v_mfma_f32_16x16x32_bf16 v[68:71], a[212:215], a[20:23], v[68:71]\n"
    "s_add_u32 m0, s54, 98304\n"
    "ds_read_b64_tr_b16 a[198:199], v3 offset:6144\n"
    "v_fma_f32 v57, v57, s52, v15\n"
    "v_mfma_f32_16x16x32_bf16 v[72:75], a[212:215], a[44:47], v[72:75]\n"
    "ds_read_b64_tr_b16 a[202:203], v4 offset:6144\n"
    "v_fma_f32 v54, v54, s52, v14\n"
    "v_mfma_f32_16x16x32_bf16 v[72:75], a[208:211], a[40:43], v[72:75]\n"
    "v_exp_f32 v49, v49\n"
    "v_mfma_f32_16x16x32_bf16 v[72:75], a[204:207], a[36:39], v[72:75]\n"
    "v_fma_f32 v68, v68, s52, v12\n"
    "v_fma_f32 v69, v69, s52, v12\n"
    "v_mfma_f32_16x16x32_bf16 v[76:79], a[204:207], a[60:63], v[76:79]\n"
    "v_fma_f32 v70, v70, s52, v12\n"
    "v_fma_f32 v71, v71, s52, v12\n"
    "v_mfma_f32_16x16x32_bf16 v[76:79], a[208:211], a[64:67], v[76:79]\n"
    "v_exp_f32 v68, v68\n"
    "v_mfma_f32_16x16x32_bf16 v[76:79], a[212:215], a[68:71], v[76:79]\n"
    "v_exp_f32 v69, v69\n"
    "v_mfma_f32_16x16x32_bf16 v[80:83], a[212:215], a[92:95], v[80:83]\n"
    "ds_read_b64_tr_b16 a[212:213], v3 offset:128\n"
    "v_cvt_pk_bf16_f32 v46, v68, v69\n"
    "v_mfma_f32_16x16x32_bf16 v[80:83], a[208:211], a[88:91], v[80:83]\n"
    "ds_read_b64_tr_b16 a[208:209], v2 offset:128\n"
    "v_fma_f32 v72, v72, s52, v13\n"
    "v_mfma_f32_16x16x32_bf16 v[80:83], a[204:207], a[84:87], v[80:83]\n"
    "ds_read_b64_tr_b16 a[204:205], v5 offset:0\n"
    "v_fma_f32 v73, v73, s52, v13\n"
    "v_smfmac_f32_16x16x64_bf16 a[236:239], a[252:255], v[28:35], v23\n"
    "ds_read_b64_tr_b16 a[206:207], v5 offset:6144\n"
    "v_fma_f32 v74, v74, s52, v13\n"
    "v_smfmac_f32_16x16x64_bf16 a[240:243], a[252:255], v[36:43], v23\n"
    "s_waitcnt lgkmcnt(6)\n"
    "ds_read_b64_tr_b16 a[210:211], v2 offset:6272\n"
    "v_fma_f32 v75, v75, s52, v13\n"
    "v_mfma_f32_16x16x32_bf16 v[96:99], a[192:195], v[40:43], v[96:99]\n"
    "ds_read_b64_tr_b16 a[214:215], v3 offset:6272\n"
    "v_cvt_pk_bf16_f32 v48, v48, v49\n"
    "v_mfma_f32_16x16x32_bf16 v[92:95], a[192:195], v[36:39], v[92:95]\n"
    "v_exp_f32 v70, v70\n"
    "v_mfma_f32_16x16x32_bf16 v[88:91], a[192:195], v[32:35], v[88:91]\n"
    "buffer_load_dwordx4 v6, s[40:43], s56 offen offset:0 nt lds\n"
    "v_mfma_f32_16x16x32_bf16 v[84:87], a[192:195], v[28:31], v[84:87]\n"
    "s_waitcnt lgkmcnt(7)\n"
    "ds_read_b64_tr_b16 a[192:193], v4 offset:128\n"
    "v_fma_f32 v55, v55, s52, v14\n"
    "v_mfma_f32_16x16x32_bf16 a[216:219], a[196:199], v[28:31], a[216:219]\n"
    "ds_read_b64_tr_b16 a[194:195], v4 offset:6272\n"
    "v_fma_f32 v58, v58, s52, v15\n"
    "v_mfma_f32_16x16x32_bf16 a[220:223], a[196:199], v[32:35], a[220:223]\n"
    "v_exp_f32 v71, v71\n"
    "v_mfma_f32_16x16x32_bf16 a[224:227], a[196:199], v[36:39], a[224:227]\n"
    "v_cvt_pk_bf16_f32 v47, v70, v71\n"
    "v_fma_f32 v59, v59, s52, v15\n"
    "v_mfma_f32_16x16x32_bf16 a[228:231], a[196:199], v[40:43], a[228:231]\n"
    "s_waitcnt lgkmcnt(8)\n"
    "ds_read_b64_tr_b16 a[196:197], v5 offset:128\n"
    "v_fma_f32 v76, v76, s52, v14\n"
    "v_mfma_f32_16x16x32_bf16 v[112:115], a[200:203], v[40:43], v[112:115]\n"
    "ds_read_b64_tr_b16 a[198:199], v5 offset:6272\n"
    "v_fma_f32 v77, v77, s52, v14\n"
    "v_mfma_f32_16x16x32_bf16 v[108:111], a[200:203], v[36:39], v[108:111]\n"
    "v_exp_f32 v50, v50\n"
    "v_mfma_f32_16x16x32_bf16 v[104:107], a[200:203], v[32:35], v[104:107]\n"
    "v_exp_f32 v51, v51\n"
    "v_mfma_f32_16x16x32_bf16 v[100:103], a[200:203], v[28:31], v[100:103]\n"
    "s_waitcnt lgkmcnt(6)\n"
    "ds_read_b64_tr_b16 a[200:201], v2 offset:256\n"
    "v_cvt_pk_bf16_f32 v49, v50, v51\n"
    "v_mfma_f32_16x16x32_bf16 v[116:119], a[204:207], v[28:31], v[116:119]\n"
    "ds_read_b64_tr_b16 a[202:203], v2 offset:6400\n"
    "v_fma_f32 v78, v78, s52, v14\n"
    "v_mfma_f32_16x16x32_bf16 v[120:123], a[204:207], v[32:35], v[120:123]\n"
    "v_exp_f32 v72, v72\n"
    "v_mfma_f32_16x16x32_bf16 v[124:127], a[204:207], v[36:39], v[124:127]\n"
    "v_exp_f32 v73, v73\n"
    "v_mfma_f32_16x16x32_bf16 v[128:131], a[204:207], v[40:43], v[128:131]\n"
    "s_waitcnt lgkmcnt(7)\n"
    "ds_read_b64_tr_b16 a[204:205], v3 offset:256\n"
    "v_cvt_pk_bf16_f32 v50, v72, v73\n"
    "v_mfma_f32_16x16x32_bf16 v[144:147], a[208:211], v[40:43], v[144:147]\n"
    "buffer_load_dwordx4 v7, s[40:43], s56 offen offset:1024 nt lds\n"
    "v_mfma_f32_16x16x32_bf16 v[140:143], a[208:211], v[36:39], v[140:143]\n"
    "ds_read_b64_tr_b16 a[206:207], v3 offset:6400\n"
    "v_fma_f32 v79, v79, s52, v14\n"
    "v_mfma_f32_16x16x32_bf16 v[136:139], a[208:211], v[32:35], v[136:139]\n"
    "v_exp_f32 v74, v74\n"
    "v_mfma_f32_16x16x32_bf16 v[132:135], a[208:211], v[28:31], v[132:135]\n"
    "s_waitcnt lgkmcnt(8)\n"
    "ds_read_b64_tr_b16 a[208:209], v4 offset:256\n"
    "v_fma_f32 v80, v80, s52, v15\n"
    "v_mfma_f32_16x16x32_bf16 a[232:235], a[212:215], v[28:31], a[232:235]\n"
    "ds_read_b64_tr_b16 a[210:211], v4 offset:6400\n"
    "v_fma_f32 v81, v81, s52, v15\n"
    "v_mfma_f32_16x16x32_bf16 v[148:151], a[212:215], v[32:35], v[148:151]\n"
    "v_exp_f32 v75, v75\n"
    "v_mfma_f32_16x16x32_bf16 v[152:155], a[212:215], v[36:39], v[152:155]\n"
    "v_cvt_pk_bf16_f32 v51, v74, v75\n"
    "v_fma_f32 v82, v82, s52, v15\n"
    "v_mfma_f32_16x16x32_bf16 v[156:159], a[212:215], v[40:43], v[156:159]\n"
    "s_waitcnt lgkmcnt(8)\n"
    "ds_read_b64_tr_b16 a[212:213], v5 offset:256\n"
    "v_fma_f32 v83, v83, s52, v15\n"
    "v_mfma_f32_16x16x32_bf16 v[172:175], a[192:195], v[40:43], v[172:175]\n"
    "v_exp_f32 v52, v52\n"
    "v_mfma_f32_16x16x32_bf16 v[168:171], a[192:195], v[36:39], v[168:171]\n"
    "v_exp_f32 v53, v53\n"
    "v_mfma_f32_16x16x32_bf16 v[164:167], a[192:195], v[32:35], v[164:167]\n"
    "ds_read_b64_tr_b16 a[214:215], v5 offset:6400\n"
    "v_cvt_pk_bf16_f32 v52, v52, v53\n"
    "v_mfma_f32_16x16x32_bf16 v[160:163], a[192:195], v[28:31], v[160:163]\n"
    "s_waitcnt vmcnt(21) lgkmcnt(8)\n"
    "v_exp_f32 v56, v56\n"
    "v_mfma_f32_16x16x32_bf16 v[176:179], a[196:199], v[28:31], v[176:179]\n"
    "s_nop 0\n"
    "ds_read_b128 a[152:155], v10 offset:2048\n"
    "ds_read_b128 a[156:159], v11 offset:2048\n"
    "v_mfma_f32_16x16x32_bf16 v[180:183], a[196:199], v[32:35], v[180:183]\n"
    "v_exp_f32 v57, v57\n"
    "v_mfma_f32_16x16x32_bf16 v[184:187], a[196:199], v[36:39], v[184:187]\n"
    "buffer_load_dwordx4 v8, s[40:43], s56 offen offset:2048 nt lds\n"
    "v_mfma_f32_16x16x32_bf16 v[188:191], a[196:199], v[40:43], v[188:191]\n"
    "s_waitcnt lgkmcnt(8)\n"
    "s_add_u32 m0, s57, 8192\n"
    "v_cvt_pk_bf16_f32 v56, v56, v57\n"
    "v_mfma_f32_16x16x32_bf16 v[204:207], a[200:203], v[40:43], v[204:207]\n"
    "s_waitcnt vmcnt(16) lgkmcnt(2)\n"
    "buffer_load_dwordx4 v9, s[44:47], s63 offen offset:0 nt lds\n"
    "v_mfma_f32_16x16x32_bf16 v[200:203], a[200:203], v[36:39], v[200:203]\n"
    "s_barrier\n"
    "buffer_load_dwordx4 v9, s[44:47], s64 offen offset:1024 nt lds\n"
    "v_mfma_f32_16x16x32_bf16 v[196:199], a[200:203], v[32:35], v[196:199]\n"
    "s_add_u32 s56, s56, 24576\n"
    "ds_read_b128 a[192:195], v0 offset:24576\n"
    "ds_read_b128 a[196:199], v1 offset:24576\n"
    "v_mfma_f32_16x16x32_bf16 v[192:195], a[200:203], v[28:31], v[192:195]\n"
    "s_add_u32 m0, s54, 0\n"
    "v_exp_f32 v54, v54\n"
    "v_mfma_f32_16x16x32_bf16 v[208:211], a[204:207], v[28:31], v[208:211]\n"
    "s_add_u32 s63, s62, 12544\n"
    "v_exp_f32 v55, v55\n"
    "v_mfma_f32_16x16x32_bf16 v[212:215], a[204:207], v[32:35], v[212:215]\n"
    "s_add_u32 s64, s62, 14592\n"
    "ds_read_b128 a[200:203], v0 offset:24704\n"
    "v_cvt_pk_bf16_f32 v53, v54, v55\n"
    "v_mfma_f32_16x16x32_bf16 v[216:219], a[204:207], v[36:39], v[216:219]\n"
    "v_exp_f32 v58, v58\n"
    "v_mfma_f32_16x16x32_bf16 v[220:223], a[204:207], v[40:43], v[220:223]\n"
    "v_exp_f32 v59, v59\n"
    "v_mfma_f32_16x16x32_bf16 v[236:239], a[208:211], v[40:43], v[236:239]\n"
    "ds_read_b128 a[204:207], v1 offset:24704\n"
    "v_cvt_pk_bf16_f32 v57, v58, v59\n"
    "v_mfma_f32_16x16x32_bf16 v[232:235], a[208:211], v[36:39], v[232:235]\n"
    "v_exp_f32 v76, v76\n"
    "v_mfma_f32_16x16x32_bf16 v[228:231], a[208:211], v[32:35], v[228:231]\n"
    "v_exp_f32 v77, v77\n"
    "v_mfma_f32_16x16x32_bf16 v[224:227], a[208:211], v[28:31], v[224:227]\n"
    "ds_read_b128 a[208:211], v0 offset:24832\n"
    "v_cvt_pk_bf16_f32 v54, v76, v77\n"
    "v_mfma_f32_16x16x32_bf16 v[240:243], a[212:215], v[28:31], v[240:243]\n"
    "v_exp_f32 v78, v78\n"
    "v_mfma_f32_16x16x32_bf16 v[244:247], a[212:215], v[32:35], v[244:247]\n"
    "buffer_load_dwordx4 v6, s[36:39], s56 offen offset:0 nt lds\n"
    "v_mfma_f32_16x16x32_bf16 v[248:251], a[212:215], v[36:39], v[248:251]\n"
    "v_exp_f32 v79, v79\n"
    "v_mfma_f32_16x16x32_bf16 v[252:255], a[212:215], v[40:43], v[252:255]\n"
    "s_waitcnt lgkmcnt(4)\n"
    "ds_read_b128 a[212:215], v1 offset:24832\n"
    "v_cvt_pk_bf16_f32 v55, v78, v79\n"
    "v_mfma_f32_16x16x32_bf16 v[28:31], a[192:195], a[0:3], 0\n"
    "s_waitcnt lgkmcnt(4)\n"
    "v_exp_f32 v80, v80\n"
    "v_mfma_f32_16x16x32_bf16 v[28:31], a[196:199], a[4:7], v[28:31]\n"
    "s_waitcnt lgkmcnt(3)\n"
    "v_exp_f32 v81, v81\n"
    "v_mfma_f32_16x16x32_bf16 v[28:31], a[200:203], a[8:11], v[28:31]\n"
    "v_exp_f32 v82, v82\n"
    "v_mfma_f32_16x16x32_bf16 v[32:35], a[200:203], a[32:35], 0\n"
    "v_exp_f32 v83, v83\n"
    "v_mfma_f32_16x16x32_bf16 v[32:35], a[196:199], a[28:31], v[32:35]\n"
    "v_cvt_pk_bf16_f32 v58, v80, v81\n"
    "v_cvt_pk_bf16_f32 v59, v82, v83\n"
    "v_mfma_f32_16x16x32_bf16 v[32:35], a[192:195], a[24:27], v[32:35]\n"
    "v_mfma_f32_16x16x32_bf16 v[36:39], a[192:195], a[48:51], 0\n"
    "v_mfma_f32_16x16x32_bf16 v[36:39], a[196:199], a[52:55], v[36:39]\n"
    "v_mfma_f32_16x16x32_bf16 v[36:39], a[200:203], a[56:59], v[36:39]\n"
    "v_mfma_f32_16x16x32_bf16 v[40:43], a[200:203], a[80:83], 0\n"
    "ds_read_b128 a[200:203], v0 offset:30848\n"
    "v_mfma_f32_16x16x32_bf16 v[40:43], a[196:199], a[76:79], v[40:43]\n"
    "ds_read_b128 a[196:199], v1 offset:30720\n"
    "v_mfma_f32_16x16x32_bf16 v[40:43], a[192:195], a[72:75], v[40:43]\n"
    "s_waitcnt lgkmcnt(4)\n"
    "buffer_load_dwordx4 v7, s[36:39], s56 offen offset:1024 nt lds\n"
    "v_mfma_f32_16x16x32_bf16 v[28:31], a[204:207], a[12:15], v[28:31]\n"
    "s_waitcnt lgkmcnt(3)\n"
    "ds_read_b128 a[192:195], v0 offset:30720\n"
    "v_mfma_f32_16x16x32_bf16 v[28:31], a[208:211], a[16:19], v[28:31]\n"
    "s_waitcnt lgkmcnt(3)\n"
    "v_mfma_f32_16x16x32_bf16 v[28:31], a[212:215], a[20:23], v[28:31]\n"
    "v_mfma_f32_16x16x32_bf16 v[32:35], a[212:215], a[44:47], v[32:35]\n"
    "v_mfma_f32_16x16x32_bf16 v[32:35], a[208:211], a[40:43], v[32:35]\n"
    "v_mfma_f32_16x16x32_bf16 v[32:35], a[204:207], a[36:39], v[32:35]\n"
    "v_mfma_f32_16x16x32_bf16 v[36:39], a[204:207], a[60:63], v[36:39]\n"
    "v_mfma_f32_16x16x32_bf16 v[36:39], a[208:211], a[64:67], v[36:39]\n"
    "v_mfma_f32_16x16x32_bf16 v[36:39], a[212:215], a[68:71], v[36:39]\n"
    "v_mfma_f32_16x16x32_bf16 v[40:43], a[212:215], a[92:95], v[40:43]\n"
    "ds_read_b128 a[212:215], v1 offset:30976\n"
    "v_fma_f32 v28, v28, s52, v12\n"
    "v_mfma_f32_16x16x32_bf16 v[40:43], a[208:211], a[88:91], v[40:43]\n"
    "ds_read_b128 a[208:211], v0 offset:30976\n"
    "v_fma_f32 v29, v29, s52, v12\n"
    "v_mfma_f32_16x16x32_bf16 v[40:43], a[204:207], a[84:87], v[40:43]\n"
    "s_waitcnt lgkmcnt(2)\n"
    "ds_read_b128 a[204:207], v1 offset:30848\n"
    "v_fma_f32 v30, v30, s52, v12\n"
    "v_mfma_f32_16x16x32_bf16 v[60:63], a[192:195], a[0:3], 0\n"
    "v_exp_f32 v28, v28\n"
    "v_mfma_f32_16x16x32_bf16 v[60:63], a[196:199], a[4:7], v[60:63]\n"
    "buffer_load_dwordx4 v8, s[36:39], s56 offen offset:2048 nt lds\n"
    "v_mfma_f32_16x16x32_bf16 v[60:63], a[200:203], a[8:11], v[60:63]\n"
    "s_add_u32 m0, s54, 110592\n"
    "v_exp_f32 v29, v29\n"
    "v_mfma_f32_16x16x32_bf16 v[64:67], a[200:203], a[32:35], 0\n"
    "v_fma_f32 v31, v31, s52, v12\n"
    "v_cvt_pk_bf16_f32 v28, v28, v29\n"
    "v_mfma_f32_16x16x32_bf16 v[64:67], a[196:199], a[28:31], v[64:67]\n"
    "v_exp_f32 v30, v30\n"
    "v_mfma_f32_16x16x32_bf16 v[64:67], a[192:195], a[24:27], v[64:67]\n"
    "v_exp_f32 v31, v31\n"
    "v_mfma_f32_16x16x32_bf16 v[68:71], a[192:195], a[48:51], 0\n"
    "v_cvt_pk_bf16_f32 v29, v30, v31\n"
    "v_fma_f32 v32, v32, s52, v13\n"
    "v_mfma_f32_16x16x32_bf16 v[68:71], a[196:199], a[52:55], v[68:71]\n"
    "v_fma_f32 v33, v33, s52, v13\n"
    "v_fma_f32 v34, v34, s52, v13\n"
    "v_mfma_f32_16x16x32_bf16 v[68:71], a[200:203], a[56:59], v[68:71]\n"
    "v_exp_f32 v32, v32\n"
    "v_mfma_f32_16x16x32_bf16 v[72:75], a[200:203], a[80:83], 0\n"
    "ds_read_b64_tr_b16 a[200:201], v4 offset:12288\n"
    "v_fma_f32 v35, v35, s52, v13\n"
    "v_mfma_f32_16x16x32_bf16 v[72:75], a[196:199], a[76:79], v[72:75]\n"
    "ds_read_b64_tr_b16 a[196:197], v3 offset:12288\n"
    "v_fma_f32 v36, v36, s52, v14\n"
    "v_mfma_f32_16x16x32_bf16 v[72:75], a[192:195], a[72:75], v[72:75]\n"
    "s_waitcnt lgkmcnt(2)\n"
    "ds_read_b64_tr_b16 a[192:193], v2 offset:12288\n"
    "v_fma_f32 v37, v37, s52, v14\n"
    "v_mfma_f32_16x16x32_bf16 v[60:63], a[204:207], a[12:15], v[60:63]\n"
    "ds_read_b64_tr_b16 a[194:195], v2 offset:18432\n"
    "v_fma_f32 v40, v40, s52, v15\n"
    "v_mfma_f32_16x16x32_bf16 v[60:63], a[208:211], a[16:19], v[60:63]\n"
    "ds_read_b64_tr_b16 a[198:199], v3 offset:18432\n"
    "v_fma_f32 v41, v41, s52, v15\n"
    "v_mfma_f32_16x16x32_bf16 v[60:63], a[212:215], a[20:23], v[60:63]\n"
    "ds_read_b64_tr_b16 a[202:203], v4 offset:18432\n"
    "v_fma_f32 v38, v38, s52, v14\n"
    "v_mfma_f32_16x16x32_bf16 v[64:67], a[212:215], a[44:47], v[64:67]\n"
    "v_exp_f32 v33, v33\n"
    "v_mfma_f32_16x16x32_bf16 v[64:67], a[208:211], a[40:43], v[64:67]\n"
    "buffer_load_dwordx4 v6, s[40:43], s55 offen offset:0 nt lds\n"
    "v_mfma_f32_16x16x32_bf16 v[64:67], a[204:207], a[36:39], v[64:67]\n"
    "v_exp_f32 v34, v34\n"
    "v_mfma_f32_16x16x32_bf16 v[68:71], a[204:207], a[60:63], v[68:71]\n"
    "v_fma_f32 v60, v60, s52, v12\n"
    "v_fma_f32 v61, v61, s52, v12\n"
    "v_mfma_f32_16x16x32_bf16 v[68:71], a[208:211], a[64:67], v[68:71]\n"
    "v_fma_f32 v62, v62, s52, v12\n"
    "v_fma_f32 v63, v63, s52, v12\n"
    "v_mfma_f32_16x16x32_bf16 v[68:71], a[212:215], a[68:71], v[68:71]\n"
    "v_exp_f32 v60, v60\n"
    "v_mfma_f32_16x16x32_bf16 v[72:75], a[212:215], a[92:95], v[72:75]\n"
    "ds_read_b64_tr_b16 a[212:213], v3 offset:12416\n"
    "v_fma_f32 v64, v64, s52, v13\n"
    "v_mfma_f32_16x16x32_bf16 v[72:75], a[208:211], a[88:91], v[72:75]\n"
    "ds_read_b64_tr_b16 a[208:209], v2 offset:12416\n"
    "v_fma_f32 v65, v65, s52, v13\n"
    "v_mfma_f32_16x16x32_bf16 v[72:75], a[204:207], a[84:87], v[72:75]\n"
    "ds_read_b64_tr_b16 a[204:205], v5 offset:12288\n"
    "v_fma_f32 v66, v66, s52, v13\n"
    "v_smfmac_f32_16x16x64_bf16 a[236:239], a[252:255], v[44:51], v23\n"
    "ds_read_b64_tr_b16 a[206:207], v5 offset:18432\n"
    "v_fma_f32 v67, v67, s52, v13\n"
    "v_smfmac_f32_16x16x64_bf16 a[240:243], a[252:255], v[52:59], v23\n"
    "s_waitcnt lgkmcnt(6)\n"
    "ds_read_b64_tr_b16 a[210:211], v2 offset:18560\n"
    "v_cvt_pk_bf16_f32 v32, v32, v33\n"
    "v_mfma_f32_16x16x32_bf16 v[96:99], a[192:195], v[56:59], v[96:99]\n"
    "ds_read_b64_tr_b16 a[214:215], v3 offset:18560\n"
    "v_fma_f32 v39, v39, s52, v14\n"
    "v_mfma_f32_16x16x32_bf16 v[92:95], a[192:195], v[52:55], v[92:95]\n"
    "v_exp_f32 v61, v61\n"
    "v_mfma_f32_16x16x32_bf16 v[88:91], a[192:195], v[48:51], v[88:91]\n"
    "v_exp_f32 v62, v62\n"
    "v_mfma_f32_16x16x32_bf16 v[84:87], a[192:195], v[44:47], v[84:87]\n"
    "s_waitcnt lgkmcnt(7)\n"
    "ds_read_b64_tr_b16 a[192:193], v4 offset:12416\n"
    "v_cvt_pk_bf16_f32 v30, v60, v61\n"
    "v_mfma_f32_16x16x32_bf16 a[216:219], a[196:199], v[44:47], a[216:219]\n"
    "buffer_load_dwordx4 v7, s[40:43], s55 offen offset:1024 nt lds\n"
    "v_mfma_f32_16x16x32_bf16 a[220:223], a[196:199], v[48:51], a[220:223]\n"
    "ds_read_b64_tr_b16 a[194:195], v4 offset:18560\n"
    "v_fma_f32 v42, v42, s52, v15\n"
    "v_mfma_f32_16x16x32_bf16 a[224:227], a[196:199], v[52:55], a[224:227]\n"
    "v_exp_f32 v63, v63\n"
    "v_mfma_f32_16x16x32_bf16 a[228:231], a[196:199], v[56:59], a[228:231]\n"
    "s_waitcnt vmcnt(21) lgkmcnt(8)\n"
    "ds_read_b64_tr_b16 a[196:197], v5 offset:12416\n"
    "v_cvt_pk_bf16_f32 v31, v62, v63\n"
    "v_mfma_f32_16x16x32_bf16 v[112:115], a[200:203], v[56:59], v[112:115]\n"
    "ds_read_b64_tr_b16 a[198:199], v5 offset:18560\n"
    "v_fma_f32 v43, v43, s52, v15\n"
    "v_mfma_f32_16x16x32_bf16 v[108:111], a[200:203], v[52:55], v[108:111]\n"
    "ds_read_b128 a[176:179], v10 offset:4096\n"
    "v_fma_f32 v68, v68, s52, v14\n"
    "v_mfma_f32_16x16x32_bf16 v[104:107], a[200:203], v[48:51], v[104:107]\n"
    "ds_read_b128 a[180:183], v11 offset:4096\n"
    "v_fma_f32 v69, v69, s52, v14\n"
    "v_mfma_f32_16x16x32_bf16 v[100:103], a[200:203], v[44:47], v[100:103]\n"
    "s_waitcnt lgkmcnt(8)\n"
    "ds_read_b64_tr_b16 a[200:201], v2 offset:12544\n"
    "v_fma_f32 v70, v70, s52, v14\n"
    "v_mfma_f32_16x16x32_bf16 v[116:119], a[204:207], v[44:47], v[116:119]\n"
    "ds_read_b64_tr_b16 a[202:203], v2 offset:18688\n"
    "v_fma_f32 v71, v71, s52, v14\n"
    "v_mfma_f32_16x16x32_bf16 v[120:123], a[204:207], v[48:51], v[120:123]\n"
    "v_exp_f32 v35, v35\n"
    "v_mfma_f32_16x16x32_bf16 v[124:127], a[204:207], v[52:55], v[124:127]\n"
    "v_exp_f32 v64, v64\n"
    "v_mfma_f32_16x16x32_bf16 v[128:131], a[204:207], v[56:59], v[128:131]\n"
    "s_waitcnt lgkmcnt(9)\n"
    "ds_read_b64_tr_b16 a[204:205], v3 offset:12544\n"
    "v_cvt_pk_bf16_f32 v33, v34, v35\n"
    "v_mfma_f32_16x16x32_bf16 v[144:147], a[208:211], v[56:59], v[144:147]\n"
    "ds_read_b64_tr_b16 a[206:207], v3 offset:18688\n"
    "v_fma_f32 v72, v72, s52, v15\n"
    "v_mfma_f32_16x16x32_bf16 v[140:143], a[208:211], v[52:55], v[140:143]\n"
    "v_exp_f32 v65, v65\n"
    "v_mfma_f32_16x16x32_bf16 v[136:139], a[208:211], v[48:51], v[136:139]\n"
    "buffer_load_dwordx4 v8, s[40:43], s55 offen offset:2048 nt lds\n"
    "v_mfma_f32_16x16x32_bf16 v[132:135], a[208:211], v[44:47], v[132:135]\n"
    "s_waitcnt lgkmcnt(10)\n"
    "s_add_u32 m0, s57, 0\n"
    "ds_read_b64_tr_b16 a[208:209], v4 offset:12544\n"
    "v_mfma_f32_16x16x32_bf16 a[232:235], a[212:215], v[44:47], a[232:235]\n"
    "s_add_u32 s55, s55, 24576\n"
    "buffer_load_dwordx4 v9, s[44:47], s63 offen offset:0 nt lds\n"
    "v_mfma_f32_16x16x32_bf16 v[148:151], a[212:215], v[48:51], v[148:151]\n"
    "s_add_u32 s63, s62, 18688\n"
    "buffer_load_dwordx4 v9, s[44:47], s64 offen offset:1024 nt lds\n"
    "v_mfma_f32_16x16x32_bf16 v[152:155], a[212:215], v[52:55], v[152:155]\n"
    "s_add_u32 m0, s54, 12288\n"
    "ds_read_b64_tr_b16 a[210:211], v4 offset:18688\n"
    "v_cvt_pk_bf16_f32 v34, v64, v65\n"
    "v_mfma_f32_16x16x32_bf16 v[156:159], a[212:215], v[56:59], v[156:159]\n"
    "s_waitcnt lgkmcnt(10)\n"
    "ds_read_b64_tr_b16 a[212:213], v5 offset:12544\n"
    "v_fma_f32 v73, v73, s52, v15\n"
    "v_mfma_f32_16x16x32_bf16 v[172:175], a[192:195], v[56:59], v[172:175]\n"
    "s_add_u32 s64, s62, 20736\n"
    "ds_read_b64_tr_b16 a[214:215], v5 offset:18688\n"
    "v_fma_f32 v74, v74, s52, v15\n"
    "v_mfma_f32_16x16x32_bf16 v[168:171], a[192:195], v[52:55], v[168:171]\n"
    "v_exp_f32 v66, v66\n"
    "v_mfma_f32_16x16x32_bf16 v[164:167], a[192:195], v[48:51], v[164:167]\n"
    "v_exp_f32 v67, v67\n"
    "v_mfma_f32_16x16x32_bf16 v[160:163], a[192:195], v[44:47], v[160:163]\n"
    "s_waitcnt lgkmcnt(10)\n"
    "v_cvt_pk_bf16_f32 v35, v66, v67\n"
    "v_fma_f32 v75, v75, s52, v15\n"
    "v_mfma_f32_16x16x32_bf16 v[176:179], a[196:199], v[44:47], v[176:179]\n"
    "v_exp_f32 v36, v36\n"
    "v_mfma_f32_16x16x32_bf16 v[180:183], a[196:199], v[48:51], v[180:183]\n"
    "v_exp_f32 v37, v37\n"
    "v_mfma_f32_16x16x32_bf16 v[184:187], a[196:199], v[52:55], v[184:187]\n"
    "s_waitcnt vmcnt(18) lgkmcnt(0)\n"
    "v_exp_f32 v40, v40\n"
    "v_mfma_f32_16x16x32_bf16 v[188:191], a[196:199], v[56:59], v[188:191]\n"
    "s_barrier\n"
    "ds_read_b128 a[192:195], v0 offset:36864\n"
    "v_cvt_pk_bf16_f32 v36, v36, v37\n"
    "v_mfma_f32_16x16x32_bf16 v[204:207], a[200:203], v[56:59], v[204:207]\n"
    "v_exp_f32 v41, v41\n"
    "v_mfma_f32_16x16x32_bf16 v[200:203], a[200:203], v[52:55], v[200:203]\n"
    "buffer_load_dwordx4 v6, s[36:39], s55 offen offset:0 nt lds\n"
    "v_mfma_f32_16x16x32_bf16 v[196:199], a[200:203], v[48:51], v[196:199]\n"
    "ds_read_b128 a[196:199], v1 offset:36864\n"
    "v_cvt_pk_bf16_f32 v40, v40, v41\n"
    "v_mfma_f32_16x16x32_bf16 v[192:195], a[200:203], v[44:47], v[192:195]\n"
    "v_exp_f32 v38, v38\n"
    "v_mfma_f32_16x16x32_bf16 v[208:211], a[204:207], v[44:47], v[208:211]\n"
    "v_exp_f32 v39, v39\n"
    "v_mfma_f32_16x16x32_bf16 v[212:215], a[204:207], v[48:51], v[212:215]\n"
    "ds_read_b128 a[200:203], v0 offset:36992\n"
    "v_cvt_pk_bf16_f32 v37, v38, v39\n"
    "v_mfma_f32_16x16x32_bf16 v[216:219], a[204:207], v[52:55], v[216:219]\n"
    "v_exp_f32 v42, v42\n"
    "v_mfma_f32_16x16x32_bf16 v[220:223], a[204:207], v[56:59], v[220:223]\n"
    "v_exp_f32 v43, v43\n"
    "v_mfma_f32_16x16x32_bf16 v[236:239], a[208:211], v[56:59], v[236:239]\n"
    "ds_read_b128 a[204:207], v1 offset:36992\n"
    "v_cvt_pk_bf16_f32 v41, v42, v43\n"
    "v_mfma_f32_16x16x32_bf16 v[232:235], a[208:211], v[52:55], v[232:235]\n"
    "v_exp_f32 v68, v68\n"
    "v_mfma_f32_16x16x32_bf16 v[228:231], a[208:211], v[48:51], v[228:231]\n"
    "v_exp_f32 v69, v69\n"
    "v_mfma_f32_16x16x32_bf16 v[224:227], a[208:211], v[44:47], v[224:227]\n"
    "ds_read_b128 a[208:211], v0 offset:37120\n"
    "v_cvt_pk_bf16_f32 v38, v68, v69\n"
    "v_mfma_f32_16x16x32_bf16 v[240:243], a[212:215], v[44:47], v[240:243]\n"
    "v_exp_f32 v70, v70\n"
    "v_mfma_f32_16x16x32_bf16 v[244:247], a[212:215], v[48:51], v[244:247]\n"
    "v_exp_f32 v71, v71\n"
    "v_mfma_f32_16x16x32_bf16 v[248:251], a[212:215], v[52:55], v[248:251]\n"
    "v_exp_f32 v72, v72\n"
    "v_mfma_f32_16x16x32_bf16 v[252:255], a[212:215], v[56:59], v[252:255]\n"
    "s_waitcnt lgkmcnt(4)\n"
    "buffer_load_dwordx4 v7, s[36:39], s55 offen offset:1024 nt lds\n"
    "v_mfma_f32_16x16x32_bf16 v[44:47], a[192:195], a[0:3], 0\n"
    "s_waitcnt lgkmcnt(3)\n"
    "ds_read_b128 a[212:215], v1 offset:37120\n"
    "v_cvt_pk_bf16_f32 v39, v70, v71\n"
    "v_mfma_f32_16x16x32_bf16 v[44:47], a[196:199], a[4:7], v[44:47]\n"
    "s_waitcnt lgkmcnt(3)\n"
    "v_exp_f32 v73, v73\n"
    "v_mfma_f32_16x16x32_bf16 v[44:47], a[200:203], a[8:11], v[44:47]\n"
    "v_exp_f32 v74, v74\n"
    "v_mfma_f32_16x16x32_bf16 v[48:51], a[200:203], a[32:35], 0\n"
    "v_exp_f32 v75, v75\n"
    "v_mfma_f32_16x16x32_bf16 v[48:51], a[196:199], a[28:31], v[48:51]\n"
    "v_cvt_pk_bf16_f32 v42, v72, v73\n"
    "v_cvt_pk_bf16_f32 v43, v74, v75\n"
    "v_mfma_f32_16x16x32_bf16 v[48:51], a[192:195], a[24:27], v[48:51]\n"
    "v_mfma_f32_16x16x32_bf16 v[52:55], a[192:195], a[48:51], 0\n"
    "v_mfma_f32_16x16x32_bf16 v[52:55], a[196:199], a[52:55], v[52:55]\n"
    "v_mfma_f32_16x16x32_bf16 v[52:55], a[200:203], a[56:59], v[52:55]\n"
    "v_mfma_f32_16x16x32_bf16 v[56:59], a[200:203], a[80:83], 0\n"
    "ds_read_b128 a[200:203], v0 offset:43136\n"
    "v_mfma_f32_16x16x32_bf16 v[56:59], a[196:199], a[76:79], v[56:59]\n"
    "ds_read_b128 a[196:199], v1 offset:43008\n"
    "v_mfma_f32_16x16x32_bf16 v[56:59], a[192:195], a[72:75], v[56:59]\n"
    "s_waitcnt lgkmcnt(4)\n"
    "ds_read_b128 a[192:195], v0 offset:43008\n"
    "v_mfma_f32_16x16x32_bf16 v[44:47], a[204:207], a[12:15], v[44:47]\n"
    "s_waitcnt lgkmcnt(4)\n"
    "v_mfma_f32_16x16x32_bf16 v[44:47], a[208:211], a[16:19], v[44:47]\n"
    "s_waitcnt lgkmcnt(3)\n"
    "buffer_load_dwordx4 v8, s[36:39], s55 offen offset:2048 nt lds\n"
    "v_mfma_f32_16x16x32_bf16 v[44:47], a[212:215], a[20:23], v[44:47]\n"
    "s_add_u32 m0, s57, 2048\n"
    "v_mfma_f32_16x16x32_bf16 v[48:51], a[212:215], a[44:47], v[48:51]\n"
    "buffer_load_dwordx4 v9, s[44:47], s63 offen offset:0 nt lds\n"
    "v_mfma_f32_16x16x32_bf16 v[48:51], a[208:211], a[40:43], v[48:51]\n"
    "buffer_load_dwordx4 v9, s[44:47], s64 offen offset:1024 nt lds\n"
    "v_mfma_f32_16x16x32_bf16 v[48:51], a[204:207], a[36:39], v[48:51]\n"
    "s_add_u32 m0, s54, 61440\n"
    "v_mfma_f32_16x16x32_bf16 v[52:55], a[204:207], a[60:63], v[52:55]\n"
    "v_fma_f32 v44, v44, s52, v12\n"
    "v_fma_f32 v45, v45, s52, v12\n"
    "v_mfma_f32_16x16x32_bf16 v[52:55], a[208:211], a[64:67], v[52:55]\n"
    "v_exp_f32 v44, v44\n"
    "v_mfma_f32_16x16x32_bf16 v[52:55], a[212:215], a[68:71], v[52:55]\n"
    "v_exp_f32 v45, v45\n"
    "v_mfma_f32_16x16x32_bf16 v[56:59], a[212:215], a[92:95], v[56:59]\n"
    "ds_read_b128 a[212:215], v1 offset:43264\n"
    "v_fma_f32 v46, v46, s52, v12\n"
    "v_mfma_f32_16x16x32_bf16 v[56:59], a[208:211], a[88:91], v[56:59]\n"
    "ds_read_b128 a[208:211], v0 offset:43264\n"
    "v_fma_f32 v47, v47, s52, v12\n"
    "v_mfma_f32_16x16x32_bf16 v[56:59], a[204:207], a[84:87], v[56:59]\n"
    "s_waitcnt lgkmcnt(2)\n"
    "ds_read_b128 a[204:207], v1 offset:43136\n"
    "v_cvt_pk_bf16_f32 v44, v44, v45\n"
    "v_mfma_f32_16x16x32_bf16 v[76:79], a[192:195], a[0:3], 0\n"
    "v_exp_f32 v46, v46\n"
    "v_mfma_f32_16x16x32_bf16 v[76:79], a[196:199], a[4:7], v[76:79]\n"
    "v_exp_f32 v47, v47\n"
    "v_mfma_f32_16x16x32_bf16 v[76:79], a[200:203], a[8:11], v[76:79]\n"
    "v_cvt_pk_bf16_f32 v45, v46, v47\n"
    "v_fma_f32 v48, v48, s52, v13\n"
    "v_mfma_f32_16x16x32_bf16 v[80:83], a[200:203], a[32:35], 0\n"
    "v_fma_f32 v49, v49, s52, v13\n"
    "v_fma_f32 v50, v50, s52, v13\n"
    "v_mfma_f32_16x16x32_bf16 v[80:83], a[196:199], a[28:31], v[80:83]\n"
    "buffer_load_dwordx4 v6, s[40:43], s56 offen offset:0 nt lds\n"
    "v_mfma_f32_16x16x32_bf16 v[80:83], a[192:195], a[24:27], v[80:83]\n"
    "v_exp_f32 v48, v48\n"
    "v_mfma_f32_16x16x32_bf16 v[60:63], a[192:195], a[48:51], 0\n"
    "v_exp_f32 v49, v49\n"
    "v_mfma_f32_16x16x32_bf16 v[60:63], a[196:199], a[52:55], v[60:63]\n"
    "v_fma_f32 v51, v51, s52, v13\n"
    "v_cvt_pk_bf16_f32 v48, v48, v49\n"
    "v_mfma_f32_16x16x32_bf16 v[60:63], a[200:203], a[56:59], v[60:63]\n"
    "v_exp_f32 v50, v50\n"
    "v_mfma_f32_16x16x32_bf16 v[64:67], a[200:203], a[80:83], 0\n"
    "ds_read_b64_tr_b16 a[200:201], v4 offset:24576\n"
    "v_fma_f32 v52, v52, s52, v14\n"
    "v_mfma_f32_16x16x32_bf16 v[64:67], a[196:199], a[76:79], v[64:67]\n"
    "ds_read_b64_tr_b16 a[196:197], v3 offset:24576\n"
    "v_fma_f32 v53, v53, s52, v14\n"
    "v_mfma_f32_16x16x32_bf16 v[64:67], a[192:195], a[72:75], v[64:67]\n"
    "s_waitcnt lgkmcnt(2)\n"
    "ds_read_b64_tr_b16 a[192:193], v2 offset:24576\n"
    "v_fma_f32 v56, v56, s52, v15\n"
    "v_mfma_f32_16x16x32_bf16 v[76:79], a[204:207], a[12:15], v[76:79]\n"
    "ds_read_b64_tr_b16 a[194:195], v2 offset:30720\n"
    "v_fma_f32 v57, v57, s52, v15\n"
    "v_mfma_f32_16x16x32_bf16 v[76:79], a[208:211], a[16:19], v[76:79]\n"
    "s_waitcnt vmcnt(22)\n"
    "ds_read_b64_tr_b16 a[198:199], v3 offset:30720\n"
    "v_fma_f32 v54, v54, s52, v14\n"
    "v_mfma_f32_16x16x32_bf16 v[76:79], a[212:215], a[20:23], v[76:79]\n"
    "ds_read_b64_tr_b16 a[202:203], v4 offset:30720\n"
    "v_fma_f32 v55, v55, s52, v14\n"
    "v_mfma_f32_16x16x32_bf16 v[80:83], a[212:215], a[44:47], v[80:83]\n"
    "ds_read_b128 a[112:115], v10 offset:6144\n"
    "v_fma_f32 v58, v58, s52, v15\n"
    "v_mfma_f32_16x16x32_bf16 v[80:83], a[208:211], a[40:43], v[80:83]\n"
    "ds_read_b128 a[116:119], v11 offset:6144\n"
    "v_fma_f32 v59, v59, s52, v15\n"
    "v_mfma_f32_16x16x32_bf16 v[80:83], a[204:207], a[36:39], v[80:83]\n"
    "v_fma_f32 v76, v76, s52, v12\n"
    "v_fma_f32 v77, v77, s52, v12\n"
    "v_mfma_f32_16x16x32_bf16 v[60:63], a[204:207], a[60:63], v[60:63]\n"
    "buffer_load_dwordx4 v7, s[40:43], s56 offen offset:1024 nt lds\n"
    "v_mfma_f32_16x16x32_bf16 v[60:63], a[208:211], a[64:67], v[60:63]\n"
    "v_fma_f32 v78, v78, s52, v12\n"
    "v_fma_f32 v79, v79, s52, v12\n"
    "v_mfma_f32_16x16x32_bf16 v[60:63], a[212:215], a[68:71], v[60:63]\n"
    "v_exp_f32 v76, v76\n"
    "v_mfma_f32_16x16x32_bf16 v[64:67], a[212:215], a[92:95], v[64:67]\n"
    "ds_read_b64_tr_b16 a[212:213], v3 offset:24704\n"
    "v_fma_f32 v80, v80, s52, v13\n"
    "v_mfma_f32_16x16x32_bf16 v[64:67], a[208:211], a[88:91], v[64:67]\n"
    "ds_read_b64_tr_b16 a[208:209], v2 offset:24704\n"
    "v_fma_f32 v81, v81, s52, v13\n"
    "v_mfma_f32_16x16x32_bf16 v[64:67], a[204:207], a[84:87], v[64:67]\n"
    "ds_read_b64_tr_b16 a[204:205], v5 offset:24576\n"
    "v_fma_f32 v82, v82, s52, v13\n"
    "v_smfmac_f32_16x16x64_bf16 a[236:239], a[252:255], v[28:35], v23\n"
    "ds_read_b64_tr_b16 a[206:207], v5 offset:30720\n"
    "v_fma_f32 v83, v83, s52, v13\n"
    "v_smfmac_f32_16x16x64_bf16 a[240:243], a[252:255], v[36:43], v23\n"
    "s_waitcnt lgkmcnt(8)\n"
    "ds_read_b64_tr_b16 a[210:211], v2 offset:30848\n"
    "v_fma_f32 v60, v60, s52, v14\n"
    "v_mfma_f32_16x16x32_bf16 v[96:99], a[192:195], v[40:43], v[96:99]\n"
    "ds_read_b64_tr_b16 a[214:215], v3 offset:30848\n"
    "v_fma_f32 v61, v61, s52, v14\n"
    "v_mfma_f32_16x16x32_bf16 v[92:95], a[192:195], v[36:39], v[92:95]\n"
    "v_exp_f32 v77, v77\n"
    "v_mfma_f32_16x16x32_bf16 v[88:91], a[192:195], v[32:35], v[88:91]\n"
    "v_exp_f32 v78, v78\n"
    "v_mfma_f32_16x16x32_bf16 v[84:87], a[192:195], v[28:31], v[84:87]\n"
    "s_waitcnt lgkmcnt(9)\n"
    "ds_read_b64_tr_b16 a[192:193], v4 offset:24704\n"
    "v_cvt_pk_bf16_f32 v46, v76, v77\n"
    "v_mfma_f32_16x16x32_bf16 a[216:219], a[196:199], v[28:31], a[216:219]\n"
    "ds_read_b64_tr_b16 a[194:195], v4 offset:30848\n"
    "v_fma_f32 v62, v62, s52, v14\n"
    "v_mfma_f32_16x16x32_bf16 a[220:223], a[196:199], v[32:35], a[220:223]\n"
    "v_exp_f32 v79, v79\n"
    "v_mfma_f32_16x16x32_bf16 a[224:227], a[196:199], v[36:39], a[224:227]\n"
    "buffer_load_dwordx4 v8, s[40:43], s56 offen offset:2048 nt lds\n"
    "v_mfma_f32_16x16x32_bf16 a[228:231], a[196:199], v[40:43], a[228:231]\n"
    "s_waitcnt lgkmcnt(10)\n"
    "ds_read_b64_tr_b16 a[196:197], v5 offset:24704\n"
    "v_cvt_pk_bf16_f32 v47, v78, v79\n"
    "v_mfma_f32_16x16x32_bf16 v[112:115], a[200:203], v[40:43], v[112:115]\n"
    "s_add_u32 s56, s56, 24576\n"
    "ds_read_b64_tr_b16 a[198:199], v5 offset:30848\n"
    "v_fma_f32 v63, v63, s52, v14\n"
    "v_mfma_f32_16x16x32_bf16 v[108:111], a[200:203], v[36:39], v[108:111]\n"
    "s_add_u32 m0, s54, 24576\n"
    "v_exp_f32 v51, v51\n"
    "v_mfma_f32_16x16x32_bf16 v[104:107], a[200:203], v[32:35], v[104:107]\n"
    "v_exp_f32 v80, v80\n"
    "v_mfma_f32_16x16x32_bf16 v[100:103], a[200:203], v[28:31], v[100:103]\n"
    "s_waitcnt lgkmcnt(6)\n"
    "ds_read_b64_tr_b16 a[200:201], v2 offset:24832\n"
    "v_cvt_pk_bf16_f32 v49, v50, v51\n"
    "v_mfma_f32_16x16x32_bf16 v[116:119], a[204:207], v[28:31], v[116:119]\n"
    "ds_read_b64_tr_b16 a[202:203], v2 offset:30976\n"
    "v_fma_f32 v64, v64, s52, v15\n"
    "v_mfma_f32_16x16x32_bf16 v[120:123], a[204:207], v[32:35], v[120:123]\n"
    "v_exp_f32 v81, v81\n"
    "v_mfma_f32_16x16x32_bf16 v[124:127], a[204:207], v[36:39], v[124:127]\n"
    "v_exp_f32 v82, v82\n"
    "v_mfma_f32_16x16x32_bf16 v[128:131], a[204:207], v[40:43], v[128:131]\n"
    "s_waitcnt lgkmcnt(7)\n"
    "ds_read_b64_tr_b16 a[204:205], v3 offset:24832\n"
    "v_cvt_pk_bf16_f32 v50, v80, v81\n"
    "v_mfma_f32_16x16x32_bf16 v[144:147], a[208:211], v[40:43], v[144:147]\n"
    "ds_read_b64_tr_b16 a[206:207], v3 offset:30976\n"
    "v_fma_f32 v65, v65, s52, v15\n"
    "v_mfma_f32_16x16x32_bf16 v[140:143], a[208:211], v[36:39], v[140:143]\n"
    "v_exp_f32 v83, v83\n"
    "v_mfma_f32_16x16x32_bf16 v[136:139], a[208:211], v[32:35], v[136:139]\n"
    "v_cvt_pk_bf16_f32 v51, v82, v83\n"
    "v_fma_f32 v66, v66, s52, v15\n"
    "v_mfma_f32_16x16x32_bf16 v[132:135], a[208:211], v[28:31], v[132:135]\n"
    "s_waitcnt lgkmcnt(8)\n"
    "ds_read_b64_tr_b16 a[208:209], v4 offset:24832\n"
    "v_fma_f32 v67, v67, s52, v15\n"
    "v_mfma_f32_16x16x32_bf16 a[232:235], a[212:215], v[28:31], a[232:235]\n"
    "v_exp_f32 v52, v52\n"
    "v_mfma_f32_16x16x32_bf16 v[148:151], a[212:215], v[32:35], v[148:151]\n"
    "buffer_load_dwordx4 v6, s[36:39], s56 offen offset:0 nt lds\n"
    "v_mfma_f32_16x16x32_bf16 v[152:155], a[212:215], v[36:39], v[152:155]\n"
    "v_exp_f32 v53, v53\n"
    "v_mfma_f32_16x16x32_bf16 v[156:159], a[212:215], v[40:43], v[156:159]\n"
    "s_waitcnt lgkmcnt(7)\n"
    "ds_read_b64_tr_b16 a[210:211], v4 offset:30976\n"
    "v_cvt_pk_bf16_f32 v52, v52, v53\n"
    "v_mfma_f32_16x16x32_bf16 v[172:175], a[192:195], v[40:43], v[172:175]\n"
    "s_nop 0\n"
    "ds_read_b64_tr_b16 a[212:213], v5 offset:24832\n"
    "ds_read_b64_tr_b16 a[214:215], v5 offset:30976\n"
    "v_mfma_f32_16x16x32_bf16 v[168:171], a[192:195], v[36:39], v[168:171]\n"
    "v_exp_f32 v56, v56\n"
    "v_mfma_f32_16x16x32_bf16 v[164:167], a[192:195], v[32:35], v[164:167]\n"
    "v_exp_f32 v57, v57\n"
    "v_mfma_f32_16x16x32_bf16 v[160:163], a[192:195], v[28:31], v[160:163]\n"
    "s_waitcnt lgkmcnt(8)\n"
    "v_exp_f32 v54, v54\n"
    "v_mfma_f32_16x16x32_bf16 v[176:179], a[196:199], v[28:31], v[176:179]\n"
    "v_exp_f32 v55, v55\n"
    "v_mfma_f32_16x16x32_bf16 v[180:183], a[196:199], v[32:35], v[180:183]\n"
    "v_exp_f32 v58, v58\n"
    "v_mfma_f32_16x16x32_bf16 v[184:187], a[196:199], v[36:39], v[184:187]\n"
    "s_waitcnt vmcnt(19) lgkmcnt(0)\n"
    "v_exp_f32 v59, v59\n"
    "v_mfma_f32_16x16x32_bf16 v[188:191], a[196:199], v[40:43], v[188:191]\n"
    "s_barrier\n"
    "ds_read_b128 a[192:195], v0 offset:49152\n"
    "v_cvt_pk_bf16_f32 v56, v56, v57\n"
    "v_mfma_f32_16x16x32_bf16 v[204:207], a[200:203], v[40:43], v[204:207]\n"
    "ds_read_b128 a[196:199], v1 offset:49152\n"
    "v_cvt_pk_bf16_f32 v53, v54, v55\n"
    "v_mfma_f32_16x16x32_bf16 v[200:203], a[200:203], v[36:39], v[200:203]\n"
    "v_exp_f32 v60, v60\n"
    "v_mfma_f32_16x16x32_bf16 v[196:199], a[200:203], v[32:35], v[196:199]\n"
    "v_exp_f32 v61, v61\n"
    "v_mfma_f32_16x16x32_bf16 v[192:195], a[200:203], v[28:31], v[192:195]\n"
    "buffer_load_dwordx4 v7, s[36:39], s56 offen offset:1024 nt lds\n"
    "v_mfma_f32_16x16x32_bf16 v[208:211], a[204:207], v[28:31], v[208:211]\n"
    "ds_read_b128 a[200:203], v0 offset:49280\n"
    "v_cvt_pk_bf16_f32 v57, v58, v59\n"
    "v_mfma_f32_16x16x32_bf16 v[212:215], a[204:207], v[32:35], v[212:215]\n"
    "v_exp_f32 v62, v62\n"
    "v_mfma_f32_16x16x32_bf16 v[216:219], a[204:207], v[36:39], v[216:219]\n"
    "v_exp_f32 v63, v63\n"
    "v_mfma_f32_16x16x32_bf16 v[220:223], a[204:207], v[40:43], v[220:223]\n"
    "ds_read_b128 a[204:207], v1 offset:49280\n"
    "v_cvt_pk_bf16_f32 v54, v60, v61\n"
    "v_mfma_f32_16x16x32_bf16 v[236:239], a[208:211], v[40:43], v[236:239]\n"
    "v_exp_f32 v64, v64\n"
    "v_mfma_f32_16x16x32_bf16 v[232:235], a[208:211], v[36:39], v[232:235]\n"
    "v_exp_f32 v65, v65\n"
    "v_mfma_f32_16x16x32_bf16 v[228:231], a[208:211], v[32:35], v[228:231]\n"
    "v_exp_f32 v66, v66\n"
    "v_mfma_f32_16x16x32_bf16 v[224:227], a[208:211], v[28:31], v[224:227]\n"
    "ds_read_b128 a[208:211], v0 offset:49408\n"
    "v_cvt_pk_bf16_f32 v55, v62, v63\n"
    "v_mfma_f32_16x16x32_bf16 v[240:243], a[212:215], v[28:31], v[240:243]\n"
    "v_exp_f32 v67, v67\n"
    "v_mfma_f32_16x16x32_bf16 v[244:247], a[212:215], v[32:35], v[244:247]\n"
    "v_cvt_pk_bf16_f32 v58, v64, v65\n"
    "v_cvt_pk_bf16_f32 v59, v66, v67\n"
    "v_mfma_f32_16x16x32_bf16 v[248:251], a[212:215], v[36:39], v[248:251]\n"
    "v_mfma_f32_16x16x32_bf16 v[252:255], a[212:215], v[40:43], v[252:255]\n"
    "s_waitcnt lgkmcnt(4)\n"
    "ds_read_b128 a[212:215], v1 offset:49408\n"
    "v_mfma_f32_16x16x32_bf16 v[28:31], a[192:195], a[0:3], 0\n"
    "s_waitcnt lgkmcnt(4)\n"
    "v_mfma_f32_16x16x32_bf16 v[28:31], a[196:199], a[4:7], v[28:31]\n"
    "s_waitcnt lgkmcnt(3)\n"
    "buffer_load_dwordx4 v8, s[36:39], s56 offen offset:2048 nt lds\n"
    "v_mfma_f32_16x16x32_bf16 v[28:31], a[200:203], a[8:11], v[28:31]\n"
    "s_add_u32 m0, s54, 73728\n"
    "v_mfma_f32_16x16x32_bf16 v[32:35], a[200:203], a[32:35], 0\n"
    "v_mfma_f32_16x16x32_bf16 v[32:35], a[196:199], a[28:31], v[32:35]\n"
    "v_mfma_f32_16x16x32_bf16 v[32:35], a[192:195], a[24:27], v[32:35]\n"
    "v_mfma_f32_16x16x32_bf16 v[36:39], a[192:195], a[48:51], 0\n"
    "v_mfma_f32_16x16x32_bf16 v[36:39], a[196:199], a[52:55], v[36:39]\n"
    "v_mfma_f32_16x16x32_bf16 v[36:39], a[200:203], a[56:59], v[36:39]\n"
    "v_mfma_f32_16x16x32_bf16 v[40:43], a[200:203], a[80:83], 0\n"
    "ds_read_b128 a[200:203], v0 offset:55424\n"
    "v_mfma_f32_16x16x32_bf16 v[40:43], a[196:199], a[76:79], v[40:43]\n"
    "ds_read_b128 a[196:199], v1 offset:55296\n"
    "v_mfma_f32_16x16x32_bf16 v[40:43], a[192:195], a[72:75], v[40:43]\n"
    "s_waitcnt lgkmcnt(4)\n"
    "ds_read_b128 a[192:195], v0 offset:55296\n"
    "v_mfma_f32_16x16x32_bf16 v[28:31], a[204:207], a[12:15], v[28:31]\n"
    "s_waitcnt lgkmcnt(4)\n"
    "v_mfma_f32_16x16x32_bf16 v[28:31], a[208:211], a[16:19], v[28:31]\n"
    "s_waitcnt lgkmcnt(3)\n"
    "v_mfma_f32_16x16x32_bf16 v[28:31], a[212:215], a[20:23], v[28:31]\n"
    "v_mfma_f32_16x16x32_bf16 v[32:35], a[212:215], a[44:47], v[32:35]\n"
    "v_mfma_f32_16x16x32_bf16 v[32:35], a[208:211], a[40:43], v[32:35]\n"
    "buffer_load_dwordx4 v6, s[40:43], s55 offen offset:0 nt lds\n"
    "v_mfma_f32_16x16x32_bf16 v[32:35], a[204:207], a[36:39], v[32:35]\n"
    "s_waitcnt vmcnt(20)\n"
    "v_mfma_f32_16x16x32_bf16 v[36:39], a[204:207], a[60:63], v[36:39]\n"
    "s_nop 0\n"
    "ds_read_b128 a[136:139], v10 offset:8192\n"
    "ds_read_b128 a[140:143], v11 offset:8192\n"
    "v_mfma_f32_16x16x32_bf16 v[36:39], a[208:211], a[64:67], v[36:39]\n"
    "v_fma_f32 v28, v28, s52, v12\n"
    "v_fma_f32 v29, v29, s52, v12\n"
    "v_mfma_f32_16x16x32_bf16 v[36:39], a[212:215], a[68:71], v[36:39]\n"
    "v_exp_f32 v28, v28\n"
    "v_mfma_f32_16x16x32_bf16 v[40:43], a[212:215], a[92:95], v[40:43]\n"
    "ds_read_b128 a[212:215], v1 offset:55552\n"
    "v_fma_f32 v30, v30, s52, v12\n"
    "v_mfma_f32_16x16x32_bf16 v[40:43], a[208:211], a[88:91], v[40:43]\n"
    "ds_read_b128 a[208:211], v0 offset:55552\n"
    "v_fma_f32 v31, v31, s52, v12\n"
    "v_mfma_f32_16x16x32_bf16 v[40:43], a[204:207], a[84:87], v[40:43]\n"
    "s_waitcnt lgkmcnt(4)\n"
    "ds_read_b128 a[204:207], v1 offset:55424\n"
    "v_fma_f32 v32, v32, s52, v13\n"
    "v_mfma_f32_16x16x32_bf16 v[68:71], a[192:195], a[0:3], 0\n"
    "v_exp_f32 v29, v29\n"
    "v_mfma_f32_16x16x32_bf16 v[68:71], a[196:199], a[4:7], v[68:71]\n"
    "v_exp_f32 v30, v30\n"
    "v_mfma_f32_16x16x32_bf16 v[68:71], a[200:203], a[8:11], v[68:71]\n"
    "v_exp_f32 v31, v31\n"
    "v_mfma_f32_16x16x32_bf16 v[72:75], a[200:203], a[32:35], 0\n"
    "v_cvt_pk_bf16_f32 v28, v28, v29\n"
    "v_fma_f32 v33, v33, s52, v13\n"
    "v_mfma_f32_16x16x32_bf16 v[72:75], a[196:199], a[28:31], v[72:75]\n"
    "v_cvt_pk_bf16_f32 v29, v30, v31\n"
    "v_fma_f32 v34, v34, s52, v13\n"
    "v_mfma_f32_16x16x32_bf16 v[72:75], a[192:195], a[24:27], v[72:75]\n"
    "v_exp_f32 v32, v32\n"
    "v_mfma_f32_16x16x32_bf16 v[76:79], a[192:195], a[48:51], 0\n"
    "buffer_load_dwordx4 v7, s[40:43], s55 offen offset:1024 nt lds\n"
    "v_mfma_f32_16x16x32_bf16 v[76:79], a[196:199], a[52:55], v[76:79]\n"
    "v_exp_f32 v33, v33\n"
    "v_mfma_f32_16x16x32_bf16 v[76:79], a[200:203], a[56:59], v[76:79]\n"
    "v_fma_f32 v35, v35, s52, v13\n"
    "v_cvt_pk_bf16_f32 v32, v32, v33\n"
    "v_mfma_f32_16x16x32_bf16 v[80:83], a[200:203], a[80:83], 0\n"
    "ds_read_b64_tr_b16 a[200:201], v4 offset:36864\n"
    "v_fma_f32 v36, v36, s52, v14\n"
    "v_mfma_f32_16x16x32_bf16 v[80:83], a[196:199], a[76:79], v[80:83]\n"
    "ds_read_b64_tr_b16 a[196:197], v3 offset:36864\n"
    "v_fma_f32 v37, v37, s52, v14\n"
    "v_mfma_f32_16x16x32_bf16 v[80:83], a[192:195], a[72:75], v[80:83]\n"
    "s_waitcnt lgkmcnt(2)\n"
    "ds_read_b64_tr_b16 a[192:193], v2 offset:36864\n"
    "v_fma_f32 v40, v40, s52, v15\n"
    "v_mfma_f32_16x16x32_bf16 v[68:71], a[204:207], a[12:15], v[68:71]\n"
    "ds_read_b64_tr_b16 a[194:195], v2 offset:43008\n"
    "v_fma_f32 v41, v41, s52, v15\n"
    "v_mfma_f32_16x16x32_bf16 v[68:71], a[208:211], a[16:19], v[68:71]\n"
    "ds_read_b64_tr_b16 a[198:199], v3 offset:43008\n"
    "v_fma_f32 v38, v38, s52, v14\n"
    "v_mfma_f32_16x16x32_bf16 v[68:71], a[212:215], a[20:23], v[68:71]\n"
    "ds_read_b64_tr_b16 a[202:203], v4 offset:43008\n"
    "v_fma_f32 v39, v39, s52, v14\n"
    "v_mfma_f32_16x16x32_bf16 v[72:75], a[212:215], a[44:47], v[72:75]\n"
    "v_exp_f32 v34, v34\n"
    "v_mfma_f32_16x16x32_bf16 v[72:75], a[208:211], a[40:43], v[72:75]\n"
    "v_exp_f32 v35, v35\n"
    "v_mfma_f32_16x16x32_bf16 v[72:75], a[204:207], a[36:39], v[72:75]\n"
    "v_cvt_pk_bf16_f32 v33, v34, v35\n"
    "v_fma_f32 v68, v68, s52, v12\n"
    "v_mfma_f32_16x16x32_bf16 v[76:79], a[204:207], a[60:63], v[76:79]\n"
    "v_fma_f32 v69, v69, s52, v12\n"
    "v_fma_f32 v70, v70, s52, v12\n"
    "v_mfma_f32_16x16x32_bf16 v[76:79], a[208:211], a[64:67], v[76:79]\n"
    "v_fma_f32 v71, v71, s52, v12\n"
    "v_fma_f32 v42, v42, s52, v15\n"
    "v_mfma_f32_16x16x32_bf16 v[76:79], a[212:215], a[68:71], v[76:79]\n"
    "buffer_load_dwordx4 v8, s[40:43], s55 offen offset:2048 nt lds\n"
    "v_mfma_f32_16x16x32_bf16 v[80:83], a[212:215], a[92:95], v[80:83]\n"
    "s_add_u32 s55, s55, 24576\n"
    "ds_read_b64_tr_b16 a[212:213], v3 offset:36992\n"
    "v_fma_f32 v72, v72, s52, v13\n"
    "v_mfma_f32_16x16x32_bf16 v[80:83], a[208:211], a[88:91], v[80:83]\n"
    "s_add_u32 m0, s54, 36864\n"
    "ds_read_b64_tr_b16 a[208:209], v2 offset:36992\n"
    "v_fma_f32 v73, v73, s52, v13\n"
    "v_mfma_f32_16x16x32_bf16 v[80:83], a[204:207], a[84:87], v[80:83]\n"
    "ds_read_b64_tr_b16 a[204:205], v5 offset:36864\n"
    "v_fma_f32 v74, v74, s52, v13\n"
    "v_smfmac_f32_16x16x64_bf16 a[236:239], a[252:255], v[44:51], v23\n"
    "ds_read_b64_tr_b16 a[206:207], v5 offset:43008\n"
    "v_fma_f32 v75, v75, s52, v13\n"
    "v_smfmac_f32_16x16x64_bf16 a[240:243], a[252:255], v[52:59], v23\n"
    "s_waitcnt lgkmcnt(6)\n"
    "ds_read_b64_tr_b16 a[210:211], v2 offset:43136\n"
    "v_fma_f32 v43, v43, s52, v15\n"
    "v_mfma_f32_16x16x32_bf16 v[96:99], a[192:195], v[56:59], v[96:99]\n"
    "ds_read_b64_tr_b16 a[214:215], v3 offset:43136\n"
    "v_fma_f32 v76, v76, s52, v14\n"
    "v_mfma_f32_16x16x32_bf16 v[92:95], a[192:195], v[52:55], v[92:95]\n"
    "v_exp_f32 v68, v68\n"
    "v_mfma_f32_16x16x32_bf16 v[88:91], a[192:195], v[48:51], v[88:91]\n"
    "v_exp_f32 v69, v69\n"
    "v_mfma_f32_16x16x32_bf16 v[84:87], a[192:195], v[44:47], v[84:87]\n"
    "s_waitcnt lgkmcnt(7)\n"
    "ds_read_b64_tr_b16 a[192:193], v4 offset:36992\n"
    "v_cvt_pk_bf16_f32 v30, v68, v69\n"
    "v_mfma_f32_16x16x32_bf16 a[216:219], a[196:199], v[44:47], a[216:219]\n"
    "ds_read_b64_tr_b16 a[194:195], v4 offset:43136\n"
    "v_fma_f32 v77, v77, s52, v14\n"
    "v_mfma_f32_16x16x32_bf16 a[220:223], a[196:199], v[48:51], a[220:223]\n"
    "v_exp_f32 v70, v70\n"
    "v_mfma_f32_16x16x32_bf16 a[224:227], a[196:199], v[52:55], a[224:227]\n"
    "v_exp_f32 v71, v71\n"
    "v_mfma_f32_16x16x32_bf16 a[228:231], a[196:199], v[56:59], a[228:231]\n"
    "s_waitcnt lgkmcnt(8)\n"
    "ds_read_b64_tr_b16 a[196:197], v5 offset:36992\n"
    "v_cvt_pk_bf16_f32 v31, v70, v71\n"
    "v_mfma_f32_16x16x32_bf16 v[112:115], a[200:203], v[56:59], v[112:115]\n"
    "ds_read_b64_tr_b16 a[198:199], v5 offset:43136\n"
    "v_fma_f32 v78, v78, s52, v14\n"
    "v_mfma_f32_16x16x32_bf16 v[108:111], a[200:203], v[52:55], v[108:111]\n"
    "buffer_load_dwordx4 v6, s[36:39], s55 offen offset:0 nt lds\n"
    "v_mfma_f32_16x16x32_bf16 v[104:107], a[200:203], v[48:51], v[104:107]\n"
    "v_exp_f32 v72, v72\n"
    "v_mfma_f32_16x16x32_bf16 v[100:103], a[200:203], v[44:47], v[100:103]\n"
    "s_waitcnt lgkmcnt(6)\n"
    "ds_read_b64_tr_b16 a[200:201], v2 offset:37120\n"
    "v_fma_f32 v79, v79, s52, v14\n"
    "v_mfma_f32_16x16x32_bf16 v[116:119], a[204:207], v[44:47], v[116:119]\n"
    "ds_read_b64_tr_b16 a[202:203], v2 offset:43264\n"
    "v_fma_f32 v80, v80, s52, v15\n"
    "v_mfma_f32_16x16x32_bf16 v[120:123], a[204:207], v[48:51], v[120:123]\n"
    "v_exp_f32 v73, v73\n"
    "v_mfma_f32_16x16x32_bf16 v[124:127], a[204:207], v[52:55], v[124:127]\n"
    "v_exp_f32 v74, v74\n"
    "v_mfma_f32_16x16x32_bf16 v[128:131], a[204:207], v[56:59], v[128:131]\n"
    "s_waitcnt lgkmcnt(7)\n"
    "ds_read_b64_tr_b16 a[204:205], v3 offset:37120\n"
    "v_cvt_pk_bf16_f32 v34, v72, v73\n"
    "v_mfma_f32_16x16x32_bf16 v[144:147], a[208:211], v[56:59], v[144:147]\n"
    "ds_read_b64_tr_b16 a[206:207], v3 offset:43264\n"
    "v_fma_f32 v81, v81, s52, v15\n"
    "v_mfma_f32_16x16x32_bf16 v[140:143], a[208:211], v[52:55], v[140:143]\n"
    "v_exp_f32 v75, v75\n"
    "v_mfma_f32_16x16x32_bf16 v[136:139], a[208:211], v[48:51], v[136:139]\n"
    "v_cvt_pk_bf16_f32 v35, v74, v75\n"
    "v_fma_f32 v82, v82, s52, v15\n"
    "v_mfma_f32_16x16x32_bf16 v[132:135], a[208:211], v[44:47], v[132:135]\n"
    "s_waitcnt lgkmcnt(8)\n"
    "ds_read_b64_tr_b16 a[208:209], v4 offset:37120\n"
    "v_fma_f32 v83, v83, s52, v15\n"
    "v_mfma_f32_16x16x32_bf16 a[232:235], a[212:215], v[44:47], a[232:235]\n"
    "v_exp_f32 v36, v36\n"
    "v_mfma_f32_16x16x32_bf16 v[148:151], a[212:215], v[48:51], v[148:151]\n"
    "v_exp_f32 v37, v37\n"
    "v_mfma_f32_16x16x32_bf16 v[152:155], a[212:215], v[52:55], v[152:155]\n"
    "ds_read_b64_tr_b16 a[210:211], v4 offset:43264\n"
    "v_cvt_pk_bf16_f32 v36, v36, v37\n"
    "v_mfma_f32_16x16x32_bf16 v[156:159], a[212:215], v[56:59], v[156:159]\n"
    "s_waitcnt lgkmcnt(8)\n"
    "buffer_load_dwordx4 v7, s[36:39], s55 offen offset:1024 nt lds\n"
    "v_mfma_f32_16x16x32_bf16 v[172:175], a[192:195], v[56:59], v[172:175]\n"
    "s_nop 0\n"
    "ds_read_b64_tr_b16 a[212:213], v5 offset:37120\n"
    "ds_read_b64_tr_b16 a[214:215], v5 offset:43264\n"
    "v_mfma_f32_16x16x32_bf16 v[168:171], a[192:195], v[52:55], v[168:171]\n"
    "v_exp_f32 v40, v40\n"
    "v_mfma_f32_16x16x32_bf16 v[164:167], a[192:195], v[48:51], v[164:167]\n"
    "v_exp_f32 v41, v41\n"
    "v_mfma_f32_16x16x32_bf16 v[160:163], a[192:195], v[44:47], v[160:163]\n"
    "s_waitcnt lgkmcnt(8)\n"
    "v_exp_f32 v38, v38\n"
    "v_mfma_f32_16x16x32_bf16 v[176:179], a[196:199], v[44:47], v[176:179]\n"
    "v_exp_f32 v39, v39\n"
    "v_mfma_f32_16x16x32_bf16 v[180:183], a[196:199], v[48:51], v[180:183]\n"
    "v_exp_f32 v42, v42\n"
    "v_mfma_f32_16x16x32_bf16 v[184:187], a[196:199], v[52:55], v[184:187]\n"
    "s_waitcnt vmcnt(18) lgkmcnt(0)\n"
    "v_exp_f32 v43, v43\n"
    "v_mfma_f32_16x16x32_bf16 v[188:191], a[196:199], v[56:59], v[188:191]\n"
    "s_barrier\n"
    "ds_read_b128 a[192:195], v0 offset:0\n"
    "v_cvt_pk_bf16_f32 v40, v40, v41\n"
    "v_mfma_f32_16x16x32_bf16 v[204:207], a[200:203], v[56:59], v[204:207]\n"
    "ds_read_b128 a[196:199], v1 offset:0\n"
    "v_cvt_pk_bf16_f32 v37, v38, v39\n"
    "v_mfma_f32_16x16x32_bf16 v[200:203], a[200:203], v[52:55], v[200:203]\n"
    "v_exp_f32 v76, v76\n"
    "v_mfma_f32_16x16x32_bf16 v[196:199], a[200:203], v[48:51], v[196:199]\n"
    "v_exp_f32 v77, v77\n"
    "v_mfma_f32_16x16x32_bf16 v[192:195], a[200:203], v[44:47], v[192:195]\n"
    "ds_read_b128 a[200:203], v0 offset:128\n"
    "v_cvt_pk_bf16_f32 v41, v42, v43\n"
    "v_mfma_f32_16x16x32_bf16 v[208:211], a[204:207], v[44:47], v[208:211]\n"
    "v_exp_f32 v78, v78\n"
    "v_mfma_f32_16x16x32_bf16 v[212:215], a[204:207], v[48:51], v[212:215]\n"
    "buffer_load_dwordx4 v8, s[36:39], s55 offen offset:2048 nt lds\n"
    "v_mfma_f32_16x16x32_bf16 v[216:219], a[204:207], v[52:55], v[216:219]\n"
    "s_add_u32 m0, s54, 86016\n"
    "v_exp_f32 v79, v79\n"
    "v_mfma_f32_16x16x32_bf16 v[220:223], a[204:207], v[56:59], v[220:223]\n"
    "ds_read_b128 a[204:207], v1 offset:128\n"
    "v_cvt_pk_bf16_f32 v38, v76, v77\n"
    "v_mfma_f32_16x16x32_bf16 v[236:239], a[208:211], v[56:59], v[236:239]\n"
    "v_exp_f32 v80, v80\n"
    "v_mfma_f32_16x16x32_bf16 v[232:235], a[208:211], v[52:55], v[232:235]\n"
    "v_exp_f32 v81, v81\n"
    "v_mfma_f32_16x16x32_bf16 v[228:231], a[208:211], v[48:51], v[228:231]\n"
    "v_exp_f32 v82, v82\n"
    "v_mfma_f32_16x16x32_bf16 v[224:227], a[208:211], v[44:47], v[224:227]\n"
    "ds_read_b128 a[208:211], v0 offset:256\n"
    "v_cvt_pk_bf16_f32 v39, v78, v79\n"
    "v_mfma_f32_16x16x32_bf16 v[240:243], a[212:215], v[44:47], v[240:243]\n"
    "v_exp_f32 v83, v83\n"
    "v_mfma_f32_16x16x32_bf16 v[244:247], a[212:215], v[48:51], v[244:247]\n"
    "s_waitcnt vmcnt(17)\n"
    "v_cvt_pk_bf16_f32 v42, v80, v81\n"
    "v_cvt_pk_bf16_f32 v43, v82, v83\n"
    "v_mfma_f32_16x16x32_bf16 v[248:251], a[212:215], v[52:55], v[248:251]\n"
    "s_nop 0\n"
    "ds_read_b128 a[160:163], v10 offset:0\n"
    "ds_read_b128 a[164:167], v11 offset:0\n"
    "v_mfma_f32_16x16x32_bf16 v[252:255], a[212:215], v[56:59], v[252:255]\n"
    "s_waitcnt lgkmcnt(6)\n"
    "ds_read_b128 a[212:215], v1 offset:256\n"
    "v_mfma_f32_16x16x32_bf16 v[44:47], a[192:195], a[0:3], 0\n"
    "s_waitcnt lgkmcnt(6)\n"
    "v_mfma_f32_16x16x32_bf16 v[44:47], a[196:199], a[4:7], v[44:47]\n"
    "s_waitcnt lgkmcnt(5)\n"
    "v_mfma_f32_16x16x32_bf16 v[44:47], a[200:203], a[8:11], v[44:47]\n"
    "v_mfma_f32_16x16x32_bf16 v[48:51], a[200:203], a[32:35], 0\n"
    "v_mfma_f32_16x16x32_bf16 v[48:51], a[196:199], a[28:31], v[48:51]\n"
    "buffer_load_dwordx4 v6, s[40:43], s56 offen offset:0 nt lds\n"
    "v_mfma_f32_16x16x32_bf16 v[48:51], a[192:195], a[24:27], v[48:51]\n"
    "v_mfma_f32_16x16x32_bf16 v[52:55], a[192:195], a[48:51], 0\n"
    "v_mfma_f32_16x16x32_bf16 v[52:55], a[196:199], a[52:55], v[52:55]\n"
    "v_mfma_f32_16x16x32_bf16 v[52:55], a[200:203], a[56:59], v[52:55]\n"
    "v_mfma_f32_16x16x32_bf16 v[56:59], a[200:203], a[80:83], 0\n"
    "ds_read_b128 a[200:203], v0 offset:6272\n"
    "v_mfma_f32_16x16x32_bf16 v[56:59], a[196:199], a[76:79], v[56:59]\n"
    "ds_read_b128 a[196:199], v1 offset:6144\n"
    "v_mfma_f32_16x16x32_bf16 v[56:59], a[192:195], a[72:75], v[56:59]\n"
    "s_waitcnt lgkmcnt(6)\n"
    "ds_read_b128 a[192:195], v0 offset:6144\n"
    "v_mfma_f32_16x16x32_bf16 v[44:47], a[204:207], a[12:15], v[44:47]\n"
    "s_waitcnt lgkmcnt(6)\n"
    "v_mfma_f32_16x16x32_bf16 v[44:47], a[208:211], a[16:19], v[44:47]\n"
    "s_waitcnt lgkmcnt(3)\n"
    "v_mfma_f32_16x16x32_bf16 v[44:47], a[212:215], a[20:23], v[44:47]\n"
    "v_mfma_f32_16x16x32_bf16 v[48:51], a[212:215], a[44:47], v[48:51]\n"
    "v_mfma_f32_16x16x32_bf16 v[48:51], a[208:211], a[40:43], v[48:51]\n"
    "v_mfma_f32_16x16x32_bf16 v[48:51], a[204:207], a[36:39], v[48:51]\n"
    "v_mfma_f32_16x16x32_bf16 v[52:55], a[204:207], a[60:63], v[52:55]\n"
    "buffer_load_dwordx4 v7, s[40:43], s56 offen offset:1024 nt lds\n"
    "v_mfma_f32_16x16x32_bf16 v[52:55], a[208:211], a[64:67], v[52:55]\n"
    "v_mfma_f32_16x16x32_bf16 v[52:55], a[212:215], a[68:71], v[52:55]\n"
    "v_mfma_f32_16x16x32_bf16 v[56:59], a[212:215], a[92:95], v[56:59]\n"
    "ds_read_b128 a[212:215], v1 offset:6400\n"
    "v_fma_f32 v44, v44, s52, v12\n"
    "v_mfma_f32_16x16x32_bf16 v[56:59], a[208:211], a[88:91], v[56:59]\n"
    "ds_read_b128 a[208:211], v0 offset:6400\n"
    "v_fma_f32 v45, v45, s52, v12\n"
    "v_mfma_f32_16x16x32_bf16 v[56:59], a[204:207], a[84:87], v[56:59]\n"
    "s_waitcnt lgkmcnt(2)\n"
    "ds_read_b128 a[204:207], v1 offset:6272\n"
    "v_fma_f32 v46, v46, s52, v12\n"
    "v_mfma_f32_16x16x32_bf16 v[60:63], a[192:195], a[0:3], 0\n"
    "v_exp_f32 v44, v44\n"
    "v_mfma_f32_16x16x32_bf16 v[60:63], a[196:199], a[4:7], v[60:63]\n"
    "v_exp_f32 v45, v45\n"
    "v_mfma_f32_16x16x32_bf16 v[60:63], a[200:203], a[8:11], v[60:63]\n"
    "v_fma_f32 v47, v47, s52, v12\n"
    "v_cvt_pk_bf16_f32 v44, v44, v45\n"
    "v_mfma_f32_16x16x32_bf16 v[64:67], a[200:203], a[32:35], 0\n"
    "v_exp_f32 v46, v46\n"
    "v_mfma_f32_16x16x32_bf16 v[64:67], a[196:199], a[28:31], v[64:67]\n"
    "v_exp_f32 v47, v47\n"
    "v_mfma_f32_16x16x32_bf16 v[64:67], a[192:195], a[24:27], v[64:67]\n"
    "v_cvt_pk_bf16_f32 v45, v46, v47\n"
    "v_fma_f32 v48, v48, s52, v13\n"
    "v_mfma_f32_16x16x32_bf16 v[68:71], a[192:195], a[48:51], 0\n"
    "v_fma_f32 v49, v49, s52, v13\n"
    "v_fma_f32 v50, v50, s52, v13\n"
    "v_mfma_f32_16x16x32_bf16 v[68:71], a[196:199], a[52:55], v[68:71]\n"
    "v_exp_f32 v48, v48\n"
    "v_mfma_f32_16x16x32_bf16 v[68:71], a[200:203], a[56:59], v[68:71]\n"
    "buffer_load_dwordx4 v8, s[40:43], s56 offen offset:2048 nt lds\n"
    "v_mfma_f32_16x16x32_bf16 v[72:75], a[200:203], a[80:83], 0\n"
    "s_add_u32 s56, s56, 24576\n"
    "ds_read_b64_tr_b16 a[200:201], v4 offset:49152\n"
    "v_fma_f32 v51, v51, s52, v13\n"
    "v_mfma_f32_16x16x32_bf16 v[72:75], a[196:199], a[76:79], v[72:75]\n"
    "s_add_u32 m0, s54, 49152\n"
    "ds_read_b64_tr_b16 a[196:197], v3 offset:49152\n"
    "v_fma_f32 v52, v52, s52, v14\n"
    "v_mfma_f32_16x16x32_bf16 v[72:75], a[192:195], a[72:75], v[72:75]\n"
    "s_waitcnt lgkmcnt(2)\n"
    "ds_read_b64_tr_b16 a[192:193], v2 offset:49152\n"
    "v_fma_f32 v53, v53, s52, v14\n"
    "v_mfma_f32_16x16x32_bf16 v[60:63], a[204:207], a[12:15], v[60:63]\n"
    "ds_read_b64_tr_b16 a[194:195], v2 offset:55296\n"
    "v_fma_f32 v56, v56, s52, v15\n"
    "v_mfma_f32_16x16x32_bf16 v[60:63], a[208:211], a[16:19], v[60:63]\n"
    "ds_read_b64_tr_b16 a[198:199], v3 offset:55296\n"
    "v_fma_f32 v57, v57, s52, v15\n"
    "v_mfma_f32_16x16x32_bf16 v[60:63], a[212:215], a[20:23], v[60:63]\n"
    "ds_read_b64_tr_b16 a[202:203], v4 offset:55296\n"
    "v_fma_f32 v54, v54, s52, v14\n"
    "v_mfma_f32_16x16x32_bf16 v[64:67], a[212:215], a[44:47], v[64:67]\n"
    "v_exp_f32 v49, v49\n"
    "v_mfma_f32_16x16x32_bf16 v[64:67], a[208:211], a[40:43], v[64:67]\n"
    "s_add_u32 s72, s62, 0\n"
    "v_exp_f32 v50, v50\n"
    "v_mfma_f32_16x16x32_bf16 v[64:67], a[204:207], a[36:39], v[64:67]\n"
    "s_add_u32 s73, s62, 6144\n"
    "v_fma_f32 v60, v60, s52, v12\n"
    "v_fma_f32 v61, v61, s52, v12\n"
    "v_mfma_f32_16x16x32_bf16 v[68:71], a[204:207], a[60:63], v[68:71]\n"
    "s_add_u32 s74, s62, 12288\n"
    "v_fma_f32 v62, v62, s52, v12\n"
    "v_fma_f32 v63, v63, s52, v12\n"
    "v_mfma_f32_16x16x32_bf16 v[68:71], a[208:211], a[64:67], v[68:71]\n"
    "s_add_u32 s75, s62, 18432\n"
    "v_exp_f32 v60, v60\n"
    "v_mfma_f32_16x16x32_bf16 v[68:71], a[212:215], a[68:71], v[68:71]\n"
    "v_exp_f32 v61, v61\n"
    "v_mfma_f32_16x16x32_bf16 v[72:75], a[212:215], a[92:95], v[72:75]\n"
    "ds_read_b64_tr_b16 a[212:213], v3 offset:49280\n"
    "v_cvt_pk_bf16_f32 v46, v60, v61\n"
    "v_mfma_f32_16x16x32_bf16 v[72:75], a[208:211], a[88:91], v[72:75]\n"
    "ds_read_b64_tr_b16 a[208:209], v2 offset:49280\n"
    "v_fma_f32 v64, v64, s52, v13\n"
    "v_mfma_f32_16x16x32_bf16 v[72:75], a[204:207], a[84:87], v[72:75]\n"
    "buffer_load_dwordx4 v6, s[36:39], s56 offen offset:0 nt lds\n"
    "v_smfmac_f32_16x16x64_bf16 a[236:239], a[252:255], v[28:35], v23\n"
    "ds_read_b64_tr_b16 a[204:205], v5 offset:49152\n"
    "v_fma_f32 v65, v65, s52, v13\n"
    "v_smfmac_f32_16x16x64_bf16 a[240:243], a[252:255], v[36:43], v23\n"
    "s_waitcnt lgkmcnt(5)\n"
    "ds_read_b64_tr_b16 a[206:207], v5 offset:55296\n"
    "v_fma_f32 v66, v66, s52, v13\n"
    "v_mfma_f32_16x16x32_bf16 v[96:99], a[192:195], v[40:43], v[96:99]\n"
    "ds_read_b64_tr_b16 a[210:211], v2 offset:55424\n"
    "v_fma_f32 v67, v67, s52, v13\n"
    "v_mfma_f32_16x16x32_bf16 v[92:95], a[192:195], v[36:39], v[92:95]\n"
    "ds_read_b64_tr_b16 a[214:215], v3 offset:55424\n"
    "v_cvt_pk_bf16_f32 v48, v48, v49\n"
    "v_mfma_f32_16x16x32_bf16 v[88:91], a[192:195], v[32:35], v[88:91]\n"
    "v_exp_f32 v62, v62\n"
    "v_mfma_f32_16x16x32_bf16 v[84:87], a[192:195], v[28:31], v[84:87]\n"
    "s_waitcnt lgkmcnt(7)\n"
    "ds_read_b64_tr_b16 a[192:193], v4 offset:49280\n"
    "v_fma_f32 v55, v55, s52, v14\n"
    "v_mfma_f32_16x16x32_bf16 a[216:219], a[196:199], v[28:31], a[216:219]\n"
    "ds_read_b64_tr_b16 a[194:195], v4 offset:55424\n"
    "v_fma_f32 v58, v58, s52, v15\n"
    "v_mfma_f32_16x16x32_bf16 a[220:223], a[196:199], v[32:35], a[220:223]\n"
    "v_exp_f32 v63, v63\n"
    "v_mfma_f32_16x16x32_bf16 a[224:227], a[196:199], v[36:39], a[224:227]\n"
    "v_cvt_pk_bf16_f32 v47, v62, v63\n"
    "v_fma_f32 v59, v59, s52, v15\n"
    "v_mfma_f32_16x16x32_bf16 a[228:231], a[196:199], v[40:43], a[228:231]\n"
    "s_waitcnt lgkmcnt(8)\n"
    "ds_read_b64_tr_b16 a[196:197], v5 offset:49280\n"
    "v_fma_f32 v68, v68, s52, v14\n"
    "v_mfma_f32_16x16x32_bf16 v[112:115], a[200:203], v[40:43], v[112:115]\n"
    "ds_read_b64_tr_b16 a[198:199], v5 offset:55424\n"
    "v_fma_f32 v69, v69, s52, v14\n"
    "v_mfma_f32_16x16x32_bf16 v[108:111], a[200:203], v[36:39], v[108:111]\n"
    "v_exp_f32 v51, v51\n"
    "v_mfma_f32_16x16x32_bf16 v[104:107], a[200:203], v[32:35], v[104:107]\n"
    "v_exp_f32 v64, v64\n"
    "v_mfma_f32_16x16x32_bf16 v[100:103], a[200:203], v[28:31], v[100:103]\n"
    "s_waitcnt lgkmcnt(6)\n"
    "buffer_load_dwordx4 v7, s[36:39], s56 offen offset:1024 nt lds\n"
    "v_mfma_f32_16x16x32_bf16 v[116:119], a[204:207], v[28:31], v[116:119]\n"
    "ds_read_b64_tr_b16 a[200:201], v2 offset:49408\n"
    "v_cvt_pk_bf16_f32 v49, v50, v51\n"
    "v_mfma_f32_16x16x32_bf16 v[120:123], a[204:207], v[32:35], v[120:123]\n"
    "ds_read_b64_tr_b16 a[202:203], v2 offset:55552\n"
    "v_fma_f32 v70, v70, s52, v14\n"
    "v_mfma_f32_16x16x32_bf16 v[124:127], a[204:207], v[36:39], v[124:127]\n"
    "v_exp_f32 v65, v65\n"
    "v_mfma_f32_16x16x32_bf16 v[128:131], a[204:207], v[40:43], v[128:131]\n"
    "s_waitcnt lgkmcnt(7)\n"
    "ds_read_b64_tr_b16 a[204:205], v3 offset:49408\n"
    "v_cvt_pk_bf16_f32 v50, v64, v65\n"
    "v_mfma_f32_16x16x32_bf16 v[144:147], a[208:211], v[40:43], v[144:147]\n"
    "ds_read_b64_tr_b16 a[206:207], v3 offset:55552\n"
    "v_fma_f32 v71, v71, s52, v14\n"
    "v_mfma_f32_16x16x32_bf16 v[140:143], a[208:211], v[36:39], v[140:143]\n"
    "v_exp_f32 v66, v66\n"
    "v_mfma_f32_16x16x32_bf16 v[136:139], a[208:211], v[32:35], v[136:139]\n"
    "v_exp_f32 v67, v67\n"
    "v_mfma_f32_16x16x32_bf16 v[132:135], a[208:211], v[28:31], v[132:135]\n"
    "s_waitcnt lgkmcnt(8)\n"
    "ds_read_b64_tr_b16 a[208:209], v4 offset:49408\n"
    "v_cvt_pk_bf16_f32 v51, v66, v67\n"
    "v_mfma_f32_16x16x32_bf16 a[232:235], a[212:215], v[28:31], a[232:235]\n"
    "ds_read_b64_tr_b16 a[210:211], v4 offset:55552\n"
    "v_fma_f32 v72, v72, s52, v15\n"
    "v_mfma_f32_16x16x32_bf16 v[148:151], a[212:215], v[32:35], v[148:151]\n"
    "v_exp_f32 v52, v52\n"
    "v_mfma_f32_16x16x32_bf16 v[152:155], a[212:215], v[36:39], v[152:155]\n"
    "v_exp_f32 v53, v53\n"
    "v_mfma_f32_16x16x32_bf16 v[156:159], a[212:215], v[40:43], v[156:159]\n"
    "s_waitcnt lgkmcnt(8)\n"
    "ds_read_b64_tr_b16 a[212:213], v5 offset:49408\n"
    "v_fma_f32 v73, v73, s52, v15\n"
    "v_mfma_f32_16x16x32_bf16 v[172:175], a[192:195], v[40:43], v[172:175]\n"
    "ds_read_b64_tr_b16 a[214:215], v5 offset:55552\n"
    "v_fma_f32 v74, v74, s52, v15\n"
    "v_mfma_f32_16x16x32_bf16 v[168:171], a[192:195], v[36:39], v[168:171]\n"
    "s_waitcnt vmcnt(17)\n"
    "buffer_load_dwordx4 v8, s[36:39], s56 offen offset:2048 nt lds\n"
    "v_mfma_f32_16x16x32_bf16 v[164:167], a[192:195], v[32:35], v[164:167]\n"
    "s_add_u32 m0, s54, 98304\n"
    "ds_read_b128 a[184:187], v10 offset:2048\n"
    "v_fma_f32 v75, v75, s52, v15\n"
    "v_mfma_f32_16x16x32_bf16 v[160:163], a[192:195], v[28:31], v[160:163]\n"
    "s_waitcnt lgkmcnt(9)\n"
    "ds_read_b128 a[188:191], v11 offset:2048\n"
    "v_cvt_pk_bf16_f32 v52, v52, v53\n"
    "v_mfma_f32_16x16x32_bf16 v[176:179], a[196:199], v[28:31], v[176:179]\n"
    "v_exp_f32 v56, v56\n"
    "v_mfma_f32_16x16x32_bf16 v[180:183], a[196:199], v[32:35], v[180:183]\n"
    "v_exp_f32 v57, v57\n"
    "v_mfma_f32_16x16x32_bf16 v[184:187], a[196:199], v[36:39], v[184:187]\n"
    "s_waitcnt vmcnt(15) lgkmcnt(2)\n"
    "v_exp_f32 v54, v54\n"
    "v_mfma_f32_16x16x32_bf16 v[188:191], a[196:199], v[40:43], v[188:191]\n"
    "s_barrier\n"
    "ds_read_b128 a[192:195], v0 offset:12288\n"
    "v_cvt_pk_bf16_f32 v56, v56, v57\n"
    "v_mfma_f32_16x16x32_bf16 v[204:207], a[200:203], v[40:43], v[204:207]\n"
    "ds_read_b128 a[196:199], v1 offset:12288\n"
    "v_mbcnt_lo_u32_b32 v24, -1, 0\n"
    "v_mfma_f32_16x16x32_bf16 v[200:203], a[200:203], v[36:39], v[200:203]\n"
    "v_exp_f32 v55, v55\n"
    "v_mfma_f32_16x16x32_bf16 v[196:199], a[200:203], v[32:35], v[196:199]\n"
    "v_exp_f32 v58, v58\n"
    "v_mfma_f32_16x16x32_bf16 v[192:195], a[200:203], v[28:31], v[192:195]\n"
    "ds_read_b128 a[200:203], v0 offset:12416\n"
    "v_cvt_pk_bf16_f32 v53, v54, v55\n"
    "v_mfma_f32_16x16x32_bf16 v[208:211], a[204:207], v[28:31], v[208:211]\n"
    "v_exp_f32 v59, v59\n"
    "v_mfma_f32_16x16x32_bf16 v[212:215], a[204:207], v[32:35], v[212:215]\n"
    "v_exp_f32 v68, v68\n"
    "v_mfma_f32_16x16x32_bf16 v[216:219], a[204:207], v[36:39], v[216:219]\n"
    "v_exp_f32 v69, v69\n"
    "v_mfma_f32_16x16x32_bf16 v[220:223], a[204:207], v[40:43], v[220:223]\n"
    "ds_read_b128 a[204:207], v1 offset:12416\n"
    "v_cvt_pk_bf16_f32 v57, v58, v59\n"
    "v_mfma_f32_16x16x32_bf16 v[236:239], a[208:211], v[40:43], v[236:239]\n"
    "buffer_load_dwordx4 v6, s[40:43], s55 offen offset:0 nt lds\n"
    "v_mfma_f32_16x16x32_bf16 v[232:235], a[208:211], v[36:39], v[232:235]\n"
    "v_exp_f32 v70, v70\n"
    "v_mfma_f32_16x16x32_bf16 v[228:231], a[208:211], v[32:35], v[228:231]\n"
    "v_exp_f32 v71, v71\n"
    "v_mfma_f32_16x16x32_bf16 v[224:227], a[208:211], v[28:31], v[224:227]\n"
    "ds_read_b128 a[208:211], v0 offset:12544\n"
    "v_cvt_pk_bf16_f32 v54, v68, v69\n"
    "v_mfma_f32_16x16x32_bf16 v[240:243], a[212:215], v[28:31], v[240:243]\n"
    "v_exp_f32 v72, v72\n"
    "v_mfma_f32_16x16x32_bf16 v[244:247], a[212:215], v[32:35], v[244:247]\n"
    "v_exp_f32 v73, v73\n"
    "v_mfma_f32_16x16x32_bf16 v[248:251], a[212:215], v[36:39], v[248:251]\n"
    "v_exp_f32 v74, v74\n"
    "v_mfma_f32_16x16x32_bf16 v[252:255], a[212:215], v[40:43], v[252:255]\n"
    "s_waitcnt lgkmcnt(4)\n"
    "ds_read_b128 a[212:215], v1 offset:12544\n"
    "v_cvt_pk_bf16_f32 v55, v70, v71\n"
    "v_mfma_f32_16x16x32_bf16 v[28:31], a[192:195], a[96:99], 0\n"
    "s_waitcnt lgkmcnt(4)\n"
    "v_exp_f32 v75, v75\n"
    "v_mfma_f32_16x16x32_bf16 v[28:31], a[196:199], a[100:103], v[28:31]\n"
    "s_waitcnt lgkmcnt(3)\n"
    "v_cvt_pk_bf16_f32 v58, v72, v73\n"
    "v_cvt_pk_bf16_f32 v59, v74, v75\n"
    "v_mfma_f32_16x16x32_bf16 v[28:31], a[200:203], a[104:107], v[28:31]\n"
    "v_mbcnt_hi_u32_b32 v24, -1, v24\n"
    "v_bfe_u32 v26, v24, 2, 1\n"
    "v_mfma_f32_16x16x32_bf16 v[32:35], a[200:203], a[128:131], 0\n"
    "v_bfe_u32 v25, v24, 3, 1\n"
    "v_lshl_add_u32 v26, v26, 1, v25\n"
    "v_mfma_f32_16x16x32_bf16 v[32:35], a[196:199], a[124:127], v[32:35]\n"
    "v_lshrrev_b32 v25, 4, v24\n"
    "v_xor_b32 v26, v26, v25\n"
    "v_mfma_f32_16x16x32_bf16 v[32:35], a[192:195], a[120:123], v[32:35]\n"
    "v_and_b32 v25, 15, v24\n"
    "v_mul_u32_u24 v25, 160, v25\n"
    "v_mfma_f32_16x16x32_bf16 v[36:39], a[192:195], a[144:147], 0\n"
    "buffer_load_dwordx4 v7, s[40:43], s55 offen offset:1024 nt lds\n"
    "v_mfma_f32_16x16x32_bf16 v[36:39], a[196:199], a[148:151], v[36:39]\n"
    "v_lshl_add_u32 v10, v26, 3, v25\n"
    "v_bfe_u32 v25, v24, 3, 2\n"
    "v_mfma_f32_16x16x32_bf16 v[36:39], a[200:203], a[152:155], v[36:39]\n"
    "v_lshrrev_b32 v26, 5, v24\n"
    "v_add_u32 v10, s57, v10\n"
    "v_mfma_f32_16x16x32_bf16 v[40:43], a[200:203], a[176:179], 0\n"
    "ds_read_b128 a[200:203], v0 offset:18560\n"
    "v_lshl_add_u32 v25, v25, 2, v26\n"
    "v_mfma_f32_16x16x32_bf16 v[40:43], a[196:199], a[172:175], v[40:43]\n"
    "ds_read_b128 a[196:199], v1 offset:18432\n"
    "v_mul_u32_u24 v11, 160, v25\n"
    "v_mfma_f32_16x16x32_bf16 v[40:43], a[192:195], a[168:171], v[40:43]\n"
    "s_waitcnt lgkmcnt(4)\n"
    "ds_read_b128 a[192:195], v0 offset:18432\n"
    "v_mul_u32_u24 v9, 384, v25\n"
    "v_mfma_f32_16x16x32_bf16 v[28:31], a[204:207], a[108:111], v[28:31]\n"
    "s_waitcnt lgkmcnt(4)\n"
    "v_and_b32 v25, 7, v24\n"
    "v_bfe_u32 v26, v24, 3, 1\n"
    "v_mfma_f32_16x16x32_bf16 v[28:31], a[208:211], a[112:115], v[28:31]\n"
    "s_waitcnt lgkmcnt(3)\n"
    "v_lshl_add_u32 v9, v25, 4, v9\n"
    "v_xor_b32 v25, v25, v26\n"
    "v_mfma_f32_16x16x32_bf16 v[28:31], a[212:215], a[116:119], v[28:31]\n"
    "v_bfe_u32 v26, v24, 4, 1\n"
    "v_lshl_add_u32 v11, v25, 4, v11\n"
    "v_mfma_f32_16x16x32_bf16 v[32:35], a[212:215], a[140:143], v[32:35]\n"
    "v_lshlrev_b32 v26, 3, v26\n"
    "v_add3_u32 v11, v11, v26, s57\n"
    "v_mfma_f32_16x16x32_bf16 v[32:35], a[208:211], a[136:139], v[32:35]\n"
    "v_xor_b32 v20, 8, v11\n"
    "v_mfma_f32_16x16x32_bf16 v[32:35], a[204:207], a[132:135], v[32:35]\n"
    "v_max3_f32 v12, v28, v29, v30\n"
    "v_max_f32 v12, v12, v31\n"
    "v_mfma_f32_16x16x32_bf16 v[36:39], a[204:207], a[156:159], v[36:39]\n"
    "v_mfma_f32_16x16x32_bf16 v[36:39], a[208:211], a[160:163], v[36:39]\n"
    "v_mfma_f32_16x16x32_bf16 v[36:39], a[212:215], a[164:167], v[36:39]\n"
    "buffer_load_dwordx4 v8, s[40:43], s55 offen offset:2048 nt lds\n"
    "v_mfma_f32_16x16x32_bf16 v[40:43], a[212:215], a[188:191], v[40:43]\n"
    "s_add_u32 s55, s55, 24576\n"
    "ds_read_b128 a[212:215], v1 offset:18688\n"
    "v_max3_f32 v13, v32, v33, v34\n"
    "v_mfma_f32_16x16x32_bf16 v[40:43], a[208:211], a[184:187], v[40:43]\n"
    "s_add_u32 m0, s54, 0\n"
    "ds_read_b128 a[208:211], v0 offset:18688\n"
    "v_max_f32 v13, v13, v35\n"
    "v_mfma_f32_16x16x32_bf16 v[40:43], a[204:207], a[180:183], v[40:43]\n"
    "s_waitcnt lgkmcnt(2)\n"
    "ds_read_b128 a[204:207], v1 offset:18560\n"
    "v_max3_f32 v14, v36, v37, v38\n"
    "v_mfma_f32_16x16x32_bf16 v[76:79], a[192:195], a[96:99], 0\n"
    "v_max_f32 v14, v14, v39\n"
    "v_mfma_f32_16x16x32_bf16 v[76:79], a[196:199], a[100:103], v[76:79]\n"
    "v_mfma_f32_16x16x32_bf16 v[76:79], a[200:203], a[104:107], v[76:79]\n"
    "v_mfma_f32_16x16x32_bf16 v[80:83], a[200:203], a[128:131], 0\n"
    "v_max3_f32 v15, v40, v41, v42\n"
    "v_max_f32 v15, v15, v43\n"
    "v_mfma_f32_16x16x32_bf16 v[80:83], a[196:199], a[124:127], v[80:83]\n"
    "v_mfma_f32_16x16x32_bf16 v[80:83], a[192:195], a[120:123], v[80:83]\n"
    "v_mfma_f32_16x16x32_bf16 v[60:63], a[192:195], a[144:147], 0\n"
    "v_mfma_f32_16x16x32_bf16 v[60:63], a[196:199], a[148:151], v[60:63]\n"
    "v_mfma_f32_16x16x32_bf16 v[60:63], a[200:203], a[152:155], v[60:63]\n"
    "v_mfma_f32_16x16x32_bf16 v[64:67], a[200:203], a[176:179], 0\n"
    "s_nop 0\n"
    "ds_read_b64_tr_b16 a[200:201], v4 offset:0\n"
    "ds_read_b64_tr_b16 a[202:203], v4 offset:6144\n"
    "v_mfma_f32_16x16x32_bf16 v[64:67], a[196:199], a[172:175], v[64:67]\n"
    "s_nop 0\n"
    "ds_read_b64_tr_b16 a[196:197], v3 offset:0\n"
    "ds_read_b64_tr_b16 a[198:199], v3 offset:6144\n"
    "v_mfma_f32_16x16x32_bf16 v[64:67], a[192:195], a[168:171], v[64:67]\n"
    "s_waitcnt lgkmcnt(4)\n"
    "buffer_load_dwordx4 v6, s[36:39], s55 offen offset:0 nt lds\n"
    "v_mfma_f32_16x16x32_bf16 v[76:79], a[204:207], a[108:111], v[76:79]\n"
    "s_nop 0\n"
    "ds_read_b64_tr_b16 a[192:193], v2 offset:0\n"
    "ds_read_b64_tr_b16 a[194:195], v2 offset:6144\n"
    "v_mfma_f32_16x16x32_bf16 v[76:79], a[208:211], a[112:115], v[76:79]\n"
    "v_mfma_f32_16x16x32_bf16 v[76:79], a[212:215], a[116:119], v[76:79]\n"
    "v_mfma_f32_16x16x32_bf16 v[80:83], a[212:215], a[140:143], v[80:83]\n"
    "v_mfma_f32_16x16x32_bf16 v[80:83], a[208:211], a[136:139], v[80:83]\n"
    "v_mfma_f32_16x16x32_bf16 v[80:83], a[204:207], a[132:135], v[80:83]\n"
    "v_mfma_f32_16x16x32_bf16 v[60:63], a[204:207], a[156:159], v[60:63]\n"
    "v_mfma_f32_16x16x32_bf16 v[60:63], a[208:211], a[160:163], v[60:63]\n"
    "v_mfma_f32_16x16x32_bf16 v[60:63], a[212:215], a[164:167], v[60:63]\n"
    "v_mfma_f32_16x16x32_bf16 v[64:67], a[212:215], a[188:191], v[64:67]\n"
    "s_nop 0\n"
    "ds_read_b64_tr_b16 a[212:213], v3 offset:128\n"
    "ds_read_b64_tr_b16 a[214:215], v3 offset:6272\n"
    "v_mfma_f32_16x16x32_bf16 v[64:67], a[208:211], a[184:187], v[64:67]\n"
    "s_nop 0\n"
    "ds_read_b64_tr_b16 a[208:209], v2 offset:128\n"
    "ds_read_b64_tr_b16 a[210:211], v2 offset:6272\n"
    "v_mfma_f32_16x16x32_bf16 v[64:67], a[204:207], a[180:183], v[64:67]\n"
    "s_nop 0\n"
    "ds_read_b64_tr_b16 a[204:205], v5 offset:0\n"
    "ds_read_b64_tr_b16 a[206:207], v5 offset:6144\n"
    "v_smfmac_f32_16x16x64_bf16 a[236:239], a[252:255], v[44:51], v23\n"
    "v_smfmac_f32_16x16x64_bf16 a[240:243], a[252:255], v[52:59], v23\n"
    "s_waitcnt lgkmcnt(6)\n"
    "buffer_load_dwordx4 v7, s[36:39], s55 offen offset:1024 nt lds\n"
    "v_mfma_f32_16x16x32_bf16 v[84:87], a[192:195], v[44:47], v[84:87]\n"
    "v_mfma_f32_16x16x32_bf16 v[88:91], a[192:195], v[48:51], v[88:91]\n"
    "v_mfma_f32_16x16x32_bf16 v[92:95], a[192:195], v[52:55], v[92:95]\n"
    "v_mfma_f32_16x16x32_bf16 v[96:99], a[192:195], v[56:59], v[96:99]\n"
    "s_nop 0\n"
    "ds_read_b64_tr_b16 a[192:193], v4 offset:128\n"
    "ds_read_b64_tr_b16 a[194:195], v4 offset:6272\n"
    "v_mfma_f32_16x16x32_bf16 a[216:219], a[196:199], v[44:47], a[216:219]\n"
    "v_accvgpr_read_b32 v16, a236\n"
    "v_accvgpr_read_b32 v26, a237\n"
    "v_mfma_f32_16x16x32_bf16 a[220:223], a[196:199], v[48:51], a[220:223]\n"
    "v_add_f32 v16, v16, v26\n"
    "v_accvgpr_read_b32 v17, a238\n"
    "v_mfma_f32_16x16x32_bf16 a[224:227], a[196:199], v[52:55], a[224:227]\n"
    "v_accvgpr_read_b32 v26, a239\n"
    "v_accvgpr_read_b32 v18, a240\n"
    "v_mfma_f32_16x16x32_bf16 a[228:231], a[196:199], v[56:59], a[228:231]\n"
    "ds_read_b64_tr_b16 a[196:197], v5 offset:128\n"
    "v_add_f32 v17, v17, v26\n"
    "v_mfma_f32_16x16x32_bf16 v[100:103], a[200:203], v[44:47], v[100:103]\n"
    "ds_read_b64_tr_b16 a[198:199], v5 offset:6272\n"
    "v_add_f32 v27, v16, v17\n"
    "v_mfma_f32_16x16x32_bf16 v[104:107], a[200:203], v[48:51], v[104:107]\n"
    "v_rcp_f32 v16, v16\n"
    "v_mfma_f32_16x16x32_bf16 v[108:111], a[200:203], v[52:55], v[108:111]\n"
    "v_accvgpr_read_b32 v26, a241\n"
    "v_accvgpr_read_b32 v24, a218\n"
    "v_mfma_f32_16x16x32_bf16 v[112:115], a[200:203], v[56:59], v[112:115]\n"
    "s_waitcnt lgkmcnt(4)\n"
    "ds_read_b64_tr_b16 a[200:201], v2 offset:256\n"
    "v_accvgpr_read_b32 v25, a219\n"
    "v_mfma_f32_16x16x32_bf16 v[116:119], a[204:207], v[44:47], v[116:119]\n"
    "ds_read_b64_tr_b16 a[202:203], v2 offset:6400\n"
    "v_add_f32 v18, v18, v26\n"
    "v_mfma_f32_16x16x32_bf16 v[120:123], a[204:207], v[48:51], v[120:123]\n"
    "buffer_load_dwordx4 v8, s[36:39], s55 offen offset:2048 nt lds\n"
    "v_mfma_f32_16x16x32_bf16 v[124:127], a[204:207], v[52:55], v[124:127]\n"
    "s_add_u32 m0, s54, 110592\n"
    "v_rcp_f32 v17, v17\n"
    "v_mfma_f32_16x16x32_bf16 v[128:131], a[204:207], v[56:59], v[128:131]\n"
    "ds_read_b64_tr_b16 a[204:205], v3 offset:256\n"
    "ds_read_b64_tr_b16 a[206:207], v3 offset:6400\n"
    "v_mul_f32 v24, v24, v16\n"
    "v_mul_f32 v25, v25, v16\n"
    "v_add_f32 v27, v27, v18\n"
    "v_pk_mul_f32 v[88:89], v[88:89], v[16:17] op_sel:[0,1] op_sel_hi:[1,1]\n"
    "v_pk_mul_f32 v[90:91], v[90:91], v[16:17] op_sel:[0,1] op_sel_hi:[1,1]\n"
    "v_pk_mul_f32 v[104:105], v[104:105], v[16:17] op_sel:[0,1] op_sel_hi:[1,1]\n"
    "v_pk_mul_f32 v[106:107], v[106:107], v[16:17] op_sel:[0,1] op_sel_hi:[1,1]\n"
    "v_pk_mul_f32 v[120:121], v[120:121], v[16:17] op_sel:[0,1] op_sel_hi:[1,1]\n"
    "v_pk_mul_f32 v[84:85], v[84:85], v[16:17] op_sel_hi:[1,0]\n"
    "v_pk_mul_f32 v[86:87], v[86:87], v[16:17] op_sel_hi:[1,0]\n"
    "v_pk_mul_f32 v[100:101], v[100:101], v[16:17] op_sel_hi:[1,0]\n"
    "v_mfma_f32_16x16x32_bf16 v[132:135], a[208:211], v[44:47], v[132:135]\n"
    "v_cvt_pk_bf16_f32 v84, v84, v85\n"
    "v_accvgpr_read_b32 v19, a242\n"
    "v_mfma_f32_16x16x32_bf16 v[136:139], a[208:211], v[48:51], v[136:139]\n"
    "v_cvt_pk_bf16_f32 v85, v86, v87\n"
    "v_accvgpr_read_b32 v26, a243\n"
    "v_mfma_f32_16x16x32_bf16 v[140:143], a[208:211], v[52:55], v[140:143]\n"
    "v_cvt_pk_bf16_f32 v87, v24, v25\n"
    "v_cvt_pk_bf16_f32 v88, v88, v89\n"
    "v_mfma_f32_16x16x32_bf16 v[144:147], a[208:211], v[56:59], v[144:147]\n"
    "ds_read_b64_tr_b16 a[208:209], v4 offset:256\n"
    "ds_read_b64_tr_b16 a[210:211], v4 offset:6400\n"
    "v_rcp_f32 v18, v18\n"
    "v_accvgpr_read_b32 v24, a222\n"
    "v_pk_mul_f32 v[92:93], v[92:93], v[18:19] op_sel_hi:[1,0]\n"
    "v_pk_mul_f32 v[94:95], v[94:95], v[18:19] op_sel_hi:[1,0]\n"
    "v_pk_mul_f32 v[132:133], v[132:133], v[16:17] op_sel_hi:[1,0]\n"
    "v_pk_mul_f32 v[134:135], v[134:135], v[16:17] op_sel_hi:[1,0]\n"
    "v_pk_mul_f32 v[108:109], v[108:109], v[18:19] op_sel_hi:[1,0]\n"
    "v_pk_mul_f32 v[110:111], v[110:111], v[18:19] op_sel_hi:[1,0]\n"
    "v_pk_mul_f32 v[124:125], v[124:125], v[18:19] op_sel_hi:[1,0]\n"
    "v_pk_mul_f32 v[126:127], v[126:127], v[18:19] op_sel_hi:[1,0]\n"
    "v_mfma_f32_16x16x32_bf16 a[232:235], a[212:215], v[44:47], a[232:235]\n"
    "v_accvgpr_read_b32 v25, a223\n"
    "v_add_f32 v19, v19, v26\n"
    "v_mfma_f32_16x16x32_bf16 v[148:151], a[212:215], v[48:51], v[148:151]\n"
    "v_cvt_pk_bf16_f32 v89, v90, v91\n"
    "v_mul_f32 v24, v24, v17\n"
    "v_mfma_f32_16x16x32_bf16 v[152:155], a[212:215], v[52:55], v[152:155]\n"
    "v_mul_f32 v25, v25, v17\n"
    "v_add_f32 v27, v27, v19\n"
    "v_mfma_f32_16x16x32_bf16 v[156:159], a[212:215], v[56:59], v[156:159]\n"
    "s_waitcnt lgkmcnt(8)\n"
    "ds_read_b64_tr_b16 a[212:213], v5 offset:256\n"
    "ds_read_b64_tr_b16 a[214:215], v5 offset:6400\n"
    "v_cvt_pk_bf16_f32 v91, v24, v25\n"
    "v_rcp_f32 v19, v19\n"
    "v_pk_mul_f32 v[122:123], v[122:123], v[16:17] op_sel:[0,1] op_sel_hi:[1,1]\n"
    "v_pk_mul_f32 v[96:97], v[96:97], v[18:19] op_sel:[0,1] op_sel_hi:[1,1]\n"
    "v_pk_mul_f32 v[98:99], v[98:99], v[18:19] op_sel:[0,1] op_sel_hi:[1,1]\n"
    "v_pk_mul_f32 v[112:113], v[112:113], v[18:19] op_sel:[0,1] op_sel_hi:[1,1]\n"
    "v_pk_mul_f32 v[114:115], v[114:115], v[18:19] op_sel:[0,1] op_sel_hi:[1,1]\n"
    "v_pk_mul_f32 v[128:129], v[128:129], v[18:19] op_sel:[0,1] op_sel_hi:[1,1]\n"
    "v_pk_mul_f32 v[130:131], v[130:131], v[18:19] op_sel:[0,1] op_sel_hi:[1,1]\n"
    "v_pk_mul_f32 v[102:103], v[102:103], v[16:17] op_sel_hi:[1,0]\n"
    "v_mfma_f32_16x16x32_bf16 v[160:163], a[192:195], v[44:47], v[160:163]\n"
    "v_cvt_pk_bf16_f32 v92, v92, v93\n"
    "v_accvgpr_read_b32 v24, a226\n"
    "v_mfma_f32_16x16x32_bf16 v[164:167], a[192:195], v[48:51], v[164:167]\n"
    "v_accvgpr_read_b32 v25, a227\n"
    "v_cvt_pk_bf16_f32 v93, v94, v95\n"
    "v_mfma_f32_16x16x32_bf16 v[168:171], a[192:195], v[52:55], v[168:171]\n"
    "v_mul_f32 v24, v24, v18\n"
    "v_mul_f32 v25, v25, v18\n"
    "v_mfma_f32_16x16x32_bf16 v[172:175], a[192:195], v[56:59], v[172:175]\n"
    "s_waitcnt lgkmcnt(8)\n"
    "v_cvt_pk_bf16_f32 v95, v24, v25\n"
    "v_cvt_pk_bf16_f32 v96, v96, v97\n"
    "v_accvgpr_read_b32 v24, a230\n"
    "v_accvgpr_read_b32 v25, a231\n"
    "v_cvt_pk_bf16_f32 v97, v98, v99\n"
    "v_pk_mul_f32 v[164:165], v[164:165], v[16:17] op_sel:[0,1] op_sel_hi:[1,1]\n"
    "v_pk_mul_f32 v[166:167], v[166:167], v[16:17] op_sel:[0,1] op_sel_hi:[1,1]\n"
    "v_pk_mul_f32 v[172:173], v[172:173], v[18:19] op_sel:[0,1] op_sel_hi:[1,1]\n"
    "v_pk_mul_f32 v[174:175], v[174:175], v[18:19] op_sel:[0,1] op_sel_hi:[1,1]\n"
    "v_pk_mul_f32 v[116:117], v[116:117], v[16:17] op_sel_hi:[1,0]\n"
    "v_pk_mul_f32 v[118:119], v[118:119], v[16:17] op_sel_hi:[1,0]\n"
    "v_pk_mul_f32 v[168:169], v[168:169], v[18:19] op_sel_hi:[1,0]\n"
    "v_pk_mul_f32 v[170:171], v[170:171], v[18:19] op_sel_hi:[1,0]\n"
    "v_mfma_f32_16x16x32_bf16 v[176:179], a[196:199], v[44:47], v[176:179]\n"
    "buffer_load_dwordx4 v6, s[40:43], s56 offen offset:0 nt lds\n"
    "v_mfma_f32_16x16x32_bf16 v[180:183], a[196:199], v[48:51], v[180:183]\n"
    "s_waitcnt vmcnt(16) lgkmcnt(0)\n"
    "v_mul_f32 v24, v24, v19\n"
    "v_mul_f32 v25, v25, v19\n"
    "v_mfma_f32_16x16x32_bf16 v[184:187], a[196:199], v[52:55], v[184:187]\n"
    "s_barrier\n"
    "ds_read_b128 a[192:195], v0 offset:24576\n"
    "v_cvt_pk_bf16_f32 v99, v24, v25\n"
    "v_mfma_f32_16x16x32_bf16 v[188:191], a[196:199], v[56:59], v[188:191]\n"
    "ds_read_b128 a[196:199], v1 offset:24576\n"
    "v_cvt_pk_bf16_f32 v132, v132, v133\n"
    "v_accvgpr_read_b32 v24, a234\n"
    "v_accvgpr_read_b32 v25, a235\n"
    "v_cvt_pk_bf16_f32 v133, v134, v135\n"
    "v_pk_mul_f32 v[180:181], v[180:181], v[16:17] op_sel:[0,1] op_sel_hi:[1,1]\n"
    "v_pk_mul_f32 v[182:183], v[182:183], v[16:17] op_sel:[0,1] op_sel_hi:[1,1]\n"
    "v_pk_mul_f32 v[184:185], v[184:185], v[18:19] op_sel_hi:[1,0]\n"
    "v_pk_mul_f32 v[188:189], v[188:189], v[18:19] op_sel:[0,1] op_sel_hi:[1,1]\n"
    "v_pk_mul_f32 v[190:191], v[190:191], v[18:19] op_sel:[0,1] op_sel_hi:[1,1]\n"
    "v_pk_mul_f32 v[186:187], v[186:187], v[18:19] op_sel_hi:[1,0]\n"
    "v_pk_mul_f32 v[160:161], v[160:161], v[16:17] op_sel_hi:[1,0]\n"
    "v_pk_mul_f32 v[162:163], v[162:163], v[16:17] op_sel_hi:[1,0]\n"
    "v_mfma_f32_16x16x32_bf16 v[192:195], a[200:203], v[44:47], v[192:195]\n"
    "v_mul_f32 v24, v24, v16\n"
    "v_mul_f32 v25, v25, v16\n"
    "v_mfma_f32_16x16x32_bf16 v[196:199], a[200:203], v[48:51], v[196:199]\n"
    "v_cvt_pk_bf16_f32 v135, v24, v25\n"
    "v_cmp_ngt_f32 vcc, s53, v27\n"
    "v_mfma_f32_16x16x32_bf16 v[200:203], a[200:203], v[52:55], v[200:203]\n"
    "v_mov_b32 v24, v12\n"
    "v_mov_b32 v25, v13\n"
    "v_mfma_f32_16x16x32_bf16 v[204:207], a[200:203], v[56:59], v[204:207]\n"
    "s_cmp_lg_u64 vcc, 0\ns_cselect_b32 s58, 1, 0\ns_or_b32 %0, %0, s58\n"
    "ds_read_b128 a[200:203], v0 offset:24704\n"
    "v_permlane32_swap_b32 v24, v12\n"
    "v_pk_mul_f32 v[136:137], v[136:137], v[16:17] op_sel:[0,1] op_sel_hi:[1,1]\n"
    "v_pk_mul_f32 v[138:139], v[138:139], v[16:17] op_sel:[0,1] op_sel_hi:[1,1]\n"
    "v_pk_mul_f32 v[148:149], v[148:149], v[16:17] op_sel:[0,1] op_sel_hi:[1,1]\n"
    "v_pk_mul_f32 v[150:151], v[150:151], v[16:17] op_sel:[0,1] op_sel_hi:[1,1]\n"
    "v_pk_mul_f32 v[176:177], v[176:177], v[16:17] op_sel_hi:[1,0]\n"
    "v_pk_mul_f32 v[178:179], v[178:179], v[16:17] op_sel_hi:[1,0]\n"
    "v_pk_mul_f32 v[140:141], v[140:141], v[18:19] op_sel_hi:[1,0]\n"
    "v_pk_mul_f32 v[142:143], v[142:143], v[18:19] op_sel_hi:[1,0]\n"
    "v_mfma_f32_16x16x32_bf16 v[208:211], a[204:207], v[44:47], v[208:211]\n"
    "v_max_f32 v12, v12, v24\n"
    "v_accvgpr_read_b32 v26, a216\n"
    "v_mfma_f32_16x16x32_bf16 v[212:215], a[204:207], v[48:51], v[212:215]\n"
    "v_mov_b32 v24, v12\n"
    "v_accvgpr_read_b32 v27, a217\n"
    "v_mfma_f32_16x16x32_bf16 v[216:219], a[204:207], v[52:55], v[216:219]\n"
    "v_permlane16_swap_b32 v24, v12\n"
    "v_mfma_f32_16x16x32_bf16 v[220:223], a[204:207], v[56:59], v[220:223]\n"
    "ds_read_b128 a[204:207], v1 offset:24704\n"
    "v_max_f32 v12, v12, v24\n"
    "v_permlane32_swap_b32 v25, v13\n"
    "v_mul_f32 v12, -s52, v12\n"
    "v_pk_mul_f32 v[144:145], v[144:145], v[18:19] op_sel:[0,1] op_sel_hi:[1,1]\n"
    "v_pk_mul_f32 v[146:147], v[146:147], v[18:19] op_sel:[0,1] op_sel_hi:[1,1]\n"
    "v_pk_mul_f32 v[156:157], v[156:157], v[18:19] op_sel:[0,1] op_sel_hi:[1,1]\n"
    "v_pk_mul_f32 v[158:159], v[158:159], v[18:19] op_sel:[0,1] op_sel_hi:[1,1]\n"
    "v_pk_mul_f32 v[204:205], v[204:205], v[18:19] op_sel:[0,1] op_sel_hi:[1,1]\n"
    "v_pk_mul_f32 v[206:207], v[206:207], v[18:19] op_sel:[0,1] op_sel_hi:[1,1]\n"
    "v_pk_mul_f32 v[152:153], v[152:153], v[18:19] op_sel_hi:[1,0]\n"
    "v_pk_mul_f32 v[154:155], v[154:155], v[18:19] op_sel_hi:[1,0]\n"
    "v_mfma_f32_16x16x32_bf16 v[224:227], a[208:211], v[44:47], v[224:227]\n"
    "v_add_f32 v12, 0xc2200000, v12\n"
    "v_max_f32 v13, v13, v25\n"
    "v_mfma_f32_16x16x32_bf16 v[228:231], a[208:211], v[48:51], v[228:231]\n"
    "v_mov_b32 v25, v13\n"
    "v_fma_f32 v28, v28, s52, v12\n"
    "v_mfma_f32_16x16x32_bf16 v[232:235], a[208:211], v[52:55], v[232:235]\n"
    "buffer_load_dwordx4 v7, s[40:43], s56 offen offset:1024 nt lds\n"
    "v_mfma_f32_16x16x32_bf16 v[236:239], a[208:211], v[56:59], v[236:239]\n"
    "ds_read_b128 a[208:211], v0 offset:24832\n"
    "v_fma_f32 v29, v29, s52, v12\n"
    "v_permlane16_swap_b32 v25, v13\n"
    "v_fma_f32 v30, v30, s52, v12\n"
    "v_pk_mul_f32 v[220:221], v[220:221], v[18:19] op_sel:[0,1] op_sel_hi:[1,1]\n"
    "v_pk_mul_f32 v[222:223], v[222:223], v[18:19] op_sel:[0,1] op_sel_hi:[1,1]\n"
    "v_pk_mul_f32 v[196:197], v[196:197], v[16:17] op_sel:[0,1] op_sel_hi:[1,1]\n"
    "v_pk_mul_f32 v[198:199], v[198:199], v[16:17] op_sel:[0,1] op_sel_hi:[1,1]\n"
    "v_pk_mul_f32 v[200:201], v[200:201], v[18:19] op_sel_hi:[1,0]\n"
    "v_pk_mul_f32 v[202:203], v[202:203], v[18:19] op_sel_hi:[1,0]\n"
    "v_pk_mul_f32 v[216:217], v[216:217], v[18:19] op_sel_hi:[1,0]\n"
    "v_pk_mul_f32 v[218:219], v[218:219], v[18:19] op_sel_hi:[1,0]\n"
    "v_mfma_f32_16x16x32_bf16 v[240:243], a[212:215], v[44:47], v[240:243]\n"
    "v_exp_f32 v28, v28\n"
    "v_mfma_f32_16x16x32_bf16 v[244:247], a[212:215], v[48:51], v[244:247]\n"
    "v_exp_f32 v29, v29\n"
    "v_mfma_f32_16x16x32_bf16 v[248:251], a[212:215], v[52:55], v[248:251]\n"
    "v_fma_f32 v31, v31, s52, v12\n"
    "v_fma_f32 v76, v76, s52, v12\n"
    "v_mfma_f32_16x16x32_bf16 v[252:255], a[212:215], v[56:59], v[252:255]\n"
    "s_waitcnt lgkmcnt(4)\n"
    "ds_read_b128 a[212:215], v1 offset:24832\n"
    "v_exp_f32 v30, v30\n"
    "v_exp_f32 v31, v31\n"
    "v_pk_mul_f32 v[212:213], v[212:213], v[16:17] op_sel:[0,1] op_sel_hi:[1,1]\n"
    "v_pk_mul_f32 v[214:215], v[214:215], v[16:17] op_sel:[0,1] op_sel_hi:[1,1]\n"
    "v_pk_mul_f32 v[192:193], v[192:193], v[16:17] op_sel_hi:[1,0]\n"
    "v_pk_mul_f32 v[194:195], v[194:195], v[16:17] op_sel_hi:[1,0]\n"
    "v_pk_mul_f32 v[208:209], v[208:209], v[16:17] op_sel_hi:[1,0]\n"
    "v_pk_mul_f32 v[210:211], v[210:211], v[16:17] op_sel_hi:[1,0]\n"
    "v_pk_mul_f32 v[224:225], v[224:225], v[16:17] op_sel_hi:[1,0]\n"
    "v_pk_mul_f32 v[226:227], v[226:227], v[16:17] op_sel_hi:[1,0]\n"
    "v_mfma_f32_16x16x32_bf16 v[44:47], a[192:195], a[96:99], 0\n"
    "s_waitcnt lgkmcnt(4)\n"
    "v_fma_f32 v77, v77, s52, v12\n"
    "v_fma_f32 v78, v78, s52, v12\n"
    "v_mfma_f32_16x16x32_bf16 v[44:47], a[196:199], a[100:103], v[44:47]\n"
    "s_waitcnt lgkmcnt(3)\n"
    "v_fma_f32 v79, v79, s52, v12\n"
    "v_max_f32 v13, v13, v25\n"
    "v_mfma_f32_16x16x32_bf16 v[44:47], a[200:203], a[104:107], v[44:47]\n"
    "v_cvt_pk_bf16_f32 v28, v28, v29\n"
    "v_mul_f32 v13, -s52, v13\n"
    "v_mfma_f32_16x16x32_bf16 v[48:51], a[200:203], a[128:131], 0\n"
    "v_exp_f32 v76, v76\n"
    "v_exp_f32 v77, v77\n"
    "v_cvt_pk_bf16_f32 v29, v30, v31\n"
    "v_pk_mul_f32 v[228:229], v[228:229], v[16:17] op_sel:[0,1] op_sel_hi:[1,1]\n"
    "v_pk_mul_f32 v[230:231], v[230:231], v[16:17] op_sel:[0,1] op_sel_hi:[1,1]\n"
    "v_pk_mul_f32 v[244:245], v[244:245], v[16:17] op_sel:[0,1] op_sel_hi:[1,1]\n"
    "v_pk_mul_f32 v[246:247], v[246:247], v[16:17] op_sel:[0,1] op_sel_hi:[1,1]\n"
    "v_pk_mul_f32 v[240:241], v[240:241], v[16:17] op_sel_hi:[1,0]\n"
    "v_pk_mul_f32 v[242:243], v[242:243], v[16:17] op_sel_hi:[1,0]\n"
    "v_pk_mul_f32 v[232:233], v[232:233], v[18:19] op_sel_hi:[1,0]\n"
    "v_pk_mul_f32 v[234:235], v[234:235], v[18:19] op_sel_hi:[1,0]\n"
    "v_mfma_f32_16x16x32_bf16 v[48:51], a[196:199], a[124:127], v[48:51]\n"
    "v_exp_f32 v78, v78\n"
    "v_mfma_f32_16x16x32_bf16 v[48:51], a[192:195], a[120:123], v[48:51]\n"
    "v_exp_f32 v79, v79\n"
    "v_mfma_f32_16x16x32_bf16 v[52:55], a[192:195], a[144:147], 0\n"
    "v_add_f32 v13, 0xc2200000, v13\n"
    "v_cvt_pk_bf16_f32 v30, v76, v77\n"
    "v_mfma_f32_16x16x32_bf16 v[52:55], a[196:199], a[148:151], v[52:55]\n"
    "v_cvt_pk_bf16_f32 v31, v78, v79\n"
    "v_fma_f32 v32, v32, s52, v13\n"
    "v_fma_f32 v33, v33, s52, v13\n"
    "v_exp_f32 v32, v32\n"
    "v_pk_mul_f32 v[236:237], v[236:237], v[18:19] op_sel:[0,1] op_sel_hi:[1,1]\n"
    "v_pk_mul_f32 v[238:239], v[238:239], v[18:19] op_sel:[0,1] op_sel_hi:[1,1]\n"
    "v_pk_mul_f32 v[252:253], v[252:253], v[18:19] op_sel:[0,1] op_sel_hi:[1,1]\n"
    "v_pk_mul_f32 v[254:255], v[254:255], v[18:19] op_sel:[0,1] op_sel_hi:[1,1]\n"
    "v_pk_mul_f32 v[248:249], v[248:249], v[18:19] op_sel_hi:[1,0]\n"
    "v_pk_mul_f32 v[250:251], v[250:251], v[18:19] op_sel_hi:[1,0]\n"
    "v_mfma_f32_16x16x32_bf16 v[52:55], a[200:203], a[152:155], v[52:55]\n"
    "buffer_load_dwordx4 v8, s[40:43], s56 offen offset:2048 nt lds\n"
    "v_mfma_f32_16x16x32_bf16 v[56:59], a[200:203], a[176:179], 0\n"
    "s_add_u32 s56, s56, 24576\n"
    "ds_read_b128 a[200:203], v0 offset:30848\n"
    "v_fma_f32 v34, v34, s52, v13\n"
    "v_mfma_f32_16x16x32_bf16 v[56:59], a[196:199], a[172:175], v[56:59]\n"
    "s_add_u32 m0, s54, 12288\n"
    "ds_read_b128 a[196:199], v1 offset:30720\n"
    "v_fma_f32 v35, v35, s52, v13\n"
    "v_mfma_f32_16x16x32_bf16 v[56:59], a[192:195], a[168:171], v[56:59]\n"
    "s_waitcnt lgkmcnt(4)\n"
    "ds_read_b128 a[192:195], v0 offset:30720\n"
    "v_fma_f32 v80, v80, s52, v13\n"
    "v_mfma_f32_16x16x32_bf16 v[44:47], a[204:207], a[108:111], v[44:47]\n"
    "s_waitcnt lgkmcnt(4)\n"
    "v_exp_f32 v33, v33\n"
    "v_mfma_f32_16x16x32_bf16 v[44:47], a[208:211], a[112:115], v[44:47]\n"
    "s_waitcnt lgkmcnt(3)\n"
    "v_exp_f32 v34, v34\n"
    "v_mfma_f32_16x16x32_bf16 v[44:47], a[212:215], a[116:119], v[44:47]\n"
    "v_exp_f32 v35, v35\n"
    "v_mfma_f32_16x16x32_bf16 v[48:51], a[212:215], a[140:143], v[48:51]\n"
    "v_fma_f32 v81, v81, s52, v13\n"
    "v_fma_f32 v82, v82, s52, v13\n"
    "v_mfma_f32_16x16x32_bf16 v[48:51], a[208:211], a[136:139], v[48:51]\n"
    "v_fma_f32 v83, v83, s52, v13\n"
    "v_cvt_pk_bf16_f32 v32, v32, v33\n"
    "v_mfma_f32_16x16x32_bf16 v[48:51], a[204:207], a[132:135], v[48:51]\n"
    "v_exp_f32 v80, v80\n"
    "v_mfma_f32_16x16x32_bf16 v[52:55], a[204:207], a[156:159], v[52:55]\n"
    "v_exp_f32 v81, v81\n"
    "v_mfma_f32_16x16x32_bf16 v[52:55], a[208:211], a[160:163], v[52:55]\n"
    "v_exp_f32 v82, v82\n"
    "v_mfma_f32_16x16x32_bf16 v[52:55], a[212:215], a[164:167], v[52:55]\n"
    "v_exp_f32 v83, v83\n"
    "v_mfma_f32_16x16x32_bf16 v[56:59], a[212:215], a[188:191], v[56:59]\n"
    "ds_read_b128 a[212:215], v1 offset:30976\n"
    "v_cvt_pk_bf16_f32 v33, v34, v35\n"
    "v_mfma_f32_16x16x32_bf16 v[56:59], a[208:211], a[184:187], v[56:59]\n"
    "ds_read_b128 a[208:211], v0 offset:30976\n"
    "v_cvt_pk_bf16_f32 v34, v80, v81\n"
    "v_mfma_f32_16x16x32_bf16 v[56:59], a[204:207], a[180:183], v[56:59]\n"
    "s_waitcnt lgkmcnt(2)\n"
    "buffer_load_dwordx4 v6, s[36:39], s56 offen offset:0 nt lds\n"
    "v_mfma_f32_16x16x32_bf16 v[68:71], a[192:195], a[96:99], 0\n"
    "ds_read_b128 a[204:207], v1 offset:30848\n"
    "v_cvt_pk_bf16_f32 v35, v82, v83\n"
    "v_mfma_f32_16x16x32_bf16 v[68:71], a[196:199], a[100:103], v[68:71]\n"
    "v_mul_f32 v26, v26, v16\n"
    "v_mul_f32 v27, v27, v16\n"
    "v_mfma_f32_16x16x32_bf16 v[68:71], a[200:203], a[104:107], v[68:71]\n"
    "v_cvt_pk_bf16_f32 v86, v26, v27\n"
    "v_cvt_pk_bf16_f32 v112, v112, v113\n"
    "v_mfma_f32_16x16x32_bf16 v[72:75], a[200:203], a[128:131], 0\n"
    "v_accvgpr_read_b32 v26, a220\n"
    "v_accvgpr_read_b32 v27, a221\n"
    "v_mfma_f32_16x16x32_bf16 v[72:75], a[196:199], a[124:127], v[72:75]\n"
    "v_mul_f32 v26, v26, v17\n"
    "v_mul_f32 v27, v27, v17\n"
    "v_mfma_f32_16x16x32_bf16 v[72:75], a[192:195], a[120:123], v[72:75]\n"
    "v_cvt_pk_bf16_f32 v90, v26, v27\n"
    "v_cvt_pk_bf16_f32 v113, v114, v115\n"
    "v_mfma_f32_16x16x32_bf16 v[76:79], a[192:195], a[144:147], 0\n"
    "v_accvgpr_read_b32 v26, a224\n"
    "v_accvgpr_read_b32 v27, a225\n"
    "v_mfma_f32_16x16x32_bf16 v[76:79], a[196:199], a[148:151], v[76:79]\n"
    "v_mul_f32 v26, v26, v18\n"
    "v_mul_f32 v27, v27, v18\n"
    "v_mfma_f32_16x16x32_bf16 v[76:79], a[200:203], a[152:155], v[76:79]\n"
    "v_cvt_pk_bf16_f32 v94, v26, v27\n"
    "v_cvt_pk_bf16_f32 v108, v108, v109\n"
    "v_mfma_f32_16x16x32_bf16 v[80:83], a[200:203], a[176:179], 0\n"
    "v_accvgpr_read_b32 v26, a228\n"
    "v_accvgpr_read_b32 v27, a229\n"
    "v_mfma_f32_16x16x32_bf16 v[80:83], a[196:199], a[172:175], v[80:83]\n"
    "v_mul_f32 v26, v26, v19\n"
    "v_mul_f32 v27, v27, v19\n"
    "v_mfma_f32_16x16x32_bf16 v[80:83], a[192:195], a[168:171], v[80:83]\n"
    "s_waitcnt lgkmcnt(0)\n"
    "ds_read_b64_tr_b16 a[192:193], v2 offset:12288\n"
    "v_cvt_pk_bf16_f32 v98, v26, v27\n"
    "v_mfma_f32_16x16x32_bf16 v[68:71], a[204:207], a[108:111], v[68:71]\n"
    "v_accvgpr_read_b32 v26, a232\n"
    "v_accvgpr_read_b32 v27, a233\n"
    "v_mfma_f32_16x16x32_bf16 v[68:71], a[208:211], a[112:115], v[68:71]\n"
    "buffer_load_dwordx4 v7, s[36:39], s56 offen offset:1024 nt lds\n"
    "v_mfma_f32_16x16x32_bf16 v[68:71], a[212:215], a[116:119], v[68:71]\n"
    "v_mul_f32 v26, v26, v16\n"
    "v_mul_f32 v27, v27, v16\n"
    "v_mfma_f32_16x16x32_bf16 v[72:75], a[212:215], a[140:143], v[72:75]\n"
    "ds_read_b64_tr_b16 a[194:195], v2 offset:18432\n"
    "v_cvt_pk_bf16_f32 v134, v26, v27\n"
    "v_mfma_f32_16x16x32_bf16 v[72:75], a[208:211], a[136:139], v[72:75]\n"
    "v_mov_b32 v27, v15\n"
    "v_mov_b32 v26, v14\n"
    "v_mfma_f32_16x16x32_bf16 v[72:75], a[204:207], a[132:135], v[72:75]\n"
    "v_permlane32_swap_b32 v27, v15\n"
    "v_mfma_f32_16x16x32_bf16 v[76:79], a[204:207], a[156:159], v[76:79]\n"
    "v_permlane32_swap_b32 v26, v14\n"
    "v_mfma_f32_16x16x32_bf16 v[76:79], a[208:211], a[160:163], v[76:79]\n"
    "v_max_f32 v15, v15, v27\n"
    "v_max_f32 v14, v14, v26\n"
    "v_mfma_f32_16x16x32_bf16 v[76:79], a[212:215], a[164:167], v[76:79]\n"
    "v_mov_b32 v27, v15\n"
    "v_mov_b32 v26, v14\n"
    "v_mfma_f32_16x16x32_bf16 v[80:83], a[212:215], a[188:191], v[80:83]\n"
    "v_permlane16_swap_b32 v27, v15\n"
    "v_mfma_f32_16x16x32_bf16 v[80:83], a[208:211], a[184:187], v[80:83]\n"
    "v_permlane16_swap_b32 v26, v14\n"
    "v_mfma_f32_16x16x32_bf16 v[80:83], a[204:207], a[180:183], v[80:83]\n"
    "ds_read_b64_tr_b16 a[196:197], v3 offset:12288\n"
    "v_max_f32 v15, v15, v27\n"
    "v_smfmac_f32_16x16x64_bf16 a[244:247], a[252:255], v[28:35], v23\n"
    "ds_read_b64_tr_b16 a[198:199], v3 offset:18432\n"
    "v_max_f32 v14, v14, v26\n"
    "v_mul_f32 v15, -s52, v15\n"
    "v_mul_f32 v14, -s52, v14\n"
    "v_add_f32 v15, 0xc2200000, v15\n"
    "v_mov_b32 v27, 0xf149f2ca\n"
    "v_add_f32 v14, 0xc2200000, v14\n"
    "v_cndmask_b32 v15, v15, v27, s[60:61]\n"
    "v_fma_f32 v36, v36, s52, v14\n"
    "v_fma_f32 v37, v37, s52, v14\n"
    "v_fma_f32 v40, v40, s52, v15\n"
    "v_fma_f32 v41, v41, s52, v15\n"
    "v_exp_f32 v36, v36\n"
    "v_exp_f32 v37, v37\n"
    "v_fma_f32 v38, v38, s52, v14\n"
    "v_fma_f32 v39, v39, s52, v14\n"
    "v_exp_f32 v40, v40\n"
    "v_exp_f32 v41, v41\n"
    "v_fma_f32 v42, v42, s52, v15\n"
    "v_fma_f32 v43, v43, s52, v15\n"
    "v_exp_f32 v38, v38\n"
    "v_exp_f32 v39, v39\n"
    "v_exp_f32 v42, v42\n"
    "v_exp_f32 v43, v43\n"
    "v_fma_f32 v60, v60, s52, v14\n"
    "v_fma_f32 v61, v61, s52, v14\n"
    "v_fma_f32 v62, v62, s52, v14\n"
    "v_fma_f32 v63, v63, s52, v14\n"
    "v_fma_f32 v64, v64, s52, v15\n"
    "v_fma_f32 v65, v65, s52, v15\n"
    "v_fma_f32 v66, v66, s52, v15\n"
    "v_fma_f32 v67, v67, s52, v15\n"
    "v_cvt_pk_bf16_f32 v36, v36, v37\n"
    "v_cvt_pk_bf16_f32 v40, v40, v41\n"
    "v_exp_f32 v60, v60\n"
    "v_exp_f32 v61, v61\n"
    "v_exp_f32 v62, v62\n"
    "v_exp_f32 v63, v63\n"
    "v_exp_f32 v64, v64\n"
    "v_exp_f32 v65, v65\n"
    "v_exp_f32 v66, v66\n"
    "v_exp_f32 v67, v67\n"
    "v_cvt_pk_bf16_f32 v37, v38, v39\n"
    "v_cvt_pk_bf16_f32 v41, v42, v43\n"
    "v_cvt_pk_bf16_f32 v38, v60, v61\n"
    "v_cvt_pk_bf16_f32 v39, v62, v63\n"
    "v_cvt_pk_bf16_f32 v42, v64, v65\n"
    "v_cvt_pk_bf16_f32 v43, v66, v67\n"
    "s_nop 1\n"
    "v_smfmac_f32_16x16x64_bf16 a[248:251], a[252:255], v[36:43], v23\n"
    "s_waitcnt lgkmcnt(2)\n"
    "ds_read_b64_tr_b16 a[200:201], v4 offset:12288\n"
    "v_cvt_pk_bf16_f32 v114, v128, v129\n"
    "v_mfma_f32_16x16x32_bf16 a[12:15], a[192:195], v[40:43], 0\n"
    "ds_read_b64_tr_b16 a[202:203], v4 offset:18432\n"
    "v_cvt_pk_bf16_f32 v115, v130, v131\n"
    "v_mfma_f32_16x16x32_bf16 a[8:11], a[192:195], v[36:39], 0\n"
    "ds_read_b64_tr_b16 a[204:205], v5 offset:12288\n"
    "v_cvt_pk_bf16_f32 v109, v110, v111\n"
    "v_mfma_f32_16x16x32_bf16 a[4:7], a[192:195], v[32:35], 0\n"
    "ds_read_b64_tr_b16 a[206:207], v5 offset:18432\n"
    "v_cvt_pk_bf16_f32 v104, v104, v105\n"
    "v_mfma_f32_16x16x32_bf16 a[0:3], a[192:195], v[28:31], 0\n"
    "s_waitcnt lgkmcnt(4)\n"
    "buffer_load_dwordx4 v8, s[36:39], s56 offen offset:2048 nt lds\n"
    "v_mfma_f32_16x16x32_bf16 a[16:19], a[196:199], v[28:31], 0\n"
    "s_add_u32 m0, s54, 61440\n"
    "ds_read_b64_tr_b16 a[208:209], v2 offset:12416\n"
    "v_cvt_pk_bf16_f32 v110, v124, v125\n"
    "v_mfma_f32_16x16x32_bf16 a[20:23], a[196:199], v[32:35], 0\n"
    "ds_read_b64_tr_b16 a[210:211], v2 offset:18560\n"
    "v_cvt_pk_bf16_f32 v111, v126, v127\n"
    "v_mfma_f32_16x16x32_bf16 a[24:27], a[196:199], v[36:39], 0\n"
    "ds_read_b64_tr_b16 a[212:213], v3 offset:12416\n"
    "v_cvt_pk_bf16_f32 v105, v106, v107\n"
    "v_mfma_f32_16x16x32_bf16 a[28:31], a[196:199], v[40:43], 0\n"
    "s_waitcnt lgkmcnt(5)\n"
    "ds_read_b64_tr_b16 a[214:215], v3 offset:18560\n"
    "v_cvt_pk_bf16_f32 v100, v100, v101\n"
    "v_mfma_f32_16x16x32_bf16 a[44:47], a[200:203], v[40:43], 0\n"
    "ds_read_b64_tr_b16 a[192:193], v4 offset:12416\n"
    "v_cvt_pk_bf16_f32 v106, v120, v121\n"
    "v_mfma_f32_16x16x32_bf16 a[40:43], a[200:203], v[36:39], 0\n"
    "ds_read_b64_tr_b16 a[194:195], v4 offset:18560\n"
    "v_cvt_pk_bf16_f32 v107, v122, v123\n"
    "v_mfma_f32_16x16x32_bf16 a[36:39], a[200:203], v[32:35], 0\n"
    "ds_read_b64_tr_b16 a[196:197], v5 offset:12416\n"
    "v_cvt_pk_bf16_f32 v101, v102, v103\n"
    "v_mfma_f32_16x16x32_bf16 a[32:35], a[200:203], v[28:31], 0\n"
    "s_waitcnt lgkmcnt(7)\n"
    "ds_read_b64_tr_b16 a[198:199], v5 offset:18560\n"
    "v_cvt_pk_bf16_f32 v102, v116, v117\n"
    "v_mfma_f32_16x16x32_bf16 a[48:51], a[204:207], v[28:31], 0\n"
    "ds_read_b64_tr_b16 a[200:201], v2 offset:12544\n"
    "v_cvt_pk_bf16_f32 v103, v118, v119\n"
    "v_mfma_f32_16x16x32_bf16 a[52:55], a[204:207], v[32:35], 0\n"
    "ds_read_b64_tr_b16 a[202:203], v2 offset:18688\n"
    "v_cvt_pk_bf16_f32 v172, v172, v173\n"
    "v_mfma_f32_16x16x32_bf16 a[56:59], a[204:207], v[36:39], 0\n"
    "v_cvt_pk_bf16_f32 v173, v174, v175\n"
    "v_cvt_pk_bf16_f32 v168, v168, v169\n"
    "v_mfma_f32_16x16x32_bf16 a[60:63], a[204:207], v[40:43], 0\n"
    "s_waitcnt lgkmcnt(8)\n"
    "ds_read_b64_tr_b16 a[204:205], v3 offset:12544\n"
    "v_cvt_pk_bf16_f32 v174, v188, v189\n"
    "v_mfma_f32_16x16x32_bf16 a[76:79], a[208:211], v[40:43], 0\n"
    "buffer_load_dwordx4 v6, s[40:43], s55 offen offset:0 nt lds\n"
    "v_mfma_f32_16x16x32_bf16 a[72:75], a[208:211], v[36:39], 0\n"
    "ds_read_b64_tr_b16 a[206:207], v3 offset:18688\n"
    "v_cvt_pk_bf16_f32 v175, v190, v191\n"
    "v_mfma_f32_16x16x32_bf16 a[68:71], a[208:211], v[32:35], 0\n"
    "v_cvt_pk_bf16_f32 v169, v170, v171\n"
    "v_cvt_pk_bf16_f32 v164, v164, v165\n"
    "v_mfma_f32_16x16x32_bf16 a[64:67], a[208:211], v[28:31], 0\n"
    "s_waitcnt lgkmcnt(8)\n"
    "ds_read_b64_tr_b16 a[208:209], v4 offset:12544\n"
    "v_cvt_pk_bf16_f32 v170, v184, v185\n"
    "v_mfma_f32_16x16x32_bf16 a[80:83], a[212:215], v[28:31], 0\n"
    "ds_read_b64_tr_b16 a[210:211], v4 offset:18688\n"
    "v_cvt_pk_bf16_f32 v171, v186, v187\n"
    "v_mfma_f32_16x16x32_bf16 a[84:87], a[212:215], v[32:35], 0\n"
    "v_cvt_pk_bf16_f32 v165, v166, v167\n"
    "v_cvt_pk_bf16_f32 v160, v160, v161\n"
    "v_mfma_f32_16x16x32_bf16 a[88:91], a[212:215], v[36:39], 0\n"
    "v_cvt_pk_bf16_f32 v166, v180, v181\n"
    "v_cvt_pk_bf16_f32 v167, v182, v183\n"
    "v_mfma_f32_16x16x32_bf16 a[92:95], a[212:215], v[40:43], 0\n"
    "s_waitcnt lgkmcnt(8)\n"
    "ds_read_b64_tr_b16 a[212:213], v5 offset:12544\n"
    "v_cvt_pk_bf16_f32 v161, v162, v163\n"
    "v_mfma_f32_16x16x32_bf16 v[128:131], a[192:195], v[40:43], 0\n"
    "ds_read_b64_tr_b16 a[214:215], v5 offset:18688\n"
    "v_cvt_pk_bf16_f32 v162, v176, v177\n"
    "v_mfma_f32_16x16x32_bf16 v[124:127], a[192:195], v[36:39], 0\n"
    "v_cvt_pk_bf16_f32 v163, v178, v179\n"
    "v_cvt_pk_bf16_f32 v136, v136, v137\n"
    "v_mfma_f32_16x16x32_bf16 v[120:123], a[192:195], v[32:35], 0\n"
    "v_cvt_pk_bf16_f32 v137, v138, v139\n"
    "v_cvt_pk_bf16_f32 v140, v140, v141\n"
    "v_mfma_f32_16x16x32_bf16 v[116:119], a[192:195], v[28:31], 0\n"
    "s_waitcnt lgkmcnt(8)\n"
    "v_cvt_pk_bf16_f32 v138, v148, v149\n"
    "v_cvt_pk_bf16_f32 v139, v150, v151\n"
    "v_mfma_f32_16x16x32_bf16 a[216:219], a[196:199], v[28:31], 0\n"
    "v_cvt_pk_bf16_f32 v141, v142, v143\n"
    "v_cvt_pk_bf16_f32 v144, v144, v145\n"
    "v_mfma_f32_16x16x32_bf16 a[220:223], a[196:199], v[32:35], 0\n"
    "v_cvt_pk_bf16_f32 v142, v152, v153\n"
    "v_cvt_pk_bf16_f32 v143, v154, v155\n"
    "v_mfma_f32_16x16x32_bf16 a[224:227], a[196:199], v[36:39], 0\n"
    "s_waitcnt vmcnt(16) lgkmcnt(0)\n"
    "buffer_load_dwordx4 v7, s[40:43], s55 offen offset:1024 nt lds\n"
    "v_mfma_f32_16x16x32_bf16 a[228:231], a[196:199], v[40:43], 0\n"
    "s_barrier\n"
    "ds_read_b128 a[192:195], v0 offset:36864\n"
    "v_cvt_pk_bf16_f32 v145, v146, v147\n"
    "v_mfma_f32_16x16x32_bf16 v[188:191], a[200:203], v[40:43], 0\n"
    "v_cvt_pk_bf16_f32 v146, v156, v157\n"
    "v_cvt_pk_bf16_f32 v147, v158, v159\n"
    "v_mfma_f32_16x16x32_bf16 v[184:187], a[200:203], v[36:39], 0\n"
    "ds_read_b128 a[196:199], v1 offset:36864\n"
    "v_cvt_pk_bf16_f32 v204, v204, v205\n"
    "v_mfma_f32_16x16x32_bf16 v[180:183], a[200:203], v[32:35], 0\n"
    "v_cvt_pk_bf16_f32 v205, v206, v207\n"
    "v_cvt_pk_bf16_f32 v200, v200, v201\n"
    "v_mfma_f32_16x16x32_bf16 v[176:179], a[200:203], v[28:31], 0\n"
    "v_cvt_pk_bf16_f32 v206, v220, v221\n"
    "v_cvt_pk_bf16_f32 v207, v222, v223\n"
    "v_mfma_f32_16x16x32_bf16 a[232:235], a[204:207], v[28:31], 0\n"
    "ds_read_b128 a[200:203], v0 offset:36992\n"
    "v_cvt_pk_bf16_f32 v201, v202, v203\n"
    "v_mfma_f32_16x16x32_bf16 v[148:151], a[204:207], v[32:35], 0\n"
    "ds_write_b64 v10, v[84:85] offset:0\n"
    "v_mfma_f32_16x16x32_bf16 v[152:155], a[204:207], v[36:39], 0\n"
    "v_cvt_pk_bf16_f32 v196, v196, v197\n"
    "v_cvt_pk_bf16_f32 v202, v216, v217\n"
    "v_mfma_f32_16x16x32_bf16 v[156:159], a[204:207], v[40:43], 0\n"
    "v_cvt_pk_bf16_f32 v203, v218, v219\n"
    "v_cvt_pk_bf16_f32 v197, v198, v199\n"
    "v_mfma_f32_16x16x32_bf16 v[220:223], a[208:211], v[40:43], 0\n"
    "v_cvt_pk_bf16_f32 v192, v192, v193\n"
    "v_cvt_pk_bf16_f32 v198, v212, v213\n"
    "v_mfma_f32_16x16x32_bf16 v[216:219], a[208:211], v[36:39], 0\n"
    "v_cvt_pk_bf16_f32 v199, v214, v215\n"
    "v_cvt_pk_bf16_f32 v193, v194, v195\n"
    "v_mfma_f32_16x16x32_bf16 v[212:215], a[208:211], v[32:35], 0\n"
    "v_cvt_pk_bf16_f32 v194, v208, v209\n"
    "v_cvt_pk_bf16_f32 v195, v210, v211\n"
    "v_mfma_f32_16x16x32_bf16 v[208:211], a[208:211], v[28:31], 0\n"
    "v_cvt_pk_bf16_f32 v224, v224, v225\n"
    "v_cvt_pk_bf16_f32 v228, v228, v229\n"
    "v_cvt_pk_bf16_f32 v225, v226, v227\n"
    "v_cvt_pk_bf16_f32 v226, v240, v241\n"
    "v_cvt_pk_bf16_f32 v227, v242, v243\n"
    "v_mfma_f32_16x16x32_bf16 v[240:243], a[212:215], v[28:31], 0\n"
    "ds_write_b64 v10, v[86:87] offset:32\n"
    "v_cvt_pk_bf16_f32 v229, v230, v231\n"
    "v_cvt_pk_bf16_f32 v230, v244, v245\n"
    "v_cvt_pk_bf16_f32 v231, v246, v247\n"
    "v_mfma_f32_16x16x32_bf16 v[244:247], a[212:215], v[32:35], 0\n"
    "v_cvt_pk_bf16_f32 v232, v232, v233\n"
    "v_cvt_pk_bf16_f32 v236, v236, v237\n"
    "v_cvt_pk_bf16_f32 v233, v234, v235\n"
    "v_cvt_pk_bf16_f32 v234, v248, v249\n"
    "v_cvt_pk_bf16_f32 v235, v250, v251\n"
    "v_mfma_f32_16x16x32_bf16 v[248:251], a[212:215], v[36:39], 0\n"
    "buffer_load_dwordx4 v8, s[40:43], s55 offen offset:2048 nt lds\n"
    "v_cvt_pk_bf16_f32 v237, v238, v239\n"
    "v_cvt_pk_bf16_f32 v238, v252, v253\n"
    "v_cvt_pk_bf16_f32 v239, v254, v255\n"
    "v_mfma_f32_16x16x32_bf16 v[252:255], a[212:215], v[40:43], 0\n"
    "s_waitcnt lgkmcnt(4)\n"
    "ds_write_b64 v10, v[100:101] offset:64\n"
    "v_mfma_f32_16x16x32_bf16 v[28:31], a[192:195], a[96:99], 0\n"
    "s_waitcnt lgkmcnt(4)\n"
    "ds_read_b128 a[204:207], v1 offset:36992\n"
    "v_fma_f32 v44, v44, s52, v12\n"
    "v_mfma_f32_16x16x32_bf16 v[28:31], a[196:199], a[100:103], v[28:31]\n"
    "s_waitcnt lgkmcnt(4)\n"
    "ds_read_b128 a[208:211], v0 offset:37120\n"
    "v_fma_f32 v45, v45, s52, v12\n"
    "v_mfma_f32_16x16x32_bf16 v[28:31], a[200:203], a[104:107], v[28:31]\n"
    "s_add_u32 s55, s55, 24576\n"
    "ds_read_b128 a[212:215], v1 offset:37120\n"
    "v_fma_f32 v46, v46, s52, v12\n"
    "v_mfma_f32_16x16x32_bf16 v[32:35], a[200:203], a[128:131], 0\n"
    "s_add_u32 m0, s54, 24576\n"
    "v_exp_f32 v44, v44\n"
    "v_mfma_f32_16x16x32_bf16 v[32:35], a[196:199], a[124:127], v[32:35]\n"
    "ds_write_b64 v10, v[102:103] offset:96\n"
    "v_mfma_f32_16x16x32_bf16 v[32:35], a[192:195], a[120:123], v[32:35]\n"
    "v_exp_f32 v45, v45\n"
    "v_mfma_f32_16x16x32_bf16 v[36:39], a[192:195], a[144:147], 0\n"
    "v_fma_f32 v47, v47, s52, v12\n"
    "v_fma_f32 v68, v68, s52, v12\n"
    "v_mfma_f32_16x16x32_bf16 v[36:39], a[196:199], a[148:151], v[36:39]\n"
    "v_exp_f32 v46, v46\n"
    "v_mfma_f32_16x16x32_bf16 v[36:39], a[200:203], a[152:155], v[36:39]\n"
    "v_exp_f32 v47, v47\n"
    "v_mfma_f32_16x16x32_bf16 v[40:43], a[200:203], a[176:179], 0\n"
    "ds_read_b128 a[200:203], v0 offset:43136\n"
    "v_fma_f32 v69, v69, s52, v12\n"
    "v_mfma_f32_16x16x32_bf16 v[40:43], a[196:199], a[172:175], v[40:43]\n"
    "ds_read_b128 a[196:199], v1 offset:43008\n"
    "v_fma_f32 v70, v70, s52, v12\n"
    "v_mfma_f32_16x16x32_bf16 v[40:43], a[192:195], a[168:171], v[40:43]\n"
    "s_waitcnt lgkmcnt(5)\n"
    "buffer_load_dwordx4 v6, s[36:39], s55 offen offset:0 nt lds\n"
    "v_mfma_f32_16x16x32_bf16 v[28:31], a[204:207], a[108:111], v[28:31]\n"
    "s_waitcnt lgkmcnt(4)\n"
    "ds_read_b128 a[192:195], v0 offset:43008\n"
    "v_fma_f32 v71, v71, s52, v12\n"
    "v_mfma_f32_16x16x32_bf16 v[28:31], a[208:211], a[112:115], v[28:31]\n"
    "s_waitcnt lgkmcnt(4)\n"
    "ds_read_b64 v[84:85], v11 offset:0\n"
    "v_cvt_pk_bf16_f32 v44, v44, v45\n"
    "v_mfma_f32_16x16x32_bf16 v[28:31], a[212:215], a[116:119], v[28:31]\n"
    "ds_read_b64 v[86:87], v20 offset:0\n"
    "v_cvt_pk_bf16_f32 v45, v46, v47\n"
    "v_mfma_f32_16x16x32_bf16 v[32:35], a[212:215], a[140:143], v[32:35]\n"
    "v_exp_f32 v68, v68\n"
    "v_mfma_f32_16x16x32_bf16 v[32:35], a[208:211], a[136:139], v[32:35]\n"
    "v_exp_f32 v69, v69\n"
    "v_mfma_f32_16x16x32_bf16 v[32:35], a[204:207], a[132:135], v[32:35]\n"
    "v_exp_f32 v70, v70\n"
    "v_mfma_f32_16x16x32_bf16 v[36:39], a[204:207], a[156:159], v[36:39]\n"
    "v_exp_f32 v71, v71\n"
    "v_mfma_f32_16x16x32_bf16 v[36:39], a[208:211], a[160:163], v[36:39]\n"
    "v_cvt_pk_bf16_f32 v46, v68, v69\n"
    "v_cvt_pk_bf16_f32 v47, v70, v71\n"
    "v_mfma_f32_16x16x32_bf16 v[36:39], a[212:215], a[164:167], v[36:39]\n"
    "v_fma_f32 v48, v48, s52, v13\n"
    "v_fma_f32 v49, v49, s52, v13\n"
    "v_mfma_f32_16x16x32_bf16 v[40:43], a[212:215], a[188:191], v[40:43]\n"
    "ds_read_b128 a[212:215], v1 offset:43264\n"
    "v_fma_f32 v50, v50, s52, v13\n"
    "v_mfma_f32_16x16x32_bf16 v[40:43], a[208:211], a[184:187], v[40:43]\n"
    "ds_read_b128 a[208:211], v0 offset:43264\n"
    "v_fma_f32 v51, v51, s52, v13\n"
    "v_mfma_f32_16x16x32_bf16 v[40:43], a[204:207], a[180:183], v[40:43]\n"
    "s_waitcnt lgkmcnt(4)\n"
    "ds_read_b128 a[204:207], v1 offset:43136\n"
    "v_fma_f32 v72, v72, s52, v13\n"
    "v_mfma_f32_16x16x32_bf16 v[60:63], a[192:195], a[96:99], 0\n"
    "v_exp_f32 v48, v48\n"
    "v_mfma_f32_16x16x32_bf16 v[60:63], a[196:199], a[100:103], v[60:63]\n"
    "buffer_load_dwordx4 v7, s[36:39], s55 offen offset:1024 nt lds\n"
    "v_mfma_f32_16x16x32_bf16 v[60:63], a[200:203], a[104:107], v[60:63]\n"
    "v_exp_f32 v49, v49\n"
    "v_mfma_f32_16x16x32_bf16 v[64:67], a[200:203], a[128:131], 0\n"
    "v_exp_f32 v50, v50\n"
    "v_mfma_f32_16x16x32_bf16 v[64:67], a[196:199], a[124:127], v[64:67]\n"
    "s_waitcnt lgkmcnt(3)\n"
    "v_exp_f32 v51, v51\n"
    "v_mfma_f32_16x16x32_bf16 v[64:67], a[192:195], a[120:123], v[64:67]\n"
    "buffer_store_dwordx4 v[84:87], v9, s[48:51], s72 offen offset:0 sc1\n"
    "v_mfma_f32_16x16x32_bf16 v[68:71], a[192:195], a[144:147], 0\n"
    "v_fma_f32 v73, v73, s52, v13\n"
    "v_fma_f32 v74, v74, s52, v13\n"
    "v_mfma_f32_16x16x32_bf16 v[68:71], a[196:199], a[148:151], v[68:71]\n"
    "v_fma_f32 v75, v75, s52, v13\n"
    "v_cvt_pk_bf16_f32 v48, v48, v49\n"
    "v_mfma_f32_16x16x32_bf16 v[68:71], a[200:203], a[152:155], v[68:71]\n"
    "v_exp_f32 v72, v72\n"
    "v_exp_f32 v73, v73\n"
    "v_exp_f32 v74, v74\n"
    "v_exp_f32 v75, v75\n"
    "v_cvt_pk_bf16_f32 v49, v50, v51\n"
    "v_cvt_pk_bf16_f32 v50, v72, v73\n"
    "v_cvt_pk_bf16_f32 v51, v74, v75\n"
    "v_mfma_f32_16x16x32_bf16 v[72:75], a[200:203], a[176:179], 0\n"
    "v_fma_f32 v52, v52, s52, v14\n"
    "v_fma_f32 v53, v53, s52, v14\n"
    "v_mfma_f32_16x16x32_bf16 v[72:75], a[196:199], a[172:175], v[72:75]\n"
    "ds_read_b64_tr_b16 a[196:197], v3 offset:24576\n"
    "v_fma_f32 v56, v56, s52, v15\n"
    "v_mfma_f32_16x16x32_bf16 v[72:75], a[192:195], a[168:171], v[72:75]\n"
    "s_waitcnt lgkmcnt(1)\n"
    "ds_read_b64_tr_b16 a[192:193], v2 offset:24576\n"
    "v_fma_f32 v57, v57, s52, v15\n"
    "v_mfma_f32_16x16x32_bf16 v[60:63], a[204:207], a[108:111], v[60:63]\n"
    "ds_read_b64_tr_b16 a[194:195], v2 offset:30720\n"
    "v_fma_f32 v54, v54, s52, v14\n"
    "v_mfma_f32_16x16x32_bf16 v[60:63], a[208:211], a[112:115], v[60:63]\n"
    "ds_read_b64_tr_b16 a[198:199], v3 offset:30720\n"
    "v_fma_f32 v55, v55, s52, v14\n"
    "v_mfma_f32_16x16x32_bf16 v[60:63], a[212:215], a[116:119], v[60:63]\n"
    "v_exp_f32 v52, v52\n"
    "v_mfma_f32_16x16x32_bf16 v[64:67], a[212:215], a[140:143], v[64:67]\n"
    "buffer_load_dwordx4 v8, s[36:39], s55 offen offset:2048 nt lds\n"
    "v_mfma_f32_16x16x32_bf16 v[64:67], a[208:211], a[136:139], v[64:67]\n"
    "s_add_u32 m0, s54, 73728\n"
    "v_exp_f32 v53, v53\n"
    "v_mfma_f32_16x16x32_bf16 v[64:67], a[204:207], a[132:135], v[64:67]\n"
    "v_exp_f32 v56, v56\n"
    "v_mfma_f32_16x16x32_bf16 v[68:71], a[204:207], a[156:159], v[68:71]\n"
    "v_exp_f32 v57, v57\n"
    "v_mfma_f32_16x16x32_bf16 v[68:71], a[208:211], a[160:163], v[68:71]\n"
    "v_fma_f32 v58, v58, s52, v15\n"
    "v_fma_f32 v59, v59, s52, v15\n"
    "v_mfma_f32_16x16x32_bf16 v[68:71], a[212:215], a[164:167], v[68:71]\n"
    "v_exp_f32 v54, v54\n"
    "v_mfma_f32_16x16x32_bf16 v[72:75], a[212:215], a[188:191], v[72:75]\n"
    "v_exp_f32 v55, v55\n"
    "v_mfma_f32_16x16x32_bf16 v[72:75], a[208:211], a[184:187], v[72:75]\n"
    "v_exp_f32 v58, v58\n"
    "v_mfma_f32_16x16x32_bf16 v[72:75], a[204:207], a[180:183], v[72:75]\n"
    "v_exp_f32 v59, v59\n"
    "v_smfmac_f32_16x16x64_bf16 a[244:247], a[252:255], v[44:51], v23\n"
    "v_fma_f32 v76, v76, s52, v14\n"
    "v_fma_f32 v77, v77, s52, v14\n"
    "v_fma_f32 v78, v78, s52, v14\n"
    "v_fma_f32 v79, v79, s52, v14\n"
    "v_fma_f32 v80, v80, s52, v15\n"
    "v_fma_f32 v81, v81, s52, v15\n"
    "v_fma_f32 v82, v82, s52, v15\n"
    "v_fma_f32 v83, v83, s52, v15\n"
    "v_cvt_pk_bf16_f32 v52, v52, v53\n"
    "v_cvt_pk_bf16_f32 v56, v56, v57\n"
    "v_exp_f32 v76, v76\n"
    "v_exp_f32 v77, v77\n"
    "v_exp_f32 v78, v78\n"
    "v_exp_f32 v79, v79\n"
    "v_exp_f32 v80, v80\n"
    "v_exp_f32 v81, v81\n"
    "v_exp_f32 v82, v82\n"
    "v_exp_f32 v83, v83\n"
    "v_cvt_pk_bf16_f32 v53, v54, v55\n"
    "v_cvt_pk_bf16_f32 v57, v58, v59\n"
    "v_cvt_pk_bf16_f32 v54, v76, v77\n"
    "v_cvt_pk_bf16_f32 v55, v78, v79\n"
    "v_cvt_pk_bf16_f32 v58, v80, v81\n"
    "v_cvt_pk_bf16_f32 v59, v82, v83\n"
    "s_nop 1\n"
    "v_smfmac_f32_16x16x64_bf16 a[248:251], a[252:255], v[52:59], v23\n"
    "s_waitcnt lgkmcnt(1)\n"
    "ds_read_b64_tr_b16 a[200:201], v4 offset:24576\n"
    "v_fma_f32 v28, v28, s52, v12\n"
    "v_mfma_f32_16x16x32_bf16 a[12:15], a[192:195], v[56:59], a[12:15]\n"
    "ds_read_b64_tr_b16 a[202:203], v4 offset:30720\n"
    "v_fma_f32 v29, v29, s52, v12\n"
    "v_mfma_f32_16x16x32_bf16 a[8:11], a[192:195], v[52:55], a[8:11]\n"
    "ds_read_b64_tr_b16 a[204:205], v5 offset:24576\n"
    "v_fma_f32 v30, v30, s52, v12\n"
    "v_mfma_f32_16x16x32_bf16 a[4:7], a[192:195], v[48:51], a[4:7]\n"
    "ds_read_b64_tr_b16 a[206:207], v5 offset:30720\n"
    "v_fma_f32 v31, v31, s52, v12\n"
    "v_mfma_f32_16x16x32_bf16 a[0:3], a[192:195], v[44:47], a[0:3]\n"
    "s_waitcnt lgkmcnt(4)\n"
    "ds_read_b64_tr_b16 a[208:209], v2 offset:24704\n"
    "v_fma_f32 v60, v60, s52, v12\n"
    "v_mfma_f32_16x16x32_bf16 a[16:19], a[196:199], v[44:47], a[16:19]\n"
    "buffer_load_dwordx4 v6, s[40:43], s56 offen offset:0 nt lds\n"
    "v_mfma_f32_16x16x32_bf16 a[20:23], a[196:199], v[48:51], a[20:23]\n"
    "ds_read_b64_tr_b16 a[210:211], v2 offset:30848\n"
    "v_fma_f32 v61, v61, s52, v12\n"
    "v_mfma_f32_16x16x32_bf16 a[24:27], a[196:199], v[52:55], a[24:27]\n"
    "ds_read_b64_tr_b16 a[212:213], v3 offset:24704\n"
    "v_fma_f32 v62, v62, s52, v12\n"
    "v_mfma_f32_16x16x32_bf16 a[28:31], a[196:199], v[56:59], a[28:31]\n"
    "s_waitcnt lgkmcnt(5)\n"
    "ds_read_b64_tr_b16 a[214:215], v3 offset:30848\n"
    "v_fma_f32 v63, v63, s52, v12\n"
    "v_mfma_f32_16x16x32_bf16 a[44:47], a[200:203], v[56:59], a[44:47]\n"
    "ds_read_b64_tr_b16 a[192:193], v4 offset:24704\n"
    "v_fma_f32 v32, v32, s52, v13\n"
    "v_mfma_f32_16x16x32_bf16 a[40:43], a[200:203], v[52:55], a[40:43]\n"
    "ds_read_b64_tr_b16 a[194:195], v4 offset:30848\n"
    "v_fma_f32 v33, v33, s52, v13\n"
    "v_mfma_f32_16x16x32_bf16 a[36:39], a[200:203], v[48:51], a[36:39]\n"
    "ds_read_b64_tr_b16 a[196:197], v5 offset:24704\n"
    "v_fma_f32 v34, v34, s52, v13\n"
    "v_mfma_f32_16x16x32_bf16 a[32:35], a[200:203], v[44:47], a[32:35]\n"
    "s_waitcnt lgkmcnt(7)\n"
    "ds_read_b64_tr_b16 a[198:199], v5 offset:30848\n"
    "v_fma_f32 v35, v35, s52, v13\n"
    "v_mfma_f32_16x16x32_bf16 a[48:51], a[204:207], v[44:47], a[48:51]\n"
    "ds_read_b64_tr_b16 a[200:201], v2 offset:24832\n"
    "v_fma_f32 v64, v64, s52, v13\n"
    "v_mfma_f32_16x16x32_bf16 a[52:55], a[204:207], v[48:51], a[52:55]\n"
    "ds_read_b64 v[100:101], v11 offset:320\n"
    "v_fma_f32 v65, v65, s52, v13\n"
    "v_mfma_f32_16x16x32_bf16 a[56:59], a[204:207], v[52:55], a[56:59]\n"
    "ds_read_b64 v[102:103], v20 offset:320\n"
    "v_fma_f32 v66, v66, s52, v13\n"
    "v_mfma_f32_16x16x32_bf16 a[60:63], a[204:207], v[56:59], a[60:63]\n"
    "s_waitcnt lgkmcnt(9)\n"
    "ds_read_b64_tr_b16 a[202:203], v2 offset:30976\n"
    "v_fma_f32 v67, v67, s52, v13\n"
    "v_mfma_f32_16x16x32_bf16 a[76:79], a[208:211], v[56:59], a[76:79]\n"
    "ds_read_b64_tr_b16 a[204:205], v3 offset:24832\n"
    "v_fma_f32 v36, v36, s52, v14\n"
    "v_mfma_f32_16x16x32_bf16 a[72:75], a[208:211], v[52:55], a[72:75]\n"
    "ds_read_b64_tr_b16 a[206:207], v3 offset:30976\n"
    "v_fma_f32 v37, v37, s52, v14\n"
    "v_mfma_f32_16x16x32_bf16 a[68:71], a[208:211], v[48:51], a[68:71]\n"
    "buffer_load_dwordx4 v7, s[40:43], s56 offen offset:1024 nt lds\n"
    "v_mfma_f32_16x16x32_bf16 a[64:67], a[208:211], v[44:47], a[64:67]\n"
    "s_waitcnt lgkmcnt(10)\n"
    "ds_read_b64_tr_b16 a[208:209], v4 offset:24832\n"
    "v_fma_f32 v40, v40, s52, v15\n"
    "v_mfma_f32_16x16x32_bf16 a[80:83], a[212:215], v[44:47], a[80:83]\n"
    "ds_read_b64_tr_b16 a[210:211], v4 offset:30976\n"
    "v_fma_f32 v41, v41, s52, v15\n"
    "v_mfma_f32_16x16x32_bf16 a[84:87], a[212:215], v[48:51], a[84:87]\n"
    "v_exp_f32 v28, v28\n"
    "v_mfma_f32_16x16x32_bf16 a[88:91], a[212:215], v[52:55], a[88:91]\n"
    "v_exp_f32 v29, v29\n"
    "v_mfma_f32_16x16x32_bf16 a[92:95], a[212:215], v[56:59], a[92:95]\n"
    "s_waitcnt lgkmcnt(10)\n"
    "ds_read_b64_tr_b16 a[212:213], v5 offset:24832\n"
    "v_cvt_pk_bf16_f32 v28, v28, v29\n"
    "v_mfma_f32_16x16x32_bf16 v[128:131], a[192:195], v[56:59], v[128:131]\n"
    "ds_read_b64_tr_b16 a[214:215], v5 offset:30976\n"
    "v_fma_f32 v38, v38, s52, v14\n"
    "v_mfma_f32_16x16x32_bf16 v[124:127], a[192:195], v[52:55], v[124:127]\n"
    "v_exp_f32 v30, v30\n"
    "v_mfma_f32_16x16x32_bf16 v[120:123], a[192:195], v[48:51], v[120:123]\n"
    "v_exp_f32 v31, v31\n"
    "v_mfma_f32_16x16x32_bf16 v[116:119], a[192:195], v[44:47], v[116:119]\n"
    "s_waitcnt lgkmcnt(10)\n"
    "v_exp_f32 v60, v60\n"
    "v_mfma_f32_16x16x32_bf16 a[216:219], a[196:199], v[44:47], a[216:219]\n"
    "s_waitcnt lgkmcnt(7)\n"
    "v_exp_f32 v61, v61\n"
    "v_mfma_f32_16x16x32_bf16 a[220:223], a[196:199], v[48:51], a[220:223]\n"
    "buffer_store_dwordx4 v[100:103], v9, s[48:51], s72 offen offset:768 sc1\n"
    "v_mfma_f32_16x16x32_bf16 a[224:227], a[196:199], v[52:55], a[224:227]\n"
    "s_waitcnt vmcnt(19) lgkmcnt(0)\n"
    "v_exp_f32 v62, v62\n"
    "v_mfma_f32_16x16x32_bf16 a[228:231], a[196:199], v[56:59], a[228:231]\n"
    "s_barrier\n"
    "ds_read_b128 a[192:195], v0 offset:49152\n"
    "v_cvt_pk_bf16_f32 v29, v30, v31\n"
    "v_mfma_f32_16x16x32_bf16 v[188:191], a[200:203], v[56:59], v[188:191]\n"
    "buffer_load_dwordx4 v8, s[40:43], s56 offen offset:2048 nt lds\n"
    "v_mfma_f32_16x16x32_bf16 v[184:187], a[200:203], v[52:55], v[184:187]\n"
    "s_add_u32 s56, s56, 24576\n"
    "ds_read_b128 a[196:199], v1 offset:49152\n"
    "v_cvt_pk_bf16_f32 v30, v60, v61\n"
    "v_mfma_f32_16x16x32_bf16 v[180:183], a[200:203], v[48:51], v[180:183]\n"
    "s_add_u32 m0, s54, 36864\n"
    "v_exp_f32 v63, v63\n"
    "v_mfma_f32_16x16x32_bf16 v[176:179], a[200:203], v[44:47], v[176:179]\n"
    "ds_read_b128 a[200:203], v0 offset:49280\n"
    "v_cvt_pk_bf16_f32 v31, v62, v63\n"
    "v_mfma_f32_16x16x32_bf16 a[232:235], a[204:207], v[44:47], a[232:235]\n"
    "v_exp_f32 v32, v32\n"
    "v_mfma_f32_16x16x32_bf16 v[148:151], a[204:207], v[48:51], v[148:151]\n"
    "v_exp_f32 v33, v33\n"
    "v_mfma_f32_16x16x32_bf16 v[152:155], a[204:207], v[52:55], v[152:155]\n"
    "v_exp_f32 v34, v34\n"
    "v_mfma_f32_16x16x32_bf16 v[156:159], a[204:207], v[56:59], v[156:159]\n"
    "ds_write_b64 v10, v[88:89] offset:2560\n"
    "v_mfma_f32_16x16x32_bf16 v[220:223], a[208:211], v[56:59], v[220:223]\n"
    "ds_read_b128 a[204:207], v1 offset:49280\n"
    "v_cvt_pk_bf16_f32 v32, v32, v33\n"
    "v_mfma_f32_16x16x32_bf16 v[216:219], a[208:211], v[52:55], v[216:219]\n"
    "v_exp_f32 v35, v35\n"
    "v_mfma_f32_16x16x32_bf16 v[212:215], a[208:211], v[48:51], v[212:215]\n"
    "v_exp_f32 v64, v64\n"
    "v_mfma_f32_16x16x32_bf16 v[208:211], a[208:211], v[44:47], v[208:211]\n"
    "ds_read_b128 a[208:211], v0 offset:49408\n"
    "v_cvt_pk_bf16_f32 v33, v34, v35\n"
    "v_mfma_f32_16x16x32_bf16 v[240:243], a[212:215], v[44:47], v[240:243]\n"
    "ds_write_b64 v10, v[90:91] offset:2592\n"
    "v_mfma_f32_16x16x32_bf16 v[244:247], a[212:215], v[48:51], v[244:247]\n"
    "v_exp_f32 v65, v65\n"
    "v_mfma_f32_16x16x32_bf16 v[248:251], a[212:215], v[52:55], v[248:251]\n"
    "v_exp_f32 v66, v66\n"
    "v_mfma_f32_16x16x32_bf16 v[252:255], a[212:215], v[56:59], v[252:255]\n"
    "s_waitcnt lgkmcnt(6)\n"
    "buffer_load_dwordx4 v6, s[36:39], s56 offen offset:0 nt lds\n"
    "v_mfma_f32_16x16x32_bf16 v[44:47], a[192:195], a[96:99], 0\n"
    "s_waitcnt lgkmcnt(5)\n"
    "ds_read_b128 a[212:215], v1 offset:49408\n"
    "v_cvt_pk_bf16_f32 v34, v64, v65\n"
    "v_mfma_f32_16x16x32_bf16 v[44:47], a[196:199], a[100:103], v[44:47]\n"
    "s_waitcnt lgkmcnt(5)\n"
    "ds_write_b64 v10, v[104:105] offset:2624\n"
    "v_mfma_f32_16x16x32_bf16 v[44:47], a[200:203], a[104:107], v[44:47]\n"
    "v_exp_f32 v67, v67\n"
    "v_mfma_f32_16x16x32_bf16 v[48:51], a[200:203], a[128:131], 0\n"
    "v_cvt_pk_bf16_f32 v35, v66, v67\n"
    "v_fma_f32 v39, v39, s52, v14\n"
    "v_mfma_f32_16x16x32_bf16 v[48:51], a[196:199], a[124:127], v[48:51]\n"
    "v_exp_f32 v36, v36\n"
    "v_mfma_f32_16x16x32_bf16 v[48:51], a[192:195], a[120:123], v[48:51]\n"
    "v_exp_f32 v37, v37\n"
    "v_mfma_f32_16x16x32_bf16 v[52:55], a[192:195], a[144:147], 0\n"
    "ds_write_b64 v10, v[106:107] offset:2656\n"
    "v_mfma_f32_16x16x32_bf16 v[52:55], a[196:199], a[148:151], v[52:55]\n"
    "v_exp_f32 v40, v40\n"
    "v_mfma_f32_16x16x32_bf16 v[52:55], a[200:203], a[152:155], v[52:55]\n"
    "v_exp_f32 v41, v41\n"
    "v_mfma_f32_16x16x32_bf16 v[56:59], a[200:203], a[176:179], 0\n"
    "ds_read_b128 a[200:203], v0 offset:55424\n"
    "v_fma_f32 v42, v42, s52, v15\n"
    "v_mfma_f32_16x16x32_bf16 v[56:59], a[196:199], a[172:175], v[56:59]\n"
    "ds_read_b128 a[196:199], v1 offset:55296\n"
    "v_fma_f32 v43, v43, s52, v15\n"
    "v_mfma_f32_16x16x32_bf16 v[56:59], a[192:195], a[168:171], v[56:59]\n"
    "s_waitcnt lgkmcnt(7)\n"
    "ds_read_b128 a[192:195], v0 offset:55296\n"
    "v_fma_f32 v68, v68, s52, v14\n"
    "v_mfma_f32_16x16x32_bf16 v[44:47], a[204:207], a[108:111], v[44:47]\n"
    "s_waitcnt lgkmcnt(7)\n"
    "v_exp_f32 v38, v38\n"
    "v_mfma_f32_16x16x32_bf16 v[44:47], a[208:211], a[112:115], v[44:47]\n"
    "s_waitcnt lgkmcnt(5)\n"
    "buffer_load_dwordx4 v7, s[36:39], s56 offen offset:1024 nt lds\n"
    "v_mfma_f32_16x16x32_bf16 v[44:47], a[212:215], a[116:119], v[44:47]\n"
    "v_exp_f32 v39, v39\n"
    "v_mfma_f32_16x16x32_bf16 v[48:51], a[212:215], a[140:143], v[48:51]\n"
    "ds_read_b64 v[88:89], v11 offset:2560\n"
    "v_fma_f32 v69, v69, s52, v14\n"
    "v_mfma_f32_16x16x32_bf16 v[48:51], a[208:211], a[136:139], v[48:51]\n"
    "ds_read_b64 v[90:91], v20 offset:2560\n"
    "v_fma_f32 v70, v70, s52, v14\n"
    "v_mfma_f32_16x16x32_bf16 v[48:51], a[204:207], a[132:135], v[48:51]\n"
    "v_exp_f32 v42, v42\n"
    "v_mfma_f32_16x16x32_bf16 v[52:55], a[204:207], a[156:159], v[52:55]\n"
    "v_exp_f32 v43, v43\n"
    "v_mfma_f32_16x16x32_bf16 v[52:55], a[208:211], a[160:163], v[52:55]\n"
    "v_fma_f32 v71, v71, s52, v14\n"
    "v_fma_f32 v72, v72, s52, v15\n"
    "v_mfma_f32_16x16x32_bf16 v[52:55], a[212:215], a[164:167], v[52:55]\n"
    "v_fma_f32 v73, v73, s52, v15\n"
    "v_fma_f32 v74, v74, s52, v15\n"
    "v_mfma_f32_16x16x32_bf16 v[56:59], a[212:215], a[188:191], v[56:59]\n"
    "ds_read_b128 a[212:215], v1 offset:55552\n"
    "v_fma_f32 v75, v75, s52, v15\n"
    "v_mfma_f32_16x16x32_bf16 v[56:59], a[208:211], a[184:187], v[56:59]\n"
    "ds_read_b128 a[208:211], v0 offset:55552\n"
    "v_cvt_pk_bf16_f32 v36, v36, v37\n"
    "v_mfma_f32_16x16x32_bf16 v[56:59], a[204:207], a[180:183], v[56:59]\n"
    "s_waitcnt lgkmcnt(4)\n"
    "ds_read_b128 a[204:207], v1 offset:55424\n"
    "v_cvt_pk_bf16_f32 v40, v40, v41\n"
    "v_mfma_f32_16x16x32_bf16 v[76:79], a[192:195], a[96:99], 0\n"
    "v_exp_f32 v68, v68\n"
    "v_mfma_f32_16x16x32_bf16 v[76:79], a[196:199], a[100:103], v[76:79]\n"
    "v_exp_f32 v69, v69\n"
    "v_mfma_f32_16x16x32_bf16 v[76:79], a[200:203], a[104:107], v[76:79]\n"
    "v_exp_f32 v70, v70\n"
    "v_mfma_f32_16x16x32_bf16 v[80:83], a[200:203], a[128:131], 0\n"
    "buffer_load_dwordx4 v8, s[36:39], s56 offen offset:2048 nt lds\n"
    "v_mfma_f32_16x16x32_bf16 v[80:83], a[196:199], a[124:127], v[80:83]\n"
    "s_add_u32 m0, s54, 86016\n"
    "v_exp_f32 v71, v71\n"
    "v_mfma_f32_16x16x32_bf16 v[80:83], a[192:195], a[120:123], v[80:83]\n"
    "v_exp_f32 v72, v72\n"
    "v_mfma_f32_16x16x32_bf16 v[60:63], a[192:195], a[144:147], 0\n"
    "s_waitcnt lgkmcnt(3)\n"
    "v_exp_f32 v73, v73\n"
    "v_mfma_f32_16x16x32_bf16 v[60:63], a[196:199], a[148:151], v[60:63]\n"
    "buffer_store_dwordx4 v[88:91], v9, s[48:51], s73 offen offset:0 sc1\n"
    "v_mfma_f32_16x16x32_bf16 v[60:63], a[200:203], a[152:155], v[60:63]\n"
    "v_exp_f32 v74, v74\n"
    "v_mfma_f32_16x16x32_bf16 v[64:67], a[200:203], a[176:179], 0\n"
    "v_exp_f32 v75, v75\n"
    "v_mfma_f32_16x16x32_bf16 v[64:67], a[196:199], a[172:175], v[64:67]\n"
    "ds_read_b64_tr_b16 a[196:197], v3 offset:36864\n"
    "v_cvt_pk_bf16_f32 v37, v38, v39\n"
    "v_mfma_f32_16x16x32_bf16 v[64:67], a[192:195], a[168:171], v[64:67]\n"
    "s_waitcnt lgkmcnt(1)\n"
    "ds_read_b64_tr_b16 a[192:193], v2 offset:36864\n"
    "v_cvt_pk_bf16_f32 v41, v42, v43\n"
    "v_mfma_f32_16x16x32_bf16 v[76:79], a[204:207], a[108:111], v[76:79]\n"
    "ds_read_b64_tr_b16 a[194:195], v2 offset:43008\n"
    "v_cvt_pk_bf16_f32 v38, v68, v69\n"
    "v_mfma_f32_16x16x32_bf16 v[76:79], a[208:211], a[112:115], v[76:79]\n"
    "ds_read_b64_tr_b16 a[198:199], v3 offset:43008\n"
    "v_cvt_pk_bf16_f32 v39, v70, v71\n"
    "v_mfma_f32_16x16x32_bf16 v[76:79], a[212:215], a[116:119], v[76:79]\n"
    "ds_read_b64_tr_b16 a[200:201], v4 offset:36864\n"
    "v_cvt_pk_bf16_f32 v42, v72, v73\n"
    "v_mfma_f32_16x16x32_bf16 v[80:83], a[212:215], a[140:143], v[80:83]\n"
    "ds_read_b64_tr_b16 a[202:203], v4 offset:43008\n"
    "v_cvt_pk_bf16_f32 v43, v74, v75\n"
    "v_mfma_f32_16x16x32_bf16 v[80:83], a[208:211], a[136:139], v[80:83]\n"
    "v_accvgpr_read_b32 v100, a4\n"
    "v_accvgpr_read_b32 v101, a5\n"
    "v_mfma_f32_16x16x32_bf16 v[80:83], a[204:207], a[132:135], v[80:83]\n"
    "v_accvgpr_read_b32 v102, a6\n"
    "v_accvgpr_read_b32 v103, a7\n"
    "v_mfma_f32_16x16x32_bf16 v[60:63], a[204:207], a[156:159], v[60:63]\n"
    "buffer_load_dwordx4 v6, s[40:43], s55 offen offset:0 nt lds\n"
    "v_mfma_f32_16x16x32_bf16 v[60:63], a[208:211], a[160:163], v[60:63]\n"
    "v_accvgpr_read_b32 v84, a0\n"
    "v_accvgpr_read_b32 v85, a1\n"
    "v_mfma_f32_16x16x32_bf16 v[60:63], a[212:215], a[164:167], v[60:63]\n"
    "v_accvgpr_read_b32 v86, a2\n"
    "v_accvgpr_read_b32 v87, a3\n"
    "v_mfma_f32_16x16x32_bf16 v[64:67], a[212:215], a[188:191], v[64:67]\n"
    "ds_read_b64_tr_b16 a[212:213], v3 offset:36992\n"
    "v_fma_f32 v44, v44, s52, v12\n"
    "v_mfma_f32_16x16x32_bf16 v[64:67], a[208:211], a[184:187], v[64:67]\n"
    "ds_read_b64_tr_b16 a[208:209], v2 offset:36992\n"
    "v_fma_f32 v45, v45, s52, v12\n"
    "v_mfma_f32_16x16x32_bf16 v[64:67], a[204:207], a[180:183], v[64:67]\n"
    "ds_read_b64_tr_b16 a[204:205], v5 offset:36864\n"
    "v_fma_f32 v46, v46, s52, v12\n"
    "v_smfmac_f32_16x16x64_bf16 a[244:247], a[252:255], v[28:35], v23\n"
    "ds_read_b64_tr_b16 a[206:207], v5 offset:43008\n"
    "v_fma_f32 v47, v47, s52, v12\n"
    "v_smfmac_f32_16x16x64_bf16 a[248:251], a[252:255], v[36:43], v23\n"
    "s_waitcnt lgkmcnt(7)\n"
    "ds_read_b64_tr_b16 a[210:211], v2 offset:43136\n"
    "v_fma_f32 v76, v76, s52, v12\n"
    "v_mfma_f32_16x16x32_bf16 a[12:15], a[192:195], v[40:43], a[12:15]\n"
    "ds_read_b64_tr_b16 a[214:215], v3 offset:43136\n"
    "v_fma_f32 v77, v77, s52, v12\n"
    "v_mfma_f32_16x16x32_bf16 a[8:11], a[192:195], v[36:39], a[8:11]\n"
    "v_exp_f32 v44, v44\n"
    "v_mfma_f32_16x16x32_bf16 v[100:103], a[192:195], v[32:35], v[100:103]\n"
    "v_exp_f32 v45, v45\n"
    "v_mfma_f32_16x16x32_bf16 v[84:87], a[192:195], v[28:31], v[84:87]\n"
    "s_waitcnt lgkmcnt(8)\n"
    "ds_read_b64_tr_b16 a[192:193], v4 offset:36992\n"
    "v_fma_f32 v78, v78, s52, v12\n"
    "v_mfma_f32_16x16x32_bf16 a[16:19], a[196:199], v[28:31], a[16:19]\n"
    "ds_read_b64_tr_b16 a[194:195], v4 offset:43136\n"
    "v_fma_f32 v79, v79, s52, v12\n"
    "v_mfma_f32_16x16x32_bf16 a[20:23], a[196:199], v[32:35], a[20:23]\n"
    "v_exp_f32 v46, v46\n"
    "v_mfma_f32_16x16x32_bf16 a[24:27], a[196:199], v[36:39], a[24:27]\n"
    "buffer_load_dwordx4 v7, s[40:43], s55 offen offset:1024 nt lds\n"
    "v_mfma_f32_16x16x32_bf16 a[28:31], a[196:199], v[40:43], a[28:31]\n"
    "s_waitcnt lgkmcnt(8)\n"
    "ds_read_b64_tr_b16 a[196:197], v5 offset:36992\n"
    "v_cvt_pk_bf16_f32 v44, v44, v45\n"
    "v_mfma_f32_16x16x32_bf16 a[44:47], a[200:203], v[40:43], a[44:47]\n"
    "ds_read_b64_tr_b16 a[198:199], v5 offset:43136\n"
    "v_fma_f32 v48, v48, s52, v13\n"
    "v_mfma_f32_16x16x32_bf16 a[40:43], a[200:203], v[36:39], a[40:43]\n"
    "v_exp_f32 v47, v47\n"
    "v_mfma_f32_16x16x32_bf16 a[36:39], a[200:203], v[32:35], a[36:39]\n"
    "v_exp_f32 v76, v76\n"
    "v_mfma_f32_16x16x32_bf16 a[32:35], a[200:203], v[28:31], a[32:35]\n"
    "s_waitcnt lgkmcnt(6)\n"
    "ds_read_b64_tr_b16 a[200:201], v2 offset:37120\n"
    "v_cvt_pk_bf16_f32 v45, v46, v47\n"
    "v_mfma_f32_16x16x32_bf16 a[48:51], a[204:207], v[28:31], a[48:51]\n"
    "ds_read_b64_tr_b16 a[202:203], v2 offset:43264\n"
    "v_fma_f32 v49, v49, s52, v13\n"
    "v_mfma_f32_16x16x32_bf16 a[52:55], a[204:207], v[32:35], a[52:55]\n"
    "v_exp_f32 v77, v77\n"
    "v_mfma_f32_16x16x32_bf16 a[56:59], a[204:207], v[36:39], a[56:59]\n"
    "v_exp_f32 v78, v78\n"
    "v_mfma_f32_16x16x32_bf16 a[60:63], a[204:207], v[40:43], a[60:63]\n"
    "s_waitcnt lgkmcnt(7)\n"
    "ds_read_b64 v[104:105], v11 offset:2880\n"
    "v_cvt_pk_bf16_f32 v46, v76, v77\n"
    "v_mfma_f32_16x16x32_bf16 a[76:79], a[208:211], v[40:43], a[76:79]\n"
    "ds_read_b64 v[106:107], v20 offset:2880\n"
    "v_fma_f32 v50, v50, s52, v13\n"
    "v_mfma_f32_16x16x32_bf16 a[72:75], a[208:211], v[36:39], a[72:75]\n"
    "ds_read_b64_tr_b16 a[204:205], v3 offset:37120\n"
    "v_fma_f32 v51, v51, s52, v13\n"
    "v_mfma_f32_16x16x32_bf16 a[68:71], a[208:211], v[32:35], a[68:71]\n"
    "ds_read_b64_tr_b16 a[206:207], v3 offset:43264\n"
    "v_fma_f32 v80, v80, s52, v13\n"
    "v_mfma_f32_16x16x32_bf16 a[64:67], a[208:211], v[28:31], a[64:67]\n"
    "s_waitcnt lgkmcnt(10)\n"
    "ds_read_b64_tr_b16 a[208:209], v4 offset:37120\n"
    "v_fma_f32 v81, v81, s52, v13\n"
    "v_mfma_f32_16x16x32_bf16 a[80:83], a[212:215], v[28:31], a[80:83]\n"
    "buffer_load_dwordx4 v8, s[40:43], s55 offen offset:2048 nt lds\n"
    "v_mfma_f32_16x16x32_bf16 a[84:87], a[212:215], v[32:35], a[84:87]\n"
    "s_add_u32 s55, s55, 24576\n"
    "ds_read_b64_tr_b16 a[210:211], v4 offset:43264\n"
    "v_fma_f32 v82, v82, s52, v13\n"
    "v_mfma_f32_16x16x32_bf16 a[88:91], a[212:215], v[36:39], a[88:91]\n"
    "v_exp_f32 v79, v79\n"
    "v_mfma_f32_16x16x32_bf16 a[92:95], a[212:215], v[40:43], a[92:95]\n"
    "s_waitcnt lgkmcnt(10)\n"
    "ds_read_b64_tr_b16 a[212:213], v5 offset:37120\n"
    "v_cvt_pk_bf16_f32 v47, v78, v79\n"
    "v_mfma_f32_16x16x32_bf16 v[128:131], a[192:195], v[40:43], v[128:131]\n"
    "ds_read_b64_tr_b16 a[214:215], v5 offset:43264\n"
    "v_fma_f32 v83, v83, s52, v13\n"
    "v_mfma_f32_16x16x32_bf16 v[124:127], a[192:195], v[36:39], v[124:127]\n"
    "v_exp_f32 v48, v48\n"
    "v_mfma_f32_16x16x32_bf16 v[120:123], a[192:195], v[32:35], v[120:123]\n"
    "v_exp_f32 v49, v49\n"
    "v_mfma_f32_16x16x32_bf16 v[116:119], a[192:195], v[28:31], v[116:119]\n"
    "s_waitcnt lgkmcnt(10)\n"
    "v_exp_f32 v50, v50\n"
    "v_mfma_f32_16x16x32_bf16 a[216:219], a[196:199], v[28:31], a[216:219]\n"
    "v_exp_f32 v51, v51\n"
    "v_mfma_f32_16x16x32_bf16 a[220:223], a[196:199], v[32:35], a[220:223]\n"
    "v_cvt_pk_bf16_f32 v48, v48, v49\n"
    "v_fma_f32 v52, v52, s52, v14\n"
    "v_mfma_f32_16x16x32_bf16 a[224:227], a[196:199], v[36:39], a[224:227]\n"
    "s_waitcnt vmcnt(21) lgkmcnt(0)\n"
    "v_exp_f32 v80, v80\n"
    "v_mfma_f32_16x16x32_bf16 a[228:231], a[196:199], v[40:43], a[228:231]\n"
    "s_barrier\n"
    "buffer_store_dwordx4 v[104:107], v9, s[48:51], s73 offen offset:768 sc1\n"
    "v_mfma_f32_16x16x32_bf16 v[188:191], a[200:203], v[40:43], v[188:191]\n"
    "s_add_u32 m0, s54, 49152\n"
    "ds_read_b128 a[192:195], v0 offset:0\n"
    "v_cvt_pk_bf16_f32 v49, v50, v51\n"
    "v_mfma_f32_16x16x32_bf16 v[184:187], a[200:203], v[36:39], v[184:187]\n"
    "ds_read_b128 a[196:199], v1 offset:0\n"
    "v_fma_f32 v53, v53, s52, v14\n"
    "v_mfma_f32_16x16x32_bf16 v[180:183], a[200:203], v[32:35], v[180:183]\n"
    "v_exp_f32 v81, v81\n"
    "v_mfma_f32_16x16x32_bf16 v[176:179], a[200:203], v[28:31], v[176:179]\n"
    "ds_read_b128 a[200:203], v0 offset:128\n"
    "v_cvt_pk_bf16_f32 v50, v80, v81\n"
    "v_mfma_f32_16x16x32_bf16 a[232:235], a[204:207], v[28:31], a[232:235]\n"
    "v_exp_f32 v82, v82\n"
    "v_mfma_f32_16x16x32_bf16 v[148:151], a[204:207], v[32:35], v[148:151]\n"
    "v_exp_f32 v83, v83\n"
    "v_mfma_f32_16x16x32_bf16 v[152:155], a[204:207], v[36:39], v[152:155]\n"
    "v_cvt_pk_bf16_f32 v51, v82, v83\n"
    "v_fma_f32 v56, v56, s52, v15\n"
    "v_mfma_f32_16x16x32_bf16 v[156:159], a[204:207], v[40:43], v[156:159]\n"
    "ds_read_b128 a[204:207], v1 offset:128\n"
    "v_fma_f32 v57, v57, s52, v15\n"
    "v_mfma_f32_16x16x32_bf16 v[220:223], a[208:211], v[40:43], v[220:223]\n"
    "buffer_load_dwordx4 v6, s[36:39], s55 offen offset:0 nt lds\n"
    "v_mfma_f32_16x16x32_bf16 v[216:219], a[208:211], v[36:39], v[216:219]\n"
    "ds_write_b64 v10, v[92:93] offset:5120\n"
    "v_mfma_f32_16x16x32_bf16 v[212:215], a[208:211], v[32:35], v[212:215]\n"
    "v_exp_f32 v52, v52\n"
    "v_mfma_f32_16x16x32_bf16 v[208:211], a[208:211], v[28:31], v[208:211]\n"
    "ds_read_b128 a[208:211], v0 offset:256\n"
    "v_fma_f32 v54, v54, s52, v14\n"
    "v_mfma_f32_16x16x32_bf16 v[240:243], a[212:215], v[28:31], v[240:243]\n"
    "v_exp_f32 v53, v53\n"
    "v_mfma_f32_16x16x32_bf16 v[244:247], a[212:215], v[32:35], v[244:247]\n"
    "v_fma_f32 v55, v55, s52, v14\n"
    "v_fma_f32 v58, v58, s52, v15\n"
    "v_mfma_f32_16x16x32_bf16 v[248:251], a[212:215], v[36:39], v[248:251]\n"
    "ds_write_b64 v10, v[94:95] offset:5152\n"
    "v_mfma_f32_16x16x32_bf16 v[252:255], a[212:215], v[40:43], v[252:255]\n"
    "s_waitcnt lgkmcnt(6)\n"
    "ds_read_b128 a[212:215], v1 offset:256\n"
    "v_fma_f32 v59, v59, s52, v15\n"
    "v_mfma_f32_16x16x32_bf16 v[28:31], a[192:195], a[96:99], 0\n"
    "s_waitcnt lgkmcnt(6)\n"
    "v_exp_f32 v56, v56\n"
    "v_mfma_f32_16x16x32_bf16 v[28:31], a[196:199], a[100:103], v[28:31]\n"
    "s_waitcnt lgkmcnt(5)\n"
    "v_exp_f32 v57, v57\n"
    "v_mfma_f32_16x16x32_bf16 v[28:31], a[200:203], a[104:107], v[28:31]\n"
    "v_exp_f32 v54, v54\n"
    "v_mfma_f32_16x16x32_bf16 v[32:35], a[200:203], a[128:131], 0\n"
    "ds_write_b64 v10, v[108:109] offset:5184\n"
    "v_mfma_f32_16x16x32_bf16 v[32:35], a[196:199], a[124:127], v[32:35]\n"
    "buffer_load_dwordx4 v7, s[36:39], s55 offen offset:1024 nt lds\n"
    "v_mfma_f32_16x16x32_bf16 v[32:35], a[192:195], a[120:123], v[32:35]\n"
    "v_exp_f32 v55, v55\n"
    "v_mfma_f32_16x16x32_bf16 v[36:39], a[192:195], a[144:147], 0\n"
    "v_exp_f32 v58, v58\n"
    "v_mfma_f32_16x16x32_bf16 v[36:39], a[196:199], a[148:151], v[36:39]\n"
    "v_exp_f32 v59, v59\n"
    "v_mfma_f32_16x16x32_bf16 v[36:39], a[200:203], a[152:155], v[36:39]\n"
    "ds_write_b64 v10, v[110:111] offset:5216\n"
    "v_mfma_f32_16x16x32_bf16 v[40:43], a[200:203], a[176:179], 0\n"
    "ds_read_b128 a[200:203], v0 offset:6272\n"
    "v_fma_f32 v60, v60, s52, v14\n"
    "v_mfma_f32_16x16x32_bf16 v[40:43], a[196:199], a[172:175], v[40:43]\n"
    "ds_read_b128 a[196:199], v1 offset:6144\n"
    "v_fma_f32 v61, v61, s52, v14\n"
    "v_mfma_f32_16x16x32_bf16 v[40:43], a[192:195], a[168:171], v[40:43]\n"
    "s_waitcnt lgkmcnt(8)\n"
    "ds_read_b128 a[192:195], v0 offset:6144\n"
    "v_fma_f32 v62, v62, s52, v14\n"
    "v_mfma_f32_16x16x32_bf16 v[28:31], a[204:207], a[108:111], v[28:31]\n"
    "s_waitcnt lgkmcnt(7)\n"
    "v_fma_f32 v63, v63, s52, v14\n"
    "v_fma_f32 v64, v64, s52, v15\n"
    "v_mfma_f32_16x16x32_bf16 v[28:31], a[208:211], a[112:115], v[28:31]\n"
    "s_waitcnt lgkmcnt(5)\n"
    "v_fma_f32 v65, v65, s52, v15\n"
    "v_fma_f32 v66, v66, s52, v15\n"
    "v_mfma_f32_16x16x32_bf16 v[28:31], a[212:215], a[116:119], v[28:31]\n"
    "v_fma_f32 v67, v67, s52, v15\n"
    "v_cvt_pk_bf16_f32 v52, v52, v53\n"
    "v_mfma_f32_16x16x32_bf16 v[32:35], a[212:215], a[140:143], v[32:35]\n"
    "v_cvt_pk_bf16_f32 v56, v56, v57\n"
    "v_cvt_pk_bf16_f32 v53, v54, v55\n"
    "v_mfma_f32_16x16x32_bf16 v[32:35], a[208:211], a[136:139], v[32:35]\n"
    "buffer_load_dwordx4 v8, s[36:39], s55 offen offset:2048 nt lds\n"
    "v_mfma_f32_16x16x32_bf16 v[32:35], a[204:207], a[132:135], v[32:35]\n"
    "s_add_u32 m0, s54, 98304\n"
    "ds_read_b64 v[92:93], v11 offset:5120\n"
    "v_cvt_pk_bf16_f32 v57, v58, v59\n"
    "v_mfma_f32_16x16x32_bf16 v[36:39], a[204:207], a[156:159], v[36:39]\n"
    "ds_read_b64 v[94:95], v20 offset:5120\n"
    "v_accvgpr_read_b32 v104, a12\n"
    "v_mfma_f32_16x16x32_bf16 v[36:39], a[208:211], a[160:163], v[36:39]\n"
    "v_exp_f32 v60, v60\n"
    "v_mfma_f32_16x16x32_bf16 v[36:39], a[212:215], a[164:167], v[36:39]\n"
    "v_exp_f32 v61, v61\n"
    "v_mfma_f32_16x16x32_bf16 v[40:43], a[212:215], a[188:191], v[40:43]\n"
    "ds_read_b128 a[212:215], v1 offset:6400\n"
    "v_cvt_pk_bf16_f32 v54, v60, v61\n"
    "v_mfma_f32_16x16x32_bf16 v[40:43], a[208:211], a[184:187], v[40:43]\n"
    "ds_read_b128 a[208:211], v0 offset:6400\n"
    "v_accvgpr_read_b32 v105, a13\n"
    "v_mfma_f32_16x16x32_bf16 v[40:43], a[204:207], a[180:183], v[40:43]\n"
    "s_waitcnt lgkmcnt(4)\n"
    "ds_read_b128 a[204:207], v1 offset:6272\n"
    "v_accvgpr_read_b32 v106, a14\n"
    "v_mfma_f32_16x16x32_bf16 v[68:71], a[192:195], a[96:99], 0\n"
    "v_exp_f32 v62, v62\n"
    "v_mfma_f32_16x16x32_bf16 v[68:71], a[196:199], a[100:103], v[68:71]\n"
    "v_exp_f32 v63, v63\n"
    "v_mfma_f32_16x16x32_bf16 v[68:71], a[200:203], a[104:107], v[68:71]\n"
    "v_exp_f32 v64, v64\n"
    "v_mfma_f32_16x16x32_bf16 v[72:75], a[200:203], a[128:131], 0\n"
    "v_exp_f32 v65, v65\n"
    "v_mfma_f32_16x16x32_bf16 v[72:75], a[196:199], a[124:127], v[72:75]\n"
    "v_exp_f32 v66, v66\n"
    "v_mfma_f32_16x16x32_bf16 v[72:75], a[192:195], a[120:123], v[72:75]\n"
    "v_exp_f32 v67, v67\n"
    "v_mfma_f32_16x16x32_bf16 v[76:79], a[192:195], a[144:147], 0\n"
    "buffer_load_dwordx4 v6, s[40:43], s56 offen offset:0 nt lds\n"
    "v_mfma_f32_16x16x32_bf16 v[76:79], a[196:199], a[148:151], v[76:79]\n"
    "v_cvt_pk_bf16_f32 v55, v62, v63\n"
    "v_cvt_pk_bf16_f32 v58, v64, v65\n"
    "v_mfma_f32_16x16x32_bf16 v[76:79], a[200:203], a[152:155], v[76:79]\n"
    "s_waitcnt lgkmcnt(3)\n"
    "v_cvt_pk_bf16_f32 v59, v66, v67\n"
    "v_accvgpr_read_b32 v107, a15\n"
    "v_mfma_f32_16x16x32_bf16 v[80:83], a[200:203], a[176:179], 0\n"
    "buffer_store_dwordx4 v[92:95], v9, s[48:51], s74 offen offset:0 sc1\n"
    "v_mfma_f32_16x16x32_bf16 v[80:83], a[196:199], a[172:175], v[80:83]\n"
    "ds_read_b64_tr_b16 a[196:197], v3 offset:49152\n"
    "v_accvgpr_read_b32 v88, a8\n"
    "v_mfma_f32_16x16x32_bf16 v[80:83], a[192:195], a[168:171], v[80:83]\n"
    "s_waitcnt lgkmcnt(1)\n"
    "ds_read_b64_tr_b16 a[192:193], v2 offset:49152\n"
    "v_accvgpr_read_b32 v89, a9\n"
    "v_mfma_f32_16x16x32_bf16 v[68:71], a[204:207], a[108:111], v[68:71]\n"
    "ds_read_b64_tr_b16 a[194:195], v2 offset:55296\n"
    "v_accvgpr_read_b32 v90, a10\n"
    "v_mfma_f32_16x16x32_bf16 v[68:71], a[208:211], a[112:115], v[68:71]\n"
    "ds_read_b64_tr_b16 a[198:199], v3 offset:55296\n"
    "v_accvgpr_read_b32 v91, a11\n"
    "v_mfma_f32_16x16x32_bf16 v[68:71], a[212:215], a[116:119], v[68:71]\n"
    "ds_read_b64_tr_b16 a[200:201], v4 offset:49152\n"
    "v_fma_f32 v28, v28, s52, v12\n"
    "v_mfma_f32_16x16x32_bf16 v[72:75], a[212:215], a[140:143], v[72:75]\n"
    "ds_read_b64_tr_b16 a[202:203], v4 offset:55296\n"
    "v_fma_f32 v29, v29, s52, v12\n"
    "v_mfma_f32_16x16x32_bf16 v[72:75], a[208:211], a[136:139], v[72:75]\n"
    "v_exp_f32 v28, v28\n"
    "v_mfma_f32_16x16x32_bf16 v[72:75], a[204:207], a[132:135], v[72:75]\n"
    "v_exp_f32 v29, v29\n"
    "v_mfma_f32_16x16x32_bf16 v[76:79], a[204:207], a[156:159], v[76:79]\n"
    "v_fma_f32 v30, v30, s52, v12\n"
    "v_fma_f32 v31, v31, s52, v12\n"
    "v_mfma_f32_16x16x32_bf16 v[76:79], a[208:211], a[160:163], v[76:79]\n"
    "v_exp_f32 v30, v30\n"
    "v_mfma_f32_16x16x32_bf16 v[76:79], a[212:215], a[164:167], v[76:79]\n"
    "buffer_load_dwordx4 v7, s[40:43], s56 offen offset:1024 nt lds\n"
    "v_mfma_f32_16x16x32_bf16 v[80:83], a[212:215], a[188:191], v[80:83]\n"
    "ds_read_b64_tr_b16 a[212:213], v3 offset:49280\n"
    "v_fma_f32 v68, v68, s52, v12\n"
    "v_mfma_f32_16x16x32_bf16 v[80:83], a[208:211], a[184:187], v[80:83]\n"
    "ds_read_b64_tr_b16 a[208:209], v2 offset:49280\n"
    "v_fma_f32 v69, v69, s52, v12\n"
    "v_mfma_f32_16x16x32_bf16 v[80:83], a[204:207], a[180:183], v[80:83]\n"
    "ds_read_b64_tr_b16 a[204:205], v5 offset:49152\n"
    "v_fma_f32 v70, v70, s52, v12\n"
    "v_smfmac_f32_16x16x64_bf16 a[244:247], a[252:255], v[44:51], v23\n"
    "ds_read_b64_tr_b16 a[206:207], v5 offset:55296\n"
    "v_fma_f32 v71, v71, s52, v12\n"
    "v_smfmac_f32_16x16x64_bf16 a[248:251], a[252:255], v[52:59], v23\n"
    "s_waitcnt lgkmcnt(7)\n"
    "ds_read_b64_tr_b16 a[210:211], v2 offset:55424\n"
    "v_cvt_pk_bf16_f32 v28, v28, v29\n"
    "v_mfma_f32_16x16x32_bf16 v[104:107], a[192:195], v[56:59], v[104:107]\n"
    "ds_read_b64_tr_b16 a[214:215], v3 offset:55424\n"
    "v_fma_f32 v32, v32, s52, v13\n"
    "v_mfma_f32_16x16x32_bf16 v[88:91], a[192:195], v[52:55], v[88:91]\n"
    "v_exp_f32 v31, v31\n"
    "v_mfma_f32_16x16x32_bf16 v[100:103], a[192:195], v[48:51], v[100:103]\n"
    "v_exp_f32 v68, v68\n"
    "v_mfma_f32_16x16x32_bf16 v[84:87], a[192:195], v[44:47], v[84:87]\n"
    "s_waitcnt lgkmcnt(8)\n"
    "ds_read_b64_tr_b16 a[192:193], v4 offset:49280\n"
    "v_cvt_pk_bf16_f32 v29, v30, v31\n"
    "v_mfma_f32_16x16x32_bf16 a[16:19], a[196:199], v[44:47], a[16:19]\n"
    "ds_read_b64_tr_b16 a[194:195], v4 offset:55424\n"
    "v_fma_f32 v33, v33, s52, v13\n"
    "v_mfma_f32_16x16x32_bf16 a[20:23], a[196:199], v[48:51], a[20:23]\n"
    "v_exp_f32 v69, v69\n"
    "v_mfma_f32_16x16x32_bf16 a[24:27], a[196:199], v[52:55], a[24:27]\n"
    "v_exp_f32 v70, v70\n"
    "v_mfma_f32_16x16x32_bf16 a[28:31], a[196:199], v[56:59], a[28:31]\n"
    "s_waitcnt lgkmcnt(8)\n"
    "ds_read_b64_tr_b16 a[196:197], v5 offset:49280\n"
    "v_cvt_pk_bf16_f32 v30, v68, v69\n"
    "v_mfma_f32_16x16x32_bf16 a[44:47], a[200:203], v[56:59], a[44:47]\n"
    "buffer_load_dwordx4 v8, s[40:43], s56 offen offset:2048 nt lds\n"
    "v_mfma_f32_16x16x32_bf16 a[40:43], a[200:203], v[52:55], a[40:43]\n"
    "s_add_u32 s56, s56, 24576\n"
    "ds_read_b64_tr_b16 a[198:199], v5 offset:55424\n"
    "v_fma_f32 v34, v34, s52, v13\n"
    "v_mfma_f32_16x16x32_bf16 a[36:39], a[200:203], v[48:51], a[36:39]\n"
    "v_exp_f32 v71, v71\n"
    "v_mfma_f32_16x16x32_bf16 a[32:35], a[200:203], v[44:47], a[32:35]\n"
    "s_waitcnt lgkmcnt(6)\n"
    "ds_read_b64_tr_b16 a[200:201], v2 offset:49408\n"
    "v_cvt_pk_bf16_f32 v31, v70, v71\n"
    "v_mfma_f32_16x16x32_bf16 a[48:51], a[204:207], v[44:47], a[48:51]\n"
    "ds_read_b64_tr_b16 a[202:203], v2 offset:55552\n"
    "v_fma_f32 v35, v35, s52, v13\n"
    "v_mfma_f32_16x16x32_bf16 a[52:55], a[204:207], v[48:51], a[52:55]\n"
    "v_exp_f32 v32, v32\n"
    "v_mfma_f32_16x16x32_bf16 a[56:59], a[204:207], v[52:55], a[56:59]\n"
    "v_exp_f32 v33, v33\n"
    "v_mfma_f32_16x16x32_bf16 a[60:63], a[204:207], v[56:59], a[60:63]\n"
    "s_waitcnt lgkmcnt(7)\n"
    "ds_read_b64_tr_b16 a[204:205], v3 offset:49408\n"
    "v_fma_f32 v72, v72, s52, v13\n"
    "v_mfma_f32_16x16x32_bf16 a[76:79], a[208:211], v[56:59], a[76:79]\n"
    "ds_read_b64_tr_b16 a[206:207], v3 offset:55552\n"
    "v_fma_f32 v73, v73, s52, v13\n"
    "v_mfma_f32_16x16x32_bf16 a[72:75], a[208:211], v[52:55], a[72:75]\n"
    "ds_read_b64 v[108:109], v11 offset:5440\n"
    "v_fma_f32 v74, v74, s52, v13\n"
    "v_mfma_f32_16x16x32_bf16 a[68:71], a[208:211], v[48:51], a[68:71]\n"
    "ds_read_b64 v[110:111], v20 offset:5440\n"
    "v_fma_f32 v75, v75, s52, v13\n"
    "v_mfma_f32_16x16x32_bf16 a[64:67], a[208:211], v[44:47], a[64:67]\n"
    "s_waitcnt lgkmcnt(10)\n"
    "ds_read_b64_tr_b16 a[208:209], v4 offset:49408\n"
    "v_cvt_pk_bf16_f32 v32, v32, v33\n"
    "v_mfma_f32_16x16x32_bf16 a[80:83], a[212:215], v[44:47], a[80:83]\n"
    "ds_read_b64_tr_b16 a[210:211], v4 offset:55552\n"
    "v_fma_f32 v36, v36, s52, v14\n"
    "v_mfma_f32_16x16x32_bf16 a[84:87], a[212:215], v[48:51], a[84:87]\n"
    "v_exp_f32 v34, v34\n"
    "v_mfma_f32_16x16x32_bf16 a[88:91], a[212:215], v[52:55], a[88:91]\n"
    "v_exp_f32 v35, v35\n"
    "v_mfma_f32_16x16x32_bf16 a[92:95], a[212:215], v[56:59], a[92:95]\n"
    "s_waitcnt lgkmcnt(10)\n"
    "ds_read_b64_tr_b16 a[212:213], v5 offset:49408\n"
    "v_cvt_pk_bf16_f32 v33, v34, v35\n"
    "v_mfma_f32_16x16x32_bf16 v[128:131], a[192:195], v[56:59], v[128:131]\n"
    "ds_read_b64_tr_b16 a[214:215], v5 offset:55552\n"
    "v_fma_f32 v37, v37, s52, v14\n"
    "v_mfma_f32_16x16x32_bf16 v[124:127], a[192:195], v[52:55], v[124:127]\n"
    "v_exp_f32 v72, v72\n"
    "v_mfma_f32_16x16x32_bf16 v[120:123], a[192:195], v[48:51], v[120:123]\n"
    "v_exp_f32 v73, v73\n"
    "v_mfma_f32_16x16x32_bf16 v[116:119], a[192:195], v[44:47], v[116:119]\n"
    "s_waitcnt lgkmcnt(10)\n"
    "v_exp_f32 v74, v74\n"
    "v_mfma_f32_16x16x32_bf16 a[216:219], a[196:199], v[44:47], a[216:219]\n"
    "v_exp_f32 v75, v75\n"
    "v_mfma_f32_16x16x32_bf16 a[220:223], a[196:199], v[48:51], a[220:223]\n"
    "v_cvt_pk_bf16_f32 v34, v72, v73\n"
    "v_cvt_pk_bf16_f32 v35, v74, v75\n"
    "v_mfma_f32_16x16x32_bf16 a[224:227], a[196:199], v[52:55], a[224:227]\n"
    "s_waitcnt vmcnt(23) lgkmcnt(0)\n"
    "v_fma_f32 v40, v40, s52, v15\n"
    "v_fma_f32 v41, v41, s52, v15\n"
    "v_mfma_f32_16x16x32_bf16 a[228:231], a[196:199], v[56:59], a[228:231]\n"
    "s_barrier\n"
    "ds_read_b128 a[192:195], v0 offset:12288\n"
    "v_fma_f32 v38, v38, s52, v14\n"
    "v_mfma_f32_16x16x32_bf16 v[188:191], a[200:203], v[56:59], v[188:191]\n"
    "s_add_u32 m0, s54, 0\n"
    "ds_read_b128 a[196:199], v1 offset:12288\n"
    "v_fma_f32 v39, v39, s52, v14\n"
    "v_mfma_f32_16x16x32_bf16 v[184:187], a[200:203], v[52:55], v[184:187]\n"
    "buffer_store_dwordx4 v[108:111], v9, s[48:51], s74 offen offset:768 sc1\n"
    "v_mfma_f32_16x16x32_bf16 v[180:183], a[200:203], v[48:51], v[180:183]\n"
    "v_exp_f32 v36, v36\n"
    "v_mfma_f32_16x16x32_bf16 v[176:179], a[200:203], v[44:47], v[176:179]\n"
    "ds_read_b128 a[200:203], v0 offset:12416\n"
    "v_fma_f32 v42, v42, s52, v15\n"
    "v_mfma_f32_16x16x32_bf16 a[232:235], a[204:207], v[44:47], a[232:235]\n"
    "v_exp_f32 v37, v37\n"
    "v_mfma_f32_16x16x32_bf16 v[148:151], a[204:207], v[48:51], v[148:151]\n"
    "v_exp_f32 v40, v40\n"
    "v_mfma_f32_16x16x32_bf16 v[152:155], a[204:207], v[52:55], v[152:155]\n"
    "v_exp_f32 v41, v41\n"
    "v_mfma_f32_16x16x32_bf16 v[156:159], a[204:207], v[56:59], v[156:159]\n"
    "ds_read_b128 a[204:207], v1 offset:12416\n"
    "v_fma_f32 v43, v43, s52, v15\n"
    "v_mfma_f32_16x16x32_bf16 v[220:223], a[208:211], v[56:59], v[220:223]\n"
    "buffer_load_dwordx4 v6, s[36:39], s56 offen offset:0 nt lds\n"
    "v_mfma_f32_16x16x32_bf16 v[216:219], a[208:211], v[52:55], v[216:219]\n"
    "v_exp_f32 v38, v38\n"
    "v_mfma_f32_16x16x32_bf16 v[212:215], a[208:211], v[48:51], v[212:215]\n"
    "v_exp_f32 v39, v39\n"
    "v_mfma_f32_16x16x32_bf16 v[208:211], a[208:211], v[44:47], v[208:211]\n"
    "ds_write_b64 v10, v[96:97] offset:7680\n"
    "v_mfma_f32_16x16x32_bf16 v[240:243], a[212:215], v[44:47], v[240:243]\n"
    "ds_read_b128 a[208:211], v0 offset:12544\n"
    "v_fma_f32 v76, v76, s52, v14\n"
    "v_mfma_f32_16x16x32_bf16 v[244:247], a[212:215], v[48:51], v[244:247]\n"
    "v_exp_f32 v42, v42\n"
    "v_mfma_f32_16x16x32_bf16 v[248:251], a[212:215], v[52:55], v[248:251]\n"
    "v_exp_f32 v43, v43\n"
    "v_mfma_f32_16x16x32_bf16 v[252:255], a[212:215], v[56:59], v[252:255]\n"
    "s_waitcnt lgkmcnt(5)\n"
    "ds_read_b128 a[212:215], v1 offset:12544\n"
    "v_fma_f32 v77, v77, s52, v14\n"
    "v_mfma_f32_16x16x32_bf16 v[44:47], a[192:195], a[96:99], 0\n"
    "s_waitcnt lgkmcnt(5)\n"
    "ds_write_b64 v10, v[98:99] offset:7712\n"
    "v_mfma_f32_16x16x32_bf16 v[44:47], a[196:199], a[100:103], v[44:47]\n"
    "s_waitcnt lgkmcnt(5)\n"
    "v_fma_f32 v78, v78, s52, v14\n"
    "v_fma_f32 v79, v79, s52, v14\n"
    "v_mfma_f32_16x16x32_bf16 v[44:47], a[200:203], a[104:107], v[44:47]\n"
    "v_fma_f32 v80, v80, s52, v15\n"
    "v_fma_f32 v81, v81, s52, v15\n"
    "v_mfma_f32_16x16x32_bf16 v[48:51], a[200:203], a[128:131], 0\n"
    "v_fma_f32 v82, v82, s52, v15\n"
    "v_fma_f32 v83, v83, s52, v15\n"
    "v_mfma_f32_16x16x32_bf16 v[48:51], a[196:199], a[124:127], v[48:51]\n"
    "buffer_load_dwordx4 v7, s[36:39], s56 offen offset:1024 nt lds\n"
    "v_mfma_f32_16x16x32_bf16 v[48:51], a[192:195], a[120:123], v[48:51]\n"
    "ds_write_b64 v10, v[112:113] offset:7744\n"
    "v_mfma_f32_16x16x32_bf16 v[52:55], a[192:195], a[144:147], 0\n"
    "v_cvt_pk_bf16_f32 v36, v36, v37\n"
    "v_cvt_pk_bf16_f32 v40, v40, v41\n"
    "v_mfma_f32_16x16x32_bf16 v[52:55], a[196:199], a[148:151], v[52:55]\n"
    "v_exp_f32 v76, v76\n"
    "v_mfma_f32_16x16x32_bf16 v[52:55], a[200:203], a[152:155], v[52:55]\n"
    "v_exp_f32 v77, v77\n"
    "v_mfma_f32_16x16x32_bf16 v[56:59], a[200:203], a[176:179], 0\n"
    "ds_read_b128 a[200:203], v0 offset:18560\n"
    "v_cvt_pk_bf16_f32 v37, v38, v39\n"
    "v_mfma_f32_16x16x32_bf16 v[56:59], a[196:199], a[172:175], v[56:59]\n"
    "ds_write_b64 v10, v[114:115] offset:7776\n"
    "v_mfma_f32_16x16x32_bf16 v[56:59], a[192:195], a[168:171], v[56:59]\n"
    "s_waitcnt lgkmcnt(7)\n"
    "ds_read_b128 a[192:195], v0 offset:18432\n"
    "v_cvt_pk_bf16_f32 v41, v42, v43\n"
    "v_mfma_f32_16x16x32_bf16 v[44:47], a[204:207], a[108:111], v[44:47]\n"
    "s_waitcnt lgkmcnt(6)\n"
    "ds_read_b128 a[196:199], v1 offset:18432\n"
    "v_cvt_pk_bf16_f32 v38, v76, v77\n"
    "v_mfma_f32_16x16x32_bf16 v[44:47], a[208:211], a[112:115], v[44:47]\n"
    "s_waitcnt lgkmcnt(6)\n"
    "v_exp_f32 v78, v78\n"
    "v_mfma_f32_16x16x32_bf16 v[44:47], a[212:215], a[116:119], v[44:47]\n"
    "v_exp_f32 v79, v79\n"
    "v_mfma_f32_16x16x32_bf16 v[48:51], a[212:215], a[140:143], v[48:51]\n"
    "v_exp_f32 v80, v80\n"
    "v_mfma_f32_16x16x32_bf16 v[48:51], a[208:211], a[136:139], v[48:51]\n"
    "buffer_load_dwordx4 v8, s[36:39], s56 offen offset:2048 nt lds\n"
    "v_mfma_f32_16x16x32_bf16 v[48:51], a[204:207], a[132:135], v[48:51]\n"
    "s_add_u32 m0, s54, 110592\n"
    "v_exp_f32 v81, v81\n"
    "v_mfma_f32_16x16x32_bf16 v[52:55], a[204:207], a[156:159], v[52:55]\n"
    "v_exp_f32 v82, v82\n"
    "v_mfma_f32_16x16x32_bf16 v[52:55], a[208:211], a[160:163], v[52:55]\n"
    "ds_read_b64 v[96:97], v11 offset:7680\n"
    "v_cvt_pk_bf16_f32 v39, v78, v79\n"
    "v_mfma_f32_16x16x32_bf16 v[52:55], a[212:215], a[164:167], v[52:55]\n"
    "ds_read_b64 v[98:99], v20 offset:7680\n"
    "v_cvt_pk_bf16_f32 v42, v80, v81\n"
    "v_mfma_f32_16x16x32_bf16 v[56:59], a[212:215], a[188:191], v[56:59]\n"
    "ds_read_b128 a[212:215], v1 offset:18688\n"
    "v_accvgpr_read_b32 v92, a16\n"
    "v_mfma_f32_16x16x32_bf16 v[56:59], a[208:211], a[184:187], v[56:59]\n"
    "ds_read_b128 a[208:211], v0 offset:18688\n"
    "v_accvgpr_read_b32 v93, a17\n"
    "v_mfma_f32_16x16x32_bf16 v[56:59], a[204:207], a[180:183], v[56:59]\n"
    "s_waitcnt lgkmcnt(5)\n"
    "ds_read_b128 a[204:207], v1 offset:18560\n"
    "v_accvgpr_read_b32 v94, a18\n"
    "v_mfma_f32_16x16x32_bf16 v[60:63], a[192:195], a[96:99], 0\n"
    "s_waitcnt lgkmcnt(5)\n"
    "v_exp_f32 v83, v83\n"
    "v_mfma_f32_16x16x32_bf16 v[60:63], a[196:199], a[100:103], v[60:63]\n"
    "v_cvt_pk_bf16_f32 v43, v82, v83\n"
    "v_accvgpr_read_b32 v95, a19\n"
    "v_mfma_f32_16x16x32_bf16 v[60:63], a[200:203], a[104:107], v[60:63]\n"
    "v_accvgpr_read_b32 v108, a20\n"
    "v_accvgpr_read_b32 v109, a21\n"
    "v_mfma_f32_16x16x32_bf16 v[64:67], a[200:203], a[128:131], 0\n"
    "v_accvgpr_read_b32 v110, a22\n"
    "v_accvgpr_read_b32 v111, a23\n"
    "v_mfma_f32_16x16x32_bf16 v[64:67], a[196:199], a[124:127], v[64:67]\n"
    "buffer_load_dwordx4 v6, s[40:43], s55 offen offset:0 nt lds\n"
    "v_mfma_f32_16x16x32_bf16 v[64:67], a[192:195], a[120:123], v[64:67]\n"
    "v_fma_f32 v44, v44, s52, v12\n"
    "v_fma_f32 v45, v45, s52, v12\n"
    "v_mfma_f32_16x16x32_bf16 v[68:71], a[192:195], a[144:147], 0\n"
    "v_exp_f32 v44, v44\n"
    "v_mfma_f32_16x16x32_bf16 v[68:71], a[196:199], a[148:151], v[68:71]\n"
    "v_exp_f32 v45, v45\n"
    "v_mfma_f32_16x16x32_bf16 v[68:71], a[200:203], a[152:155], v[68:71]\n"
    "v_fma_f32 v46, v46, s52, v12\n"
    "v_fma_f32 v47, v47, s52, v12\n"
    "v_mfma_f32_16x16x32_bf16 v[72:75], a[200:203], a[176:179], 0\n"
    "ds_read_b64_tr_b16 a[200:201], v4 offset:0\n"
    "v_cvt_pk_bf16_f32 v44, v44, v45\n"
    "v_mfma_f32_16x16x32_bf16 v[72:75], a[196:199], a[172:175], v[72:75]\n"
    "s_waitcnt lgkmcnt(4)\n"
    "ds_read_b64_tr_b16 a[196:197], v3 offset:0\n"
    "v_fma_f32 v48, v48, s52, v13\n"
    "v_mfma_f32_16x16x32_bf16 v[72:75], a[192:195], a[168:171], v[72:75]\n"
    "s_waitcnt lgkmcnt(2)\n"
    "buffer_store_dwordx4 v[96:99], v9, s[48:51], s75 offen offset:0 sc1\n"
    "v_mfma_f32_16x16x32_bf16 v[60:63], a[204:207], a[108:111], v[60:63]\n"
    "ds_read_b64_tr_b16 a[192:193], v2 offset:0\n"
    "v_fma_f32 v49, v49, s52, v13\n"
    "v_mfma_f32_16x16x32_bf16 v[60:63], a[208:211], a[112:115], v[60:63]\n"
    "ds_read_b64_tr_b16 a[194:195], v2 offset:6144\n"
    "v_fma_f32 v50, v50, s52, v13\n"
    "v_mfma_f32_16x16x32_bf16 v[60:63], a[212:215], a[116:119], v[60:63]\n"
    "ds_read_b64_tr_b16 a[198:199], v3 offset:6144\n"
    "v_fma_f32 v51, v51, s52, v13\n"
    "v_mfma_f32_16x16x32_bf16 v[64:67], a[212:215], a[140:143], v[64:67]\n"
    "ds_read_b64_tr_b16 a[202:203], v4 offset:6144\n"
    "v_fma_f32 v52, v52, s52, v14\n"
    "v_mfma_f32_16x16x32_bf16 v[64:67], a[208:211], a[136:139], v[64:67]\n"
    "buffer_load_dwordx4 v7, s[40:43], s55 offen offset:1024 nt lds\n"
    "v_mfma_f32_16x16x32_bf16 v[64:67], a[204:207], a[132:135], v[64:67]\n"
    "v_exp_f32 v46, v46\n"
    "v_mfma_f32_16x16x32_bf16 v[68:71], a[204:207], a[156:159], v[68:71]\n"
    "v_exp_f32 v47, v47\n"
    "v_mfma_f32_16x16x32_bf16 v[68:71], a[208:211], a[160:163], v[68:71]\n"
    "v_fma_f32 v60, v60, s52, v12\n"
    "v_fma_f32 v61, v61, s52, v12\n"
    "v_mfma_f32_16x16x32_bf16 v[68:71], a[212:215], a[164:167], v[68:71]\n"
    "v_fma_f32 v62, v62, s52, v12\n"
    "v_fma_f32 v63, v63, s52, v12\n"
    "v_mfma_f32_16x16x32_bf16 v[72:75], a[212:215], a[188:191], v[72:75]\n"
    "ds_read_b64_tr_b16 a[212:213], v3 offset:128\n"
    "v_cvt_pk_bf16_f32 v45, v46, v47\n"
    "v_mfma_f32_16x16x32_bf16 v[72:75], a[208:211], a[184:187], v[72:75]\n"
    "ds_read_b64_tr_b16 a[208:209], v2 offset:128\n"
    "v_fma_f32 v64, v64, s52, v13\n"
    "v_mfma_f32_16x16x32_bf16 v[72:75], a[204:207], a[180:183], v[72:75]\n"
    "ds_read_b64_tr_b16 a[204:205], v5 offset:0\n"
    "v_fma_f32 v65, v65, s52, v13\n"
    "v_smfmac_f32_16x16x64_bf16 a[244:247], a[252:255], v[28:35], v23\n"
    "ds_read_b64_tr_b16 a[206:207], v5 offset:6144\n"
    "v_fma_f32 v66, v66, s52, v13\n"
    "v_smfmac_f32_16x16x64_bf16 a[248:251], a[252:255], v[36:43], v23\n"
    "s_waitcnt lgkmcnt(6)\n"
    "ds_read_b64_tr_b16 a[210:211], v2 offset:6272\n"
    "v_fma_f32 v67, v67, s52, v13\n"
    "v_mfma_f32_16x16x32_bf16 v[104:107], a[192:195], v[40:43], v[104:107]\n"
    "ds_read_b64_tr_b16 a[214:215], v3 offset:6272\n"
    "v_fma_f32 v53, v53, s52, v14\n"
    "v_mfma_f32_16x16x32_bf16 v[88:91], a[192:195], v[36:39], v[88:91]\n"
    "v_exp_f32 v60, v60\n"
    "v_mfma_f32_16x16x32_bf16 v[100:103], a[192:195], v[32:35], v[100:103]\n"
    "buffer_load_dwordx4 v8, s[40:43], s55 offen offset:2048 nt lds\n"
    "v_mfma_f32_16x16x32_bf16 v[84:87], a[192:195], v[28:31], v[84:87]\n"
    "s_waitcnt lgkmcnt(7)\n"
    "ds_read_b64_tr_b16 a[192:193], v4 offset:128\n"
    "v_fma_f32 v56, v56, s52, v15\n"
    "v_mfma_f32_16x16x32_bf16 v[92:95], a[196:199], v[28:31], v[92:95]\n"
    "s_add_u32 s55, s55, 24576\n"
    "ds_read_b64_tr_b16 a[194:195], v4 offset:6272\n"
    "v_fma_f32 v57, v57, s52, v15\n"
    "v_mfma_f32_16x16x32_bf16 v[108:111], a[196:199], v[32:35], v[108:111]\n"
    "v_exp_f32 v61, v61\n"
    "v_mfma_f32_16x16x32_bf16 a[24:27], a[196:199], v[36:39], a[24:27]\n"
    "v_exp_f32 v62, v62\n"
    "v_mfma_f32_16x16x32_bf16 a[28:31], a[196:199], v[40:43], a[28:31]\n"
    "s_waitcnt lgkmcnt(8)\n"
    "ds_read_b64_tr_b16 a[196:197], v5 offset:128\n"
    "v_cvt_pk_bf16_f32 v46, v60, v61\n"
    "v_mfma_f32_16x16x32_bf16 a[44:47], a[200:203], v[40:43], a[44:47]\n"
    "ds_read_b64_tr_b16 a[198:199], v5 offset:6272\n"
    "v_fma_f32 v54, v54, s52, v14\n"
    "v_mfma_f32_16x16x32_bf16 a[40:43], a[200:203], v[36:39], a[40:43]\n"
    "v_exp_f32 v63, v63\n"
    "v_mfma_f32_16x16x32_bf16 a[36:39], a[200:203], v[32:35], a[36:39]\n"
    "v_cvt_pk_bf16_f32 v47, v62, v63\n"
    "v_fma_f32 v55, v55, s52, v14\n"
    "v_mfma_f32_16x16x32_bf16 a[32:35], a[200:203], v[28:31], a[32:35]\n"
    "s_waitcnt lgkmcnt(6)\n"
    "ds_read_b64_tr_b16 a[200:201], v2 offset:256\n"
    "v_fma_f32 v58, v58, s52, v15\n"
    "v_mfma_f32_16x16x32_bf16 a[48:51], a[204:207], v[28:31], a[48:51]\n"
    "ds_read_b64_tr_b16 a[202:203], v2 offset:6400\n"
    "v_fma_f32 v59, v59, s52, v15\n"
    "v_mfma_f32_16x16x32_bf16 a[52:55], a[204:207], v[32:35], a[52:55]\n"
    "v_exp_f32 v48, v48\n"
    "v_mfma_f32_16x16x32_bf16 a[56:59], a[204:207], v[36:39], a[56:59]\n"
    "v_exp_f32 v49, v49\n"
    "v_mfma_f32_16x16x32_bf16 a[60:63], a[204:207], v[40:43], a[60:63]\n"
    "s_waitcnt lgkmcnt(7)\n"
    "ds_read_b64_tr_b16 a[204:205], v3 offset:256\n"
    "v_cvt_pk_bf16_f32 v48, v48, v49\n"
    "v_mfma_f32_16x16x32_bf16 a[76:79], a[208:211], v[40:43], a[76:79]\n"
    "ds_read_b64_tr_b16 a[206:207], v3 offset:6400\n"
    "v_fma_f32 v68, v68, s52, v14\n"
    "v_mfma_f32_16x16x32_bf16 a[72:75], a[208:211], v[36:39], a[72:75]\n"
    "v_exp_f32 v50, v50\n"
    "v_mfma_f32_16x16x32_bf16 a[68:71], a[208:211], v[32:35], a[68:71]\n"
    "v_exp_f32 v51, v51\n"
    "v_mfma_f32_16x16x32_bf16 a[64:67], a[208:211], v[28:31], a[64:67]\n"
    "s_waitcnt lgkmcnt(8)\n"
    "ds_read_b64 v[112:113], v11 offset:8000\n"
    "v_cvt_pk_bf16_f32 v49, v50, v51\n"
    "v_mfma_f32_16x16x32_bf16 a[80:83], a[212:215], v[28:31], a[80:83]\n"
    "ds_read_b64 v[114:115], v20 offset:8000\n"
    "v_fma_f32 v69, v69, s52, v14\n"
    "v_mfma_f32_16x16x32_bf16 a[84:87], a[212:215], v[32:35], a[84:87]\n"
    "ds_read_b64_tr_b16 a[208:209], v4 offset:256\n"
    "v_fma_f32 v70, v70, s52, v14\n"
    "v_mfma_f32_16x16x32_bf16 a[88:91], a[212:215], v[36:39], a[88:91]\n"
    "ds_read_b64_tr_b16 a[210:211], v4 offset:6400\n"
    "v_fma_f32 v71, v71, s52, v14\n"
    "v_mfma_f32_16x16x32_bf16 a[92:95], a[212:215], v[40:43], a[92:95]\n"
    "s_waitcnt lgkmcnt(10)\n"
    "ds_read_b64_tr_b16 a[212:213], v5 offset:256\n"
    "v_fma_f32 v72, v72, s52, v15\n"
    "v_mfma_f32_16x16x32_bf16 v[128:131], a[192:195], v[40:43], v[128:131]\n"
    "ds_read_b64_tr_b16 a[214:215], v5 offset:6400\n"
    "v_fma_f32 v73, v73, s52, v15\n"
    "v_mfma_f32_16x16x32_bf16 v[124:127], a[192:195], v[36:39], v[124:127]\n"
    "v_exp_f32 v64, v64\n"
    "v_mfma_f32_16x16x32_bf16 v[120:123], a[192:195], v[32:35], v[120:123]\n"
    "v_exp_f32 v65, v65\n"
    "v_mfma_f32_16x16x32_bf16 v[116:119], a[192:195], v[28:31], v[116:119]\n"
    "s_waitcnt lgkmcnt(10)\n"
    "v_exp_f32 v66, v66\n"
    "v_mfma_f32_16x16x32_bf16 a[216:219], a[196:199], v[28:31], a[216:219]\n"
    "v_exp_f32 v67, v67\n"
    "v_mfma_f32_16x16x32_bf16 a[220:223], a[196:199], v[32:35], a[220:223]\n"
    "v_cvt_pk_bf16_f32 v50, v64, v65\n"
    "v_cvt_pk_bf16_f32 v51, v66, v67\n"
    "v_mfma_f32_16x16x32_bf16 a[224:227], a[196:199], v[36:39], a[224:227]\n"
    "s_waitcnt vmcnt(23) lgkmcnt(0)\n"
    "v_exp_f32 v52, v52\n"
    "v_mfma_f32_16x16x32_bf16 a[228:231], a[196:199], v[40:43], a[228:231]\n"
    "s_barrier\n"
    "ds_read_b128 a[192:195], v0 offset:24576\n"
    "v_fma_f32 v74, v74, s52, v15\n"
    "v_mfma_f32_16x16x32_bf16 v[188:191], a[200:203], v[40:43], v[188:191]\n"
    "s_add_u32 m0, s54, 12288\n"
    "ds_read_b128 a[196:199], v1 offset:24576\n"
    "v_fma_f32 v75, v75, s52, v15\n"
    "v_mfma_f32_16x16x32_bf16 v[184:187], a[200:203], v[36:39], v[184:187]\n"
    "v_exp_f32 v53, v53\n"
    "v_mfma_f32_16x16x32_bf16 v[180:183], a[200:203], v[32:35], v[180:183]\n"
    "v_exp_f32 v56, v56\n"
    "v_mfma_f32_16x16x32_bf16 v[176:179], a[200:203], v[28:31], v[176:179]\n"
    "buffer_store_dwordx4 v[112:115], v9, s[48:51], s75 offen offset:768 sc1\n"
    "v_mfma_f32_16x16x32_bf16 a[232:235], a[204:207], v[28:31], a[232:235]\n"
    "ds_read_b128 a[200:203], v0 offset:24704\n"
    "v_cvt_pk_bf16_f32 v52, v52, v53\n"
    "v_mfma_f32_16x16x32_bf16 v[148:151], a[204:207], v[32:35], v[148:151]\n"
    "v_exp_f32 v57, v57\n"
    "v_mfma_f32_16x16x32_bf16 v[152:155], a[204:207], v[36:39], v[152:155]\n"
    "v_exp_f32 v54, v54\n"
    "v_mfma_f32_16x16x32_bf16 v[156:159], a[204:207], v[40:43], v[156:159]\n"
    "ds_read_b128 a[204:207], v1 offset:24704\n"
    "v_cvt_pk_bf16_f32 v56, v56, v57\n"
    "v_mfma_f32_16x16x32_bf16 v[220:223], a[208:211], v[40:43], v[220:223]\n"
    "buffer_load_dwordx4 v6, s[36:39], s55 offen offset:0 nt lds\n"
    "v_mfma_f32_16x16x32_bf16 v[216:219], a[208:211], v[36:39], v[216:219]\n"
    "v_exp_f32 v55, v55\n"
    "v_mfma_f32_16x16x32_bf16 v[212:215], a[208:211], v[32:35], v[212:215]\n"
    "v_exp_f32 v58, v58\n"
    "v_mfma_f32_16x16x32_bf16 v[208:211], a[208:211], v[28:31], v[208:211]\n"
    "ds_read_b128 a[208:211], v0 offset:24832\n"
    "v_cvt_pk_bf16_f32 v53, v54, v55\n"
    "v_mfma_f32_16x16x32_bf16 v[240:243], a[212:215], v[28:31], v[240:243]\n"
    "v_exp_f32 v59, v59\n"
    "v_mfma_f32_16x16x32_bf16 v[244:247], a[212:215], v[32:35], v[244:247]\n"
    "ds_write_b64 v10, v[132:133] offset:0\n"
    "v_mfma_f32_16x16x32_bf16 v[248:251], a[212:215], v[36:39], v[248:251]\n"
    "v_exp_f32 v68, v68\n"
    "v_mfma_f32_16x16x32_bf16 v[252:255], a[212:215], v[40:43], v[252:255]\n"
    "s_waitcnt lgkmcnt(5)\n"
    "ds_read_b128 a[212:215], v1 offset:24832\n"
    "v_cvt_pk_bf16_f32 v57, v58, v59\n"
    "v_mfma_f32_16x16x32_bf16 v[28:31], a[192:195], a[96:99], 0\n"
    "s_waitcnt lgkmcnt(5)\n"
    "v_exp_f32 v69, v69\n"
    "v_mfma_f32_16x16x32_bf16 v[28:31], a[196:199], a[100:103], v[28:31]\n"
    "s_waitcnt lgkmcnt(4)\n"
    "v_exp_f32 v70, v70\n"
    "v_mfma_f32_16x16x32_bf16 v[28:31], a[200:203], a[104:107], v[28:31]\n"
    "ds_write_b64 v10, v[134:135] offset:32\n"
    "v_mfma_f32_16x16x32_bf16 v[32:35], a[200:203], a[128:131], 0\n"
    "v_exp_f32 v71, v71\n"
    "v_mfma_f32_16x16x32_bf16 v[32:35], a[196:199], a[124:127], v[32:35]\n"
    "buffer_load_dwordx4 v7, s[36:39], s55 offen offset:1024 nt lds\n"
    "v_mfma_f32_16x16x32_bf16 v[32:35], a[192:195], a[120:123], v[32:35]\n"
    "v_exp_f32 v72, v72\n"
    "v_mfma_f32_16x16x32_bf16 v[36:39], a[192:195], a[144:147], 0\n"
    "v_exp_f32 v73, v73\n"
    "v_mfma_f32_16x16x32_bf16 v[36:39], a[196:199], a[148:151], v[36:39]\n"
    "ds_write_b64 v10, v[160:161] offset:64\n"
    "v_mfma_f32_16x16x32_bf16 v[36:39], a[200:203], a[152:155], v[36:39]\n"
    "v_exp_f32 v74, v74\n"
    "v_mfma_f32_16x16x32_bf16 v[40:43], a[200:203], a[176:179], 0\n"
    "ds_read_b128 a[200:203], v0 offset:30848\n"
    "v_cvt_pk_bf16_f32 v54, v68, v69\n"
    "v_mfma_f32_16x16x32_bf16 v[40:43], a[196:199], a[172:175], v[40:43]\n"
    "ds_read_b128 a[196:199], v1 offset:30720\n"
    "v_cvt_pk_bf16_f32 v55, v70, v71\n"
    "v_mfma_f32_16x16x32_bf16 v[40:43], a[192:195], a[168:171], v[40:43]\n"
    "s_waitcnt lgkmcnt(7)\n"
    "ds_read_b128 a[192:195], v0 offset:30720\n"
    "v_cvt_pk_bf16_f32 v58, v72, v73\n"
    "v_mfma_f32_16x16x32_bf16 v[28:31], a[204:207], a[108:111], v[28:31]\n"
    "s_waitcnt lgkmcnt(7)\n"
    "ds_write_b64 v10, v[162:163] offset:96\n"
    "v_mfma_f32_16x16x32_bf16 v[28:31], a[208:211], a[112:115], v[28:31]\n"
    "s_waitcnt lgkmcnt(6)\n"
    "v_exp_f32 v75, v75\n"
    "v_mfma_f32_16x16x32_bf16 v[28:31], a[212:215], a[116:119], v[28:31]\n"
    "v_cvt_pk_bf16_f32 v59, v74, v75\n"
    "v_accvgpr_read_b32 v96, a24\n"
    "v_mfma_f32_16x16x32_bf16 v[32:35], a[212:215], a[140:143], v[32:35]\n"
    "v_accvgpr_read_b32 v97, a25\n"
    "v_accvgpr_read_b32 v98, a26\n"
    "v_mfma_f32_16x16x32_bf16 v[32:35], a[208:211], a[136:139], v[32:35]\n"
    "buffer_load_dwordx4 v8, s[36:39], s55 offen offset:2048 nt lds\n"
    "v_mfma_f32_16x16x32_bf16 v[32:35], a[204:207], a[132:135], v[32:35]\n"
    "s_add_u32 m0, s54, 61440\n"
    "v_accvgpr_read_b32 v99, a27\n"
    "v_accvgpr_read_b32 v112, a28\n"
    "v_mfma_f32_16x16x32_bf16 v[36:39], a[204:207], a[156:159], v[36:39]\n"
    "v_accvgpr_read_b32 v113, a29\n"
    "v_accvgpr_read_b32 v114, a30\n"
    "v_mfma_f32_16x16x32_bf16 v[36:39], a[208:211], a[160:163], v[36:39]\n"
    "v_accvgpr_read_b32 v115, a31\n"
    "v_fma_f32 v28, v28, s52, v12\n"
    "v_mfma_f32_16x16x32_bf16 v[36:39], a[212:215], a[164:167], v[36:39]\n"
    "v_fma_f32 v29, v29, s52, v12\n"
    "v_fma_f32 v30, v30, s52, v12\n"
    "v_mfma_f32_16x16x32_bf16 v[40:43], a[212:215], a[188:191], v[40:43]\n"
    "ds_read_b64 v[132:133], v11 offset:0\n"
    "v_fma_f32 v31, v31, s52, v12\n"
    "v_mfma_f32_16x16x32_bf16 v[40:43], a[208:211], a[184:187], v[40:43]\n"
    "ds_read_b128 a[208:211], v0 offset:30976\n"
    "v_fma_f32 v32, v32, s52, v13\n"
    "v_mfma_f32_16x16x32_bf16 v[40:43], a[204:207], a[180:183], v[40:43]\n"
    "s_waitcnt lgkmcnt(3)\n"
    "ds_read_b128 a[204:207], v1 offset:30848\n"
    "v_fma_f32 v33, v33, s52, v13\n"
    "v_mfma_f32_16x16x32_bf16 v[76:79], a[192:195], a[96:99], 0\n"
    "ds_read_b64 v[134:135], v20 offset:0\n"
    "v_fma_f32 v34, v34, s52, v13\n"
    "v_mfma_f32_16x16x32_bf16 v[76:79], a[196:199], a[100:103], v[76:79]\n"
    "ds_read_b128 a[212:215], v1 offset:30976\n"
    "v_fma_f32 v35, v35, s52, v13\n"
    "v_mfma_f32_16x16x32_bf16 v[76:79], a[200:203], a[104:107], v[76:79]\n"
    "v_exp_f32 v28, v28\n"
    "v_mfma_f32_16x16x32_bf16 v[80:83], a[200:203], a[128:131], 0\n"
    "v_exp_f32 v29, v29\n"
    "v_mfma_f32_16x16x32_bf16 v[80:83], a[196:199], a[124:127], v[80:83]\n"
    "buffer_load_dwordx4 v6, s[40:43], s56 offen offset:0 nt lds\n"
    "v_mfma_f32_16x16x32_bf16 v[80:83], a[192:195], a[120:123], v[80:83]\n"
    "v_exp_f32 v30, v30\n"
    "v_mfma_f32_16x16x32_bf16 v[60:63], a[192:195], a[144:147], 0\n"
    "v_exp_f32 v31, v31\n"
    "v_mfma_f32_16x16x32_bf16 v[60:63], a[196:199], a[148:151], v[60:63]\n"
    "v_cvt_pk_bf16_f32 v28, v28, v29\n"
    "v_fma_f32 v36, v36, s52, v14\n"
    "v_mfma_f32_16x16x32_bf16 v[60:63], a[200:203], a[152:155], v[60:63]\n"
    "v_cvt_pk_bf16_f32 v29, v30, v31\n"
    "v_fma_f32 v37, v37, s52, v14\n"
    "v_mfma_f32_16x16x32_bf16 v[64:67], a[200:203], a[176:179], 0\n"
    "ds_read_b64_tr_b16 a[200:201], v4 offset:12288\n"
    "v_fma_f32 v40, v40, s52, v15\n"
    "v_mfma_f32_16x16x32_bf16 v[64:67], a[196:199], a[172:175], v[64:67]\n"
    "ds_read_b64_tr_b16 a[196:197], v3 offset:12288\n"
    "v_fma_f32 v41, v41, s52, v15\n"
    "v_mfma_f32_16x16x32_bf16 v[64:67], a[192:195], a[168:171], v[64:67]\n"
    "s_waitcnt lgkmcnt(4)\n"
    "ds_read_b64_tr_b16 a[192:193], v2 offset:12288\n"
    "v_fma_f32 v38, v38, s52, v14\n"
    "v_mfma_f32_16x16x32_bf16 v[76:79], a[204:207], a[108:111], v[76:79]\n"
    "s_waitcnt lgkmcnt(4)\n"
    "ds_read_b64_tr_b16 a[194:195], v2 offset:18432\n"
    "v_fma_f32 v39, v39, s52, v14\n"
    "v_mfma_f32_16x16x32_bf16 v[76:79], a[208:211], a[112:115], v[76:79]\n"
    "s_waitcnt lgkmcnt(4)\n"
    "buffer_store_dwordx4 v[132:135], v9, s[48:51], s72 offen offset:128 sc1\n"
    "v_mfma_f32_16x16x32_bf16 v[76:79], a[212:215], a[116:119], v[76:79]\n"
    "ds_read_b64_tr_b16 a[198:199], v3 offset:18432\n"
    "v_fma_f32 v42, v42, s52, v15\n"
    "v_mfma_f32_16x16x32_bf16 v[80:83], a[212:215], a[140:143], v[80:83]\n"
    "ds_read_b64_tr_b16 a[202:203], v4 offset:18432\n"
    "v_fma_f32 v43, v43, s52, v15\n"
    "v_mfma_f32_16x16x32_bf16 v[80:83], a[208:211], a[136:139], v[80:83]\n"
    "buffer_load_dwordx4 v7, s[40:43], s56 offen offset:1024 nt lds\n"
    "v_mfma_f32_16x16x32_bf16 v[80:83], a[204:207], a[132:135], v[80:83]\n"
    "v_fma_f32 v76, v76, s52, v12\n"
    "v_fma_f32 v77, v77, s52, v12\n"
    "v_mfma_f32_16x16x32_bf16 v[60:63], a[204:207], a[156:159], v[60:63]\n"
    "v_fma_f32 v78, v78, s52, v12\n"
    "v_fma_f32 v79, v79, s52, v12\n"
    "v_mfma_f32_16x16x32_bf16 v[60:63], a[208:211], a[160:163], v[60:63]\n"
    "v_exp_f32 v76, v76\n"
    "v_mfma_f32_16x16x32_bf16 v[60:63], a[212:215], a[164:167], v[60:63]\n"
    "v_exp_f32 v77, v77\n"
    "v_mfma_f32_16x16x32_bf16 v[64:67], a[212:215], a[188:191], v[64:67]\n"
    "ds_read_b64_tr_b16 a[212:213], v3 offset:12416\n"
    "v_cvt_pk_bf16_f32 v30, v76, v77\n"
    "v_mfma_f32_16x16x32_bf16 v[64:67], a[208:211], a[184:187], v[64:67]\n"
    "ds_read_b64_tr_b16 a[208:209], v2 offset:12416\n"
    "v_fma_f32 v80, v80, s52, v13\n"
    "v_mfma_f32_16x16x32_bf16 v[64:67], a[204:207], a[180:183], v[64:67]\n"
    "ds_read_b64_tr_b16 a[204:205], v5 offset:12288\n"
    "v_fma_f32 v81, v81, s52, v13\n"
    "v_smfmac_f32_16x16x64_bf16 a[244:247], a[252:255], v[44:51], v23\n"
    "ds_read_b64_tr_b16 a[206:207], v5 offset:18432\n"
    "v_fma_f32 v82, v82, s52, v13\n"
    "v_smfmac_f32_16x16x64_bf16 a[248:251], a[252:255], v[52:59], v23\n"
    "s_waitcnt lgkmcnt(6)\n"
    "ds_read_b64_tr_b16 a[210:211], v2 offset:18560\n"
    "v_fma_f32 v83, v83, s52, v13\n"
    "v_mfma_f32_16x16x32_bf16 v[104:107], a[192:195], v[56:59], v[104:107]\n"
    "ds_read_b64_tr_b16 a[214:215], v3 offset:18560\n"
    "v_fma_f32 v60, v60, s52, v14\n"
    "v_mfma_f32_16x16x32_bf16 v[88:91], a[192:195], v[52:55], v[88:91]\n"
    "v_exp_f32 v78, v78\n"
    "v_mfma_f32_16x16x32_bf16 v[100:103], a[192:195], v[48:51], v[100:103]\n"
    "buffer_load_dwordx4 v8, s[40:43], s56 offen offset:2048 nt lds\n"
    "v_mfma_f32_16x16x32_bf16 v[84:87], a[192:195], v[44:47], v[84:87]\n"
    "s_waitcnt lgkmcnt(7)\n"
    "ds_read_b64_tr_b16 a[192:193], v4 offset:12416\n"
    "v_fma_f32 v61, v61, s52, v14\n"
    "v_mfma_f32_16x16x32_bf16 v[92:95], a[196:199], v[44:47], v[92:95]\n"
    "s_add_u32 s56, s56, 24576\n"
    "ds_read_b64_tr_b16 a[194:195], v4 offset:18560\n"
    "v_fma_f32 v62, v62, s52, v14\n"
    "v_mfma_f32_16x16x32_bf16 v[108:111], a[196:199], v[48:51], v[108:111]\n"
    "v_exp_f32 v79, v79\n"
    "v_mfma_f32_16x16x32_bf16 v[96:99], a[196:199], v[52:55], v[96:99]\n"
    "v_cvt_pk_bf16_f32 v31, v78, v79\n"
    "v_fma_f32 v63, v63, s52, v14\n"
    "v_mfma_f32_16x16x32_bf16 v[112:115], a[196:199], v[56:59], v[112:115]\n"
    "s_waitcnt lgkmcnt(8)\n"
    "ds_read_b64_tr_b16 a[196:197], v5 offset:12416\n"
    "v_fma_f32 v64, v64, s52, v15\n"
    "v_mfma_f32_16x16x32_bf16 a[44:47], a[200:203], v[56:59], a[44:47]\n"
    "ds_read_b64_tr_b16 a[198:199], v5 offset:18560\n"
    "v_fma_f32 v65, v65, s52, v15\n"
    "v_mfma_f32_16x16x32_bf16 a[40:43], a[200:203], v[52:55], a[40:43]\n"
    "v_exp_f32 v32, v32\n"
    "v_mfma_f32_16x16x32_bf16 a[36:39], a[200:203], v[48:51], a[36:39]\n"
    "v_exp_f32 v33, v33\n"
    "v_mfma_f32_16x16x32_bf16 a[32:35], a[200:203], v[44:47], a[32:35]\n"
    "s_waitcnt lgkmcnt(6)\n"
    "ds_read_b64_tr_b16 a[200:201], v2 offset:12544\n"
    "v_cvt_pk_bf16_f32 v32, v32, v33\n"
    "v_mfma_f32_16x16x32_bf16 a[48:51], a[204:207], v[44:47], a[48:51]\n"
    "ds_read_b64_tr_b16 a[202:203], v2 offset:18688\n"
    "v_fma_f32 v66, v66, s52, v15\n"
    "v_mfma_f32_16x16x32_bf16 a[52:55], a[204:207], v[48:51], a[52:55]\n"
    "v_exp_f32 v34, v34\n"
    "v_mfma_f32_16x16x32_bf16 a[56:59], a[204:207], v[52:55], a[56:59]\n"
    "v_exp_f32 v35, v35\n"
    "v_mfma_f32_16x16x32_bf16 a[60:63], a[204:207], v[56:59], a[60:63]\n"
    "s_waitcnt lgkmcnt(7)\n"
    "ds_read_b64_tr_b16 a[204:205], v3 offset:12544\n"
    "v_cvt_pk_bf16_f32 v33, v34, v35\n"
    "v_mfma_f32_16x16x32_bf16 a[76:79], a[208:211], v[56:59], a[76:79]\n"
    "ds_read_b64_tr_b16 a[206:207], v3 offset:18688\n"
    "v_fma_f32 v67, v67, s52, v15\n"
    "v_mfma_f32_16x16x32_bf16 a[72:75], a[208:211], v[52:55], a[72:75]\n"
    "v_exp_f32 v80, v80\n"
    "v_mfma_f32_16x16x32_bf16 a[68:71], a[208:211], v[48:51], a[68:71]\n"
    "v_exp_f32 v81, v81\n"
    "v_mfma_f32_16x16x32_bf16 a[64:67], a[208:211], v[44:47], a[64:67]\n"
    "s_waitcnt lgkmcnt(8)\n"
    "ds_read_b64_tr_b16 a[208:209], v4 offset:12544\n"
    "v_cvt_pk_bf16_f32 v34, v80, v81\n"
    "v_mfma_f32_16x16x32_bf16 a[80:83], a[212:215], v[44:47], a[80:83]\n"
    "ds_read_b64_tr_b16 a[210:211], v4 offset:18688\n"
    "v_accvgpr_read_b32 v132, a32\n"
    "v_mfma_f32_16x16x32_bf16 a[84:87], a[212:215], v[48:51], a[84:87]\n"
    "ds_read_b64 v[160:161], v11 offset:320\n"
    "v_accvgpr_read_b32 v133, a33\n"
    "v_mfma_f32_16x16x32_bf16 a[88:91], a[212:215], v[52:55], a[88:91]\n"
    "ds_read_b64 v[162:163], v20 offset:320\n"
    "v_accvgpr_read_b32 v134, a34\n"
    "v_mfma_f32_16x16x32_bf16 a[92:95], a[212:215], v[56:59], a[92:95]\n"
    "s_waitcnt lgkmcnt(10)\n"
    "ds_read_b64_tr_b16 a[212:213], v5 offset:12544\n"
    "v_accvgpr_read_b32 v135, a35\n"
    "v_mfma_f32_16x16x32_bf16 v[128:131], a[192:195], v[56:59], v[128:131]\n"
    "v_exp_f32 v82, v82\n"
    "v_mfma_f32_16x16x32_bf16 v[124:127], a[192:195], v[52:55], v[124:127]\n"
    "v_exp_f32 v83, v83\n"
    "v_mfma_f32_16x16x32_bf16 v[120:123], a[192:195], v[48:51], v[120:123]\n"
    "ds_read_b64_tr_b16 a[214:215], v5 offset:18688\n"
    "v_cvt_pk_bf16_f32 v35, v82, v83\n"
    "v_mfma_f32_16x16x32_bf16 v[116:119], a[192:195], v[44:47], v[116:119]\n"
    "s_waitcnt lgkmcnt(10)\n"
    "v_exp_f32 v36, v36\n"
    "v_mfma_f32_16x16x32_bf16 a[216:219], a[196:199], v[44:47], a[216:219]\n"
    "v_exp_f32 v37, v37\n"
    "v_mfma_f32_16x16x32_bf16 a[220:223], a[196:199], v[48:51], a[220:223]\n"
    "v_exp_f32 v40, v40\n"
    "v_mfma_f32_16x16x32_bf16 a[224:227], a[196:199], v[52:55], a[224:227]\n"
    "v_exp_f32 v41, v41\n"
    "v_mfma_f32_16x16x32_bf16 a[228:231], a[196:199], v[56:59], a[228:231]\n"
    "s_waitcnt lgkmcnt(8)\n"
    "v_exp_f32 v38, v38\n"
    "v_mfma_f32_16x16x32_bf16 v[188:191], a[200:203], v[56:59], v[188:191]\n"
    "s_waitcnt vmcnt(24) lgkmcnt(0)\n"
    "v_exp_f32 v39, v39\n"
    "v_mfma_f32_16x16x32_bf16 v[184:187], a[200:203], v[52:55], v[184:187]\n"
    "s_barrier\n"
    "ds_read_b128 a[192:195], v0 offset:36864\n"
    "v_cvt_pk_bf16_f32 v36, v36, v37\n"
    "v_mfma_f32_16x16x32_bf16 v[180:183], a[200:203], v[48:51], v[180:183]\n"
    "s_add_u32 m0, s54, 24576\n"
    "ds_read_b128 a[196:199], v1 offset:36864\n"
    "v_cvt_pk_bf16_f32 v40, v40, v41\n"
    "v_mfma_f32_16x16x32_bf16 v[176:179], a[200:203], v[44:47], v[176:179]\n"
    "ds_read_b128 a[200:203], v0 offset:36992\n"
    "v_cvt_pk_bf16_f32 v37, v38, v39\n"
    "v_mfma_f32_16x16x32_bf16 a[232:235], a[204:207], v[44:47], a[232:235]\n"
    "v_exp_f32 v42, v42\n"
    "v_mfma_f32_16x16x32_bf16 v[148:151], a[204:207], v[48:51], v[148:151]\n"
    "buffer_store_dwordx4 v[160:163], v9, s[48:51], s72 offen offset:896 sc1\n"
    "v_mfma_f32_16x16x32_bf16 v[152:155], a[204:207], v[52:55], v[152:155]\n"
    "v_exp_f32 v43, v43\n"
    "v_mfma_f32_16x16x32_bf16 v[156:159], a[204:207], v[56:59], v[156:159]\n"
    "ds_read_b128 a[204:207], v1 offset:36992\n"
    "v_cvt_pk_bf16_f32 v41, v42, v43\n"
    "v_mfma_f32_16x16x32_bf16 v[220:223], a[208:211], v[56:59], v[220:223]\n"
    "buffer_load_dwordx4 v6, s[36:39], s56 offen offset:0 nt lds\n"
    "v_mfma_f32_16x16x32_bf16 v[216:219], a[208:211], v[52:55], v[216:219]\n"
    "v_exp_f32 v60, v60\n"
    "v_mfma_f32_16x16x32_bf16 v[212:215], a[208:211], v[48:51], v[212:215]\n"
    "v_exp_f32 v61, v61\n"
    "v_mfma_f32_16x16x32_bf16 v[208:211], a[208:211], v[44:47], v[208:211]\n"
    "ds_read_b128 a[208:211], v0 offset:37120\n"
    "v_cvt_pk_bf16_f32 v38, v60, v61\n"
    "v_mfma_f32_16x16x32_bf16 v[240:243], a[212:215], v[44:47], v[240:243]\n"
    "v_exp_f32 v62, v62\n"
    "v_mfma_f32_16x16x32_bf16 v[244:247], a[212:215], v[48:51], v[244:247]\n"
    "v_exp_f32 v63, v63\n"
    "v_mfma_f32_16x16x32_bf16 v[248:251], a[212:215], v[52:55], v[248:251]\n"
    "v_exp_f32 v64, v64\n"
    "v_mfma_f32_16x16x32_bf16 v[252:255], a[212:215], v[56:59], v[252:255]\n"
    "s_waitcnt lgkmcnt(4)\n"
    "ds_write_b64 v10, v[136:137] offset:2560\n"
    "v_mfma_f32_16x16x32_bf16 v[44:47], a[192:195], a[96:99], 0\n"
    "s_waitcnt lgkmcnt(4)\n"
    "ds_read_b128 a[212:215], v1 offset:37120\n"
    "v_cvt_pk_bf16_f32 v39, v62, v63\n"
    "v_mfma_f32_16x16x32_bf16 v[44:47], a[196:199], a[100:103], v[44:47]\n"
    "s_waitcnt lgkmcnt(4)\n"
    "v_exp_f32 v65, v65\n"
    "v_mfma_f32_16x16x32_bf16 v[44:47], a[200:203], a[104:107], v[44:47]\n"
    "v_exp_f32 v66, v66\n"
    "v_mfma_f32_16x16x32_bf16 v[48:51], a[200:203], a[128:131], 0\n"
    "v_exp_f32 v67, v67\n"
    "v_mfma_f32_16x16x32_bf16 v[48:51], a[196:199], a[124:127], v[48:51]\n"
    "buffer_load_dwordx4 v7, s[36:39], s56 offen offset:1024 nt lds\n"
    "v_mfma_f32_16x16x32_bf16 v[48:51], a[192:195], a[120:123], v[48:51]\n"
    "ds_write_b64 v10, v[138:139] offset:2592\n"
    "v_mfma_f32_16x16x32_bf16 v[52:55], a[192:195], a[144:147], 0\n"
    "v_cvt_pk_bf16_f32 v42, v64, v65\n"
    "v_cvt_pk_bf16_f32 v43, v66, v67\n"
    "v_mfma_f32_16x16x32_bf16 v[52:55], a[196:199], a[148:151], v[52:55]\n"
    "v_accvgpr_read_b32 v160, a36\n"
    "v_accvgpr_read_b32 v161, a37\n"
    "v_mfma_f32_16x16x32_bf16 v[52:55], a[200:203], a[152:155], v[52:55]\n"
    "v_accvgpr_read_b32 v162, a38\n"
    "v_accvgpr_read_b32 v163, a39\n"
    "v_mfma_f32_16x16x32_bf16 v[56:59], a[200:203], a[176:179], 0\n"
    "ds_write_b64 v10, v[164:165] offset:2624\n"
    "v_mfma_f32_16x16x32_bf16 v[56:59], a[196:199], a[172:175], v[56:59]\n"
    "s_nop 0\n"
    "ds_read_b128 a[196:199], v1 offset:43008\n"
    "ds_read_b128 a[200:203], v0 offset:43136\n"
    "v_mfma_f32_16x16x32_bf16 v[56:59], a[192:195], a[168:171], v[56:59]\n"
    "s_waitcnt lgkmcnt(7)\n"
    "ds_read_b128 a[192:195], v0 offset:43008\n"
    "v_mfma_f32_16x16x32_bf16 v[44:47], a[204:207], a[108:111], v[44:47]\n"
    "s_waitcnt lgkmcnt(7)\n"
    "v_mfma_f32_16x16x32_bf16 v[44:47], a[208:211], a[112:115], v[44:47]\n"
    "s_waitcnt lgkmcnt(5)\n"
    "v_mfma_f32_16x16x32_bf16 v[44:47], a[212:215], a[116:119], v[44:47]\n"
    "ds_write_b64 v10, v[166:167] offset:2656\n"
    "v_mfma_f32_16x16x32_bf16 v[48:51], a[212:215], a[140:143], v[48:51]\n"
    "v_mfma_f32_16x16x32_bf16 v[48:51], a[208:211], a[136:139], v[48:51]\n"
    "buffer_load_dwordx4 v8, s[36:39], s56 offen offset:2048 nt lds\n"
    "v_mfma_f32_16x16x32_bf16 v[48:51], a[204:207], a[132:135], v[48:51]\n"
    "s_add_u32 m0, s54, 73728\n"
    "v_mfma_f32_16x16x32_bf16 v[52:55], a[204:207], a[156:159], v[52:55]\n"
    "v_mfma_f32_16x16x32_bf16 v[52:55], a[208:211], a[160:163], v[52:55]\n"
    "v_fma_f32 v44, v44, s52, v12\n"
    "v_fma_f32 v45, v45, s52, v12\n"
    "v_mfma_f32_16x16x32_bf16 v[52:55], a[212:215], a[164:167], v[52:55]\n"
    "v_exp_f32 v44, v44\n"
    "v_mfma_f32_16x16x32_bf16 v[56:59], a[212:215], a[188:191], v[56:59]\n"
    "ds_read_b128 a[212:215], v1 offset:43264\n"
    "v_fma_f32 v46, v46, s52, v12\n"
    "v_mfma_f32_16x16x32_bf16 v[56:59], a[208:211], a[184:187], v[56:59]\n"
    "ds_read_b128 a[208:211], v0 offset:43264\n"
    "v_fma_f32 v47, v47, s52, v12\n"
    "v_mfma_f32_16x16x32_bf16 v[56:59], a[204:207], a[180:183], v[56:59]\n"
    "s_waitcnt lgkmcnt(3)\n"
    "ds_read_b128 a[204:207], v1 offset:43136\n"
    "v_fma_f32 v48, v48, s52, v13\n"
    "v_mfma_f32_16x16x32_bf16 v[68:71], a[192:195], a[96:99], 0\n"
    "ds_read_b64 v[136:137], v11 offset:2560\n"
    "v_fma_f32 v49, v49, s52, v13\n"
    "v_mfma_f32_16x16x32_bf16 v[68:71], a[196:199], a[100:103], v[68:71]\n"
    "ds_read_b64 v[138:139], v20 offset:2560\n"
    "v_fma_f32 v50, v50, s52, v13\n"
    "v_mfma_f32_16x16x32_bf16 v[68:71], a[200:203], a[104:107], v[68:71]\n"
    "v_exp_f32 v45, v45\n"
    "v_mfma_f32_16x16x32_bf16 v[72:75], a[200:203], a[128:131], 0\n"
    "v_exp_f32 v46, v46\n"
    "v_mfma_f32_16x16x32_bf16 v[72:75], a[196:199], a[124:127], v[72:75]\n"
    "buffer_load_dwordx4 v6, s[40:43], s55 offen offset:0 nt lds\n"
    "v_mfma_f32_16x16x32_bf16 v[72:75], a[192:195], a[120:123], v[72:75]\n"
    "v_exp_f32 v47, v47\n"
    "v_mfma_f32_16x16x32_bf16 v[76:79], a[192:195], a[144:147], 0\n"
    "v_cvt_pk_bf16_f32 v44, v44, v45\n"
    "v_fma_f32 v51, v51, s52, v13\n"
    "v_mfma_f32_16x16x32_bf16 v[76:79], a[196:199], a[148:151], v[76:79]\n"
    "v_cvt_pk_bf16_f32 v45, v46, v47\n"
    "v_fma_f32 v52, v52, s52, v14\n"
    "v_mfma_f32_16x16x32_bf16 v[76:79], a[200:203], a[152:155], v[76:79]\n"
    "v_exp_f32 v48, v48\n"
    "v_mfma_f32_16x16x32_bf16 v[80:83], a[200:203], a[176:179], 0\n"
    "ds_read_b64_tr_b16 a[200:201], v4 offset:24576\n"
    "v_fma_f32 v53, v53, s52, v14\n"
    "v_mfma_f32_16x16x32_bf16 v[80:83], a[196:199], a[172:175], v[80:83]\n"
    "ds_read_b64_tr_b16 a[196:197], v3 offset:24576\n"
    "v_fma_f32 v56, v56, s52, v15\n"
    "v_mfma_f32_16x16x32_bf16 v[80:83], a[192:195], a[168:171], v[80:83]\n"
    "s_waitcnt lgkmcnt(4)\n"
    "ds_read_b64_tr_b16 a[192:193], v2 offset:24576\n"
    "v_fma_f32 v57, v57, s52, v15\n"
    "v_mfma_f32_16x16x32_bf16 v[68:71], a[204:207], a[108:111], v[68:71]\n"
    "ds_read_b64_tr_b16 a[194:195], v2 offset:30720\n"
    "v_fma_f32 v54, v54, s52, v14\n"
    "v_mfma_f32_16x16x32_bf16 v[68:71], a[208:211], a[112:115], v[68:71]\n"
    "ds_read_b64_tr_b16 a[198:199], v3 offset:30720\n"
    "v_fma_f32 v55, v55, s52, v14\n"
    "v_mfma_f32_16x16x32_bf16 v[68:71], a[212:215], a[116:119], v[68:71]\n"
    "s_waitcnt lgkmcnt(5)\n"
    "ds_read_b64_tr_b16 a[202:203], v4 offset:30720\n"
    "v_fma_f32 v58, v58, s52, v15\n"
    "v_mfma_f32_16x16x32_bf16 v[72:75], a[212:215], a[140:143], v[72:75]\n"
    "buffer_store_dwordx4 v[136:139], v9, s[48:51], s73 offen offset:128 sc1\n"
    "v_mfma_f32_16x16x32_bf16 v[72:75], a[208:211], a[136:139], v[72:75]\n"
    "buffer_load_dwordx4 v7, s[40:43], s55 offen offset:1024 nt lds\n"
    "v_mfma_f32_16x16x32_bf16 v[72:75], a[204:207], a[132:135], v[72:75]\n"
    "v_fma_f32 v68, v68, s52, v12\n"
    "v_fma_f32 v69, v69, s52, v12\n"
    "v_mfma_f32_16x16x32_bf16 v[76:79], a[204:207], a[156:159], v[76:79]\n"
    "v_fma_f32 v70, v70, s52, v12\n"
    "v_fma_f32 v71, v71, s52, v12\n"
    "v_mfma_f32_16x16x32_bf16 v[76:79], a[208:211], a[160:163], v[76:79]\n"
    "v_exp_f32 v68, v68\n"
    "v_mfma_f32_16x16x32_bf16 v[76:79], a[212:215], a[164:167], v[76:79]\n"
    "v_exp_f32 v69, v69\n"
    "v_mfma_f32_16x16x32_bf16 v[80:83], a[212:215], a[188:191], v[80:83]\n"
    "ds_read_b64_tr_b16 a[212:213], v3 offset:24704\n"
    "v_cvt_pk_bf16_f32 v46, v68, v69\n"
    "v_mfma_f32_16x16x32_bf16 v[80:83], a[208:211], a[184:187], v[80:83]\n"
    "ds_read_b64_tr_b16 a[208:209], v2 offset:24704\n"
    "v_fma_f32 v72, v72, s52, v13\n"
    "v_mfma_f32_16x16x32_bf16 v[80:83], a[204:207], a[180:183], v[80:83]\n"
    "ds_read_b64_tr_b16 a[204:205], v5 offset:24576\n"
    "v_fma_f32 v73, v73, s52, v13\n"
    "v_smfmac_f32_16x16x64_bf16 a[244:247], a[252:255], v[28:35], v23\n"
    "ds_read_b64_tr_b16 a[206:207], v5 offset:30720\n"
    "v_fma_f32 v74, v74, s52, v13\n"
    "v_smfmac_f32_16x16x64_bf16 a[248:251], a[252:255], v[36:43], v23\n"
    "s_waitcnt lgkmcnt(6)\n"
    "ds_read_b64_tr_b16 a[210:211], v2 offset:30848\n"
    "v_fma_f32 v75, v75, s52, v13\n"
    "v_mfma_f32_16x16x32_bf16 v[104:107], a[192:195], v[40:43], v[104:107]\n"
    "ds_read_b64_tr_b16 a[214:215], v3 offset:30848\n"
    "v_fma_f32 v59, v59, s52, v15\n"
    "v_mfma_f32_16x16x32_bf16 v[88:91], a[192:195], v[36:39], v[88:91]\n"
    "v_exp_f32 v70, v70\n"
    "v_mfma_f32_16x16x32_bf16 v[100:103], a[192:195], v[32:35], v[100:103]\n"
    "buffer_load_dwordx4 v8, s[40:43], s55 offen offset:2048 nt lds\n"
    "v_mfma_f32_16x16x32_bf16 v[84:87], a[192:195], v[28:31], v[84:87]\n"
    "s_waitcnt lgkmcnt(7)\n"
    "ds_read_b64_tr_b16 a[192:193], v4 offset:24704\n"
    "v_fma_f32 v76, v76, s52, v14\n"
    "v_mfma_f32_16x16x32_bf16 v[92:95], a[196:199], v[28:31], v[92:95]\n"
    "s_add_u32 s55, s55, 24576\n"
    "ds_read_b64_tr_b16 a[194:195], v4 offset:30848\n"
    "v_fma_f32 v77, v77, s52, v14\n"
    "v_mfma_f32_16x16x32_bf16 v[108:111], a[196:199], v[32:35], v[108:111]\n"
    "v_exp_f32 v71, v71\n"
    "v_mfma_f32_16x16x32_bf16 v[96:99], a[196:199], v[36:39], v[96:99]\n"
    "v_cvt_pk_bf16_f32 v47, v70, v71\n"
    "v_fma_f32 v78, v78, s52, v14\n"
    "v_mfma_f32_16x16x32_bf16 v[112:115], a[196:199], v[40:43], v[112:115]\n"
    "s_waitcnt lgkmcnt(8)\n"
    "ds_read_b64_tr_b16 a[196:197], v5 offset:24704\n"
    "v_fma_f32 v79, v79, s52, v14\n"
    "v_mfma_f32_16x16x32_bf16 a[44:47], a[200:203], v[40:43], a[44:47]\n"
    "ds_read_b64_tr_b16 a[198:199], v5 offset:30848\n"
    "v_fma_f32 v80, v80, s52, v15\n"
    "v_mfma_f32_16x16x32_bf16 a[40:43], a[200:203], v[36:39], a[40:43]\n"
    "v_exp_f32 v49, v49\n"
    "v_mfma_f32_16x16x32_bf16 v[160:163], a[200:203], v[32:35], v[160:163]\n"
    "v_exp_f32 v50, v50\n"
    "v_mfma_f32_16x16x32_bf16 v[132:135], a[200:203], v[28:31], v[132:135]\n"
    "s_waitcnt lgkmcnt(6)\n"
    "ds_read_b64_tr_b16 a[200:201], v2 offset:24832\n"
    "v_cvt_pk_bf16_f32 v48, v48, v49\n"
    "v_mfma_f32_16x16x32_bf16 a[48:51], a[204:207], v[28:31], a[48:51]\n"
    "ds_read_b64_tr_b16 a[202:203], v2 offset:30976\n"
    "v_fma_f32 v81, v81, s52, v15\n"
    "v_mfma_f32_16x16x32_bf16 a[52:55], a[204:207], v[32:35], a[52:55]\n"
    "v_exp_f32 v51, v51\n"
    "v_mfma_f32_16x16x32_bf16 a[56:59], a[204:207], v[36:39], a[56:59]\n"
    "v_exp_f32 v72, v72\n"
    "v_mfma_f32_16x16x32_bf16 a[60:63], a[204:207], v[40:43], a[60:63]\n"
    "s_waitcnt lgkmcnt(7)\n"
    "ds_read_b64_tr_b16 a[204:205], v3 offset:24832\n"
    "v_cvt_pk_bf16_f32 v49, v50, v51\n"
    "v_mfma_f32_16x16x32_bf16 a[76:79], a[208:211], v[40:43], a[76:79]\n"
    "ds_read_b64_tr_b16 a[206:207], v3 offset:30976\n"
    "v_fma_f32 v82, v82, s52, v15\n"
    "v_mfma_f32_16x16x32_bf16 a[72:75], a[208:211], v[36:39], a[72:75]\n"
    "v_exp_f32 v73, v73\n"
    "v_mfma_f32_16x16x32_bf16 a[68:71], a[208:211], v[32:35], a[68:71]\n"
    "v_exp_f32 v74, v74\n"
    "v_mfma_f32_16x16x32_bf16 a[64:67], a[208:211], v[28:31], a[64:67]\n"
    "s_waitcnt lgkmcnt(8)\n"
    "ds_read_b64_tr_b16 a[208:209], v4 offset:24832\n"
    "v_cvt_pk_bf16_f32 v50, v72, v73\n"
    "v_mfma_f32_16x16x32_bf16 a[80:83], a[212:215], v[28:31], a[80:83]\n"
    "ds_read_b64_tr_b16 a[210:211], v4 offset:30976\n"
    "v_fma_f32 v83, v83, s52, v15\n"
    "v_mfma_f32_16x16x32_bf16 a[84:87], a[212:215], v[32:35], a[84:87]\n"
    "v_exp_f32 v75, v75\n"
    "v_mfma_f32_16x16x32_bf16 a[88:91], a[212:215], v[36:39], a[88:91]\n"
    "v_cvt_pk_bf16_f32 v51, v74, v75\n"
    "v_accvgpr_read_b32 v136, a40\n"
    "v_mfma_f32_16x16x32_bf16 a[92:95], a[212:215], v[40:43], a[92:95]\n"
    "s_waitcnt lgkmcnt(8)\n"
    "ds_read_b64 v[164:165], v11 offset:2880\n"
    "v_accvgpr_read_b32 v137, a41\n"
    "v_mfma_f32_16x16x32_bf16 v[128:131], a[192:195], v[40:43], v[128:131]\n"
    "ds_read_b64 v[166:167], v20 offset:2880\n"
    "v_accvgpr_read_b32 v138, a42\n"
    "v_mfma_f32_16x16x32_bf16 v[124:127], a[192:195], v[36:39], v[124:127]\n"
    "ds_read_b64_tr_b16 a[212:213], v5 offset:24832\n"
    "v_accvgpr_read_b32 v139, a43\n"
    "v_mfma_f32_16x16x32_bf16 v[120:123], a[192:195], v[32:35], v[120:123]\n"
    "v_exp_f32 v52, v52\n"
    "v_mfma_f32_16x16x32_bf16 v[116:119], a[192:195], v[28:31], v[116:119]\n"
    "s_waitcnt lgkmcnt(9)\n"
    "v_exp_f32 v53, v53\n"
    "v_mfma_f32_16x16x32_bf16 a[216:219], a[196:199], v[28:31], a[216:219]\n"
    "ds_read_b64_tr_b16 a[214:215], v5 offset:30976\n"
    "v_cvt_pk_bf16_f32 v52, v52, v53\n"
    "v_mfma_f32_16x16x32_bf16 a[220:223], a[196:199], v[32:35], a[220:223]\n"
    "v_exp_f32 v56, v56\n"
    "v_mfma_f32_16x16x32_bf16 a[224:227], a[196:199], v[36:39], a[224:227]\n"
    "v_exp_f32 v57, v57\n"
    "v_mfma_f32_16x16x32_bf16 a[228:231], a[196:199], v[40:43], a[228:231]\n"
    "s_waitcnt lgkmcnt(8)\n"
    "v_exp_f32 v54, v54\n"
    "v_mfma_f32_16x16x32_bf16 v[188:191], a[200:203], v[40:43], v[188:191]\n"
    "v_exp_f32 v55, v55\n"
    "v_mfma_f32_16x16x32_bf16 v[184:187], a[200:203], v[36:39], v[184:187]\n"
    "v_exp_f32 v58, v58\n"
    "v_mfma_f32_16x16x32_bf16 v[180:183], a[200:203], v[32:35], v[180:183]\n"
    "s_waitcnt vmcnt(24) lgkmcnt(0)\n"
    "v_exp_f32 v59, v59\n"
    "v_mfma_f32_16x16x32_bf16 v[176:179], a[200:203], v[28:31], v[176:179]\n"
    "s_barrier\n"
    "ds_read_b128 a[192:195], v0 offset:49152\n"
    "v_cvt_pk_bf16_f32 v56, v56, v57\n"
    "v_mfma_f32_16x16x32_bf16 a[232:235], a[204:207], v[28:31], a[232:235]\n"
    "s_add_u32 m0, s54, 36864\n"
    "ds_read_b128 a[196:199], v1 offset:49152\n"
    "v_cvt_pk_bf16_f32 v53, v54, v55\n"
    "v_mfma_f32_16x16x32_bf16 v[148:151], a[204:207], v[32:35], v[148:151]\n"
    "ds_read_b128 a[200:203], v0 offset:49280\n"
    "v_cvt_pk_bf16_f32 v57, v58, v59\n"
    "v_mfma_f32_16x16x32_bf16 v[152:155], a[204:207], v[36:39], v[152:155]\n"
    "v_exp_f32 v76, v76\n"
    "v_mfma_f32_16x16x32_bf16 v[156:159], a[204:207], v[40:43], v[156:159]\n"
    "buffer_store_dwordx4 v[164:167], v9, s[48:51], s73 offen offset:896 sc1\n"
    "v_mfma_f32_16x16x32_bf16 v[220:223], a[208:211], v[40:43], v[220:223]\n"
    "buffer_load_dwordx4 v6, s[36:39], s55 offen offset:0 nt lds\n"
    "v_mfma_f32_16x16x32_bf16 v[216:219], a[208:211], v[36:39], v[216:219]\n"
    "ds_read_b128 a[204:207], v1 offset:49280\n"
    "v_accvgpr_read_b32 v164, a44\n"
    "v_mfma_f32_16x16x32_bf16 v[212:215], a[208:211], v[32:35], v[212:215]\n"
    "v_exp_f32 v77, v77\n"
    "v_mfma_f32_16x16x32_bf16 v[208:211], a[208:211], v[28:31], v[208:211]\n"
    "ds_read_b128 a[208:211], v0 offset:49408\n"
    "v_cvt_pk_bf16_f32 v54, v76, v77\n"
    "v_mfma_f32_16x16x32_bf16 v[240:243], a[212:215], v[28:31], v[240:243]\n"
    "v_exp_f32 v78, v78\n"
    "v_mfma_f32_16x16x32_bf16 v[244:247], a[212:215], v[32:35], v[244:247]\n"
    "v_exp_f32 v79, v79\n"
    "v_mfma_f32_16x16x32_bf16 v[248:251], a[212:215], v[36:39], v[248:251]\n"
    "v_exp_f32 v80, v80\n"
    "v_mfma_f32_16x16x32_bf16 v[252:255], a[212:215], v[40:43], v[252:255]\n"
    "s_waitcnt lgkmcnt(4)\n"
    "ds_read_b128 a[212:215], v1 offset:49408\n"
    "v_cvt_pk_bf16_f32 v55, v78, v79\n"
    "v_mfma_f32_16x16x32_bf16 v[28:31], a[192:195], a[96:99], 0\n"
    "s_waitcnt lgkmcnt(4)\n"
    "v_exp_f32 v81, v81\n"
    "v_mfma_f32_16x16x32_bf16 v[28:31], a[196:199], a[100:103], v[28:31]\n"
    "s_waitcnt lgkmcnt(3)\n"
    "ds_write_b64 v10, v[140:141] offset:5120\n"
    "v_mfma_f32_16x16x32_bf16 v[28:31], a[200:203], a[104:107], v[28:31]\n"
    "v_exp_f32 v82, v82\n"
    "v_mfma_f32_16x16x32_bf16 v[32:35], a[200:203], a[128:131], 0\n"
    "v_exp_f32 v83, v83\n"
    "v_mfma_f32_16x16x32_bf16 v[32:35], a[196:199], a[124:127], v[32:35]\n"
    "buffer_load_dwordx4 v7, s[36:39], s55 offen offset:1024 nt lds\n"
    "v_mfma_f32_16x16x32_bf16 v[32:35], a[192:195], a[120:123], v[32:35]\n"
    "v_cvt_pk_bf16_f32 v58, v80, v81\n"
    "v_cvt_pk_bf16_f32 v59, v82, v83\n"
    "v_mfma_f32_16x16x32_bf16 v[36:39], a[192:195], a[144:147], 0\n"
    "ds_write_b64 v10, v[142:143] offset:5152\n"
    "v_mfma_f32_16x16x32_bf16 v[36:39], a[196:199], a[148:151], v[36:39]\n"
    "v_accvgpr_read_b32 v165, a45\n"
    "v_accvgpr_read_b32 v166, a46\n"
    "v_mfma_f32_16x16x32_bf16 v[36:39], a[200:203], a[152:155], v[36:39]\n"
    "v_accvgpr_read_b32 v167, a47\n"
    "v_mfma_f32_16x16x32_bf16 v[40:43], a[200:203], a[176:179], 0\n"
    "ds_read_b128 a[200:203], v0 offset:55424\n"
    "v_mfma_f32_16x16x32_bf16 v[40:43], a[196:199], a[172:175], v[40:43]\n"
    "ds_read_b128 a[196:199], v1 offset:55296\n"
    "v_mfma_f32_16x16x32_bf16 v[40:43], a[192:195], a[168:171], v[40:43]\n"
    "s_waitcnt lgkmcnt(6)\n"
    "ds_write_b64 v10, v[168:169] offset:5184\n"
    "v_mfma_f32_16x16x32_bf16 v[28:31], a[204:207], a[108:111], v[28:31]\n"
    "s_waitcnt lgkmcnt(6)\n"
    "ds_read_b128 a[192:195], v0 offset:55296\n"
    "v_mfma_f32_16x16x32_bf16 v[28:31], a[208:211], a[112:115], v[28:31]\n"
    "s_waitcnt lgkmcnt(6)\n"
    "v_mfma_f32_16x16x32_bf16 v[28:31], a[212:215], a[116:119], v[28:31]\n"
    "v_mfma_f32_16x16x32_bf16 v[32:35], a[212:215], a[140:143], v[32:35]\n"
    "v_mfma_f32_16x16x32_bf16 v[32:35], a[208:211], a[136:139], v[32:35]\n"
    "buffer_load_dwordx4 v8, s[36:39], s55 offen offset:2048 nt lds\n"
    "v_mfma_f32_16x16x32_bf16 v[32:35], a[204:207], a[132:135], v[32:35]\n"
    "s_add_u32 m0, s54, 86016\n"
    "ds_write_b64 v10, v[170:171] offset:5216\n"
    "v_mfma_f32_16x16x32_bf16 v[36:39], a[204:207], a[156:159], v[36:39]\n"
    "v_mfma_f32_16x16x32_bf16 v[36:39], a[208:211], a[160:163], v[36:39]\n"
    "v_fma_f32 v28, v28, s52, v12\n"
    "v_fma_f32 v29, v29, s52, v12\n"
    "v_mfma_f32_16x16x32_bf16 v[36:39], a[212:215], a[164:167], v[36:39]\n"
    "v_exp_f32 v28, v28\n"
    "v_mfma_f32_16x16x32_bf16 v[40:43], a[212:215], a[188:191], v[40:43]\n"
    "ds_read_b128 a[212:215], v1 offset:55552\n"
    "v_fma_f32 v30, v30, s52, v12\n"
    "v_mfma_f32_16x16x32_bf16 v[40:43], a[208:211], a[184:187], v[40:43]\n"
    "ds_read_b128 a[208:211], v0 offset:55552\n"
    "v_fma_f32 v31, v31, s52, v12\n"
    "v_mfma_f32_16x16x32_bf16 v[40:43], a[204:207], a[180:183], v[40:43]\n"
    "s_waitcnt lgkmcnt(3)\n"
    "ds_read_b128 a[204:207], v1 offset:55424\n"
    "v_fma_f32 v32, v32, s52, v13\n"
    "v_mfma_f32_16x16x32_bf16 v[60:63], a[192:195], a[96:99], 0\n"
    "v_exp_f32 v29, v29\n"
    "v_mfma_f32_16x16x32_bf16 v[60:63], a[196:199], a[100:103], v[60:63]\n"
    "ds_read_b64 v[140:141], v11 offset:5120\n"
    "v_cvt_pk_bf16_f32 v28, v28, v29\n"
    "v_mfma_f32_16x16x32_bf16 v[60:63], a[200:203], a[104:107], v[60:63]\n"
    "ds_read_b64 v[142:143], v20 offset:5120\n"
    "v_fma_f32 v33, v33, s52, v13\n"
    "v_mfma_f32_16x16x32_bf16 v[64:67], a[200:203], a[128:131], 0\n"
    "v_exp_f32 v30, v30\n"
    "v_mfma_f32_16x16x32_bf16 v[64:67], a[196:199], a[124:127], v[64:67]\n"
    "buffer_load_dwordx4 v6, s[40:43], s56 offen offset:0 nt lds\n"
    "v_mfma_f32_16x16x32_bf16 v[64:67], a[192:195], a[120:123], v[64:67]\n"
    "v_exp_f32 v31, v31\n"
    "v_mfma_f32_16x16x32_bf16 v[68:71], a[192:195], a[144:147], 0\n"
    "v_cvt_pk_bf16_f32 v29, v30, v31\n"
    "v_fma_f32 v34, v34, s52, v13\n"
    "v_mfma_f32_16x16x32_bf16 v[68:71], a[196:199], a[148:151], v[68:71]\n"
    "v_exp_f32 v32, v32\n"
    "v_mfma_f32_16x16x32_bf16 v[68:71], a[200:203], a[152:155], v[68:71]\n"
    "v_exp_f32 v33, v33\n"
    "v_mfma_f32_16x16x32_bf16 v[72:75], a[200:203], a[176:179], 0\n"
    "ds_read_b64_tr_b16 a[200:201], v4 offset:36864\n"
    "v_fma_f32 v35, v35, s52, v13\n"
    "v_mfma_f32_16x16x32_bf16 v[72:75], a[196:199], a[172:175], v[72:75]\n"
    "ds_read_b64_tr_b16 a[196:197], v3 offset:36864\n"
    "v_cvt_pk_bf16_f32 v32, v32, v33\n"
    "v_mfma_f32_16x16x32_bf16 v[72:75], a[192:195], a[168:171], v[72:75]\n"
    "s_waitcnt lgkmcnt(4)\n"
    "ds_read_b64_tr_b16 a[192:193], v2 offset:36864\n"
    "v_fma_f32 v36, v36, s52, v14\n"
    "v_mfma_f32_16x16x32_bf16 v[60:63], a[204:207], a[108:111], v[60:63]\n"
    "ds_read_b64_tr_b16 a[194:195], v2 offset:43008\n"
    "v_fma_f32 v37, v37, s52, v14\n"
    "v_mfma_f32_16x16x32_bf16 v[60:63], a[208:211], a[112:115], v[60:63]\n"
    "ds_read_b64_tr_b16 a[198:199], v3 offset:43008\n"
    "v_fma_f32 v40, v40, s52, v15\n"
    "v_mfma_f32_16x16x32_bf16 v[60:63], a[212:215], a[116:119], v[60:63]\n"
    "ds_read_b64_tr_b16 a[202:203], v4 offset:43008\n"
    "v_fma_f32 v41, v41, s52, v15\n"
    "v_mfma_f32_16x16x32_bf16 v[64:67], a[212:215], a[140:143], v[64:67]\n"
    "v_exp_f32 v34, v34\n"
    "v_mfma_f32_16x16x32_bf16 v[64:67], a[208:211], a[136:139], v[64:67]\n"
    "s_waitcnt lgkmcnt(6)\n"
    "buffer_load_dwordx4 v7, s[40:43], s56 offen offset:1024 nt lds\n"
    "v_mfma_f32_16x16x32_bf16 v[64:67], a[204:207], a[132:135], v[64:67]\n"
    "buffer_store_dwordx4 v[140:143], v9, s[48:51], s74 offen offset:128 sc1\n"
    "v_mfma_f32_16x16x32_bf16 v[68:71], a[204:207], a[156:159], v[68:71]\n"
    "v_fma_f32 v60, v60, s52, v12\n"
    "v_fma_f32 v61, v61, s52, v12\n"
    "v_mfma_f32_16x16x32_bf16 v[68:71], a[208:211], a[160:163], v[68:71]\n"
    "v_fma_f32 v62, v62, s52, v12\n"
    "v_fma_f32 v63, v63, s52, v12\n"
    "v_mfma_f32_16x16x32_bf16 v[68:71], a[212:215], a[164:167], v[68:71]\n"
    "v_exp_f32 v60, v60\n"
    "v_mfma_f32_16x16x32_bf16 v[72:75], a[212:215], a[188:191], v[72:75]\n"
    "ds_read_b64_tr_b16 a[212:213], v3 offset:36992\n"
    "v_fma_f32 v64, v64, s52, v13\n"
    "v_mfma_f32_16x16x32_bf16 v[72:75], a[208:211], a[184:187], v[72:75]\n"
    "ds_read_b64_tr_b16 a[208:209], v2 offset:36992\n"
    "v_fma_f32 v65, v65, s52, v13\n"
    "v_mfma_f32_16x16x32_bf16 v[72:75], a[204:207], a[180:183], v[72:75]\n"
    "ds_read_b64_tr_b16 a[204:205], v5 offset:36864\n"
    "v_fma_f32 v66, v66, s52, v13\n"
    "v_smfmac_f32_16x16x64_bf16 a[244:247], a[252:255], v[44:51], v23\n"
    "ds_read_b64_tr_b16 a[206:207], v5 offset:43008\n"
    "v_fma_f32 v67, v67, s52, v13\n"
    "v_smfmac_f32_16x16x64_bf16 a[248:251], a[252:255], v[52:59], v23\n"
    "s_waitcnt lgkmcnt(6)\n"
    "ds_read_b64_tr_b16 a[210:211], v2 offset:43136\n"
    "v_fma_f32 v38, v38, s52, v14\n"
    "v_mfma_f32_16x16x32_bf16 v[104:107], a[192:195], v[56:59], v[104:107]\n"
    "ds_read_b64_tr_b16 a[214:215], v3 offset:43136\n"
    "v_fma_f32 v39, v39, s52, v14\n"
    "v_mfma_f32_16x16x32_bf16 v[88:91], a[192:195], v[52:55], v[88:91]\n"
    "v_exp_f32 v61, v61\n"
    "v_mfma_f32_16x16x32_bf16 v[100:103], a[192:195], v[48:51], v[100:103]\n"
    "buffer_load_dwordx4 v8, s[40:43], s56 offen offset:2048 nt lds\n"
    "v_mfma_f32_16x16x32_bf16 v[84:87], a[192:195], v[44:47], v[84:87]\n"
    "s_waitcnt lgkmcnt(7)\n"
    "ds_read_b64_tr_b16 a[192:193], v4 offset:36992\n"
    "v_cvt_pk_bf16_f32 v30, v60, v61\n"
    "v_mfma_f32_16x16x32_bf16 v[92:95], a[196:199], v[44:47], v[92:95]\n"
    "s_add_u32 s56, s56, 24576\n"
    "ds_read_b64_tr_b16 a[194:195], v4 offset:43136\n"
    "v_fma_f32 v42, v42, s52, v15\n"
    "v_mfma_f32_16x16x32_bf16 v[108:111], a[196:199], v[48:51], v[108:111]\n"
    "v_exp_f32 v62, v62\n"
    "v_mfma_f32_16x16x32_bf16 v[96:99], a[196:199], v[52:55], v[96:99]\n"
    "v_exp_f32 v63, v63\n"
    "v_mfma_f32_16x16x32_bf16 v[112:115], a[196:199], v[56:59], v[112:115]\n"
    "s_waitcnt lgkmcnt(8)\n"
    "ds_read_b64_tr_b16 a[196:197], v5 offset:36992\n"
    "v_cvt_pk_bf16_f32 v31, v62, v63\n"
    "v_mfma_f32_16x16x32_bf16 v[164:167], a[200:203], v[56:59], v[164:167]\n"
    "ds_read_b64_tr_b16 a[198:199], v5 offset:43136\n"
    "v_fma_f32 v43, v43, s52, v15\n"
    "v_mfma_f32_16x16x32_bf16 v[136:139], a[200:203], v[52:55], v[136:139]\n"
    "v_exp_f32 v35, v35\n"
    "v_mfma_f32_16x16x32_bf16 v[160:163], a[200:203], v[48:51], v[160:163]\n"
    "v_exp_f32 v64, v64\n"
    "v_mfma_f32_16x16x32_bf16 v[132:135], a[200:203], v[44:47], v[132:135]\n"
    "s_waitcnt lgkmcnt(6)\n"
    "ds_read_b64_tr_b16 a[200:201], v2 offset:37120\n"
    "v_cvt_pk_bf16_f32 v33, v34, v35\n"
    "v_mfma_f32_16x16x32_bf16 a[48:51], a[204:207], v[44:47], a[48:51]\n"
    "ds_read_b64_tr_b16 a[202:203], v2 offset:43264\n"
    "v_fma_f32 v68, v68, s52, v14\n"
    "v_mfma_f32_16x16x32_bf16 a[52:55], a[204:207], v[48:51], a[52:55]\n"
    "v_exp_f32 v65, v65\n"
    "v_mfma_f32_16x16x32_bf16 a[56:59], a[204:207], v[52:55], a[56:59]\n"
    "v_exp_f32 v66, v66\n"
    "v_mfma_f32_16x16x32_bf16 a[60:63], a[204:207], v[56:59], a[60:63]\n"
    "s_waitcnt lgkmcnt(7)\n"
    "ds_read_b64_tr_b16 a[204:205], v3 offset:37120\n"
    "v_cvt_pk_bf16_f32 v34, v64, v65\n"
    "v_mfma_f32_16x16x32_bf16 a[76:79], a[208:211], v[56:59], a[76:79]\n"
    "ds_read_b64_tr_b16 a[206:207], v3 offset:43264\n"
    "v_fma_f32 v69, v69, s52, v14\n"
    "v_mfma_f32_16x16x32_bf16 a[72:75], a[208:211], v[52:55], a[72:75]\n"
    "v_exp_f32 v67, v67\n"
    "v_mfma_f32_16x16x32_bf16 a[68:71], a[208:211], v[48:51], a[68:71]\n"
    "v_cvt_pk_bf16_f32 v35, v66, v67\n"
    "v_fma_f32 v70, v70, s52, v14\n"
    "v_mfma_f32_16x16x32_bf16 a[64:67], a[208:211], v[44:47], a[64:67]\n"
    "s_waitcnt lgkmcnt(8)\n"
    "ds_read_b64_tr_b16 a[208:209], v4 offset:37120\n"
    "v_fma_f32 v71, v71, s52, v14\n"
    "v_mfma_f32_16x16x32_bf16 a[80:83], a[212:215], v[44:47], a[80:83]\n"
    "ds_read_b64_tr_b16 a[210:211], v4 offset:43264\n"
    "v_fma_f32 v72, v72, s52, v15\n"
    "v_mfma_f32_16x16x32_bf16 a[84:87], a[212:215], v[48:51], a[84:87]\n"
    "v_exp_f32 v36, v36\n"
    "v_mfma_f32_16x16x32_bf16 a[88:91], a[212:215], v[52:55], a[88:91]\n"
    "v_exp_f32 v37, v37\n"
    "v_mfma_f32_16x16x32_bf16 a[92:95], a[212:215], v[56:59], a[92:95]\n"
    "s_waitcnt lgkmcnt(8)\n"
    "ds_read_b64_tr_b16 a[212:213], v5 offset:37120\n"
    "v_fma_f32 v73, v73, s52, v15\n"
    "v_mfma_f32_16x16x32_bf16 v[128:131], a[192:195], v[56:59], v[128:131]\n"
    "ds_read_b64_tr_b16 a[214:215], v5 offset:43264\n"
    "v_fma_f32 v74, v74, s52, v15\n"
    "v_mfma_f32_16x16x32_bf16 v[124:127], a[192:195], v[52:55], v[124:127]\n"
    "ds_read_b64 v[168:169], v11 offset:5440\n"
    "v_fma_f32 v75, v75, s52, v15\n"
    "v_mfma_f32_16x16x32_bf16 v[120:123], a[192:195], v[48:51], v[120:123]\n"
    "ds_read_b64 v[170:171], v20 offset:5440\n"
    "v_cvt_pk_bf16_f32 v36, v36, v37\n"
    "v_mfma_f32_16x16x32_bf16 v[116:119], a[192:195], v[44:47], v[116:119]\n"
    "s_waitcnt lgkmcnt(10)\n"
    "v_exp_f32 v40, v40\n"
    "v_mfma_f32_16x16x32_bf16 a[216:219], a[196:199], v[44:47], a[216:219]\n"
    "v_exp_f32 v41, v41\n"
    "v_mfma_f32_16x16x32_bf16 a[220:223], a[196:199], v[48:51], a[220:223]\n"
    "v_exp_f32 v38, v38\n"
    "v_mfma_f32_16x16x32_bf16 a[224:227], a[196:199], v[52:55], a[224:227]\n"
    "s_waitcnt vmcnt(24) lgkmcnt(2)\n"
    "v_exp_f32 v39, v39\n"
    "v_mfma_f32_16x16x32_bf16 a[228:231], a[196:199], v[56:59], a[228:231]\n"
    "s_barrier\n"
    "ds_read_b128 a[192:195], v0 offset:0\n"
    "v_cvt_pk_bf16_f32 v40, v40, v41\n"
    "v_mfma_f32_16x16x32_bf16 v[188:191], a[200:203], v[56:59], v[188:191]\n"
    "s_add_u32 m0, s54, 49152\n"
    "ds_read_b128 a[196:199], v1 offset:0\n"
    "v_cvt_pk_bf16_f32 v37, v38, v39\n"
    "v_mfma_f32_16x16x32_bf16 v[184:187], a[200:203], v[52:55], v[184:187]\n"
    "v_exp_f32 v42, v42\n"
    "v_mfma_f32_16x16x32_bf16 v[180:183], a[200:203], v[48:51], v[180:183]\n"
    "v_exp_f32 v43, v43\n"
    "v_mfma_f32_16x16x32_bf16 v[176:179], a[200:203], v[44:47], v[176:179]\n"
    "ds_read_b128 a[200:203], v0 offset:128\n"
    "v_cvt_pk_bf16_f32 v41, v42, v43\n"
    "v_mfma_f32_16x16x32_bf16 a[232:235], a[204:207], v[44:47], a[232:235]\n"
    "v_exp_f32 v68, v68\n"
    "v_mfma_f32_16x16x32_bf16 v[148:151], a[204:207], v[48:51], v[148:151]\n"
    "v_exp_f32 v69, v69\n"
    "v_mfma_f32_16x16x32_bf16 v[152:155], a[204:207], v[52:55], v[152:155]\n"
    "v_exp_f32 v70, v70\n"
    "v_mfma_f32_16x16x32_bf16 v[156:159], a[204:207], v[56:59], v[156:159]\n"
    "ds_read_b128 a[204:207], v1 offset:128\n"
    "v_cvt_pk_bf16_f32 v38, v68, v69\n"
    "v_mfma_f32_16x16x32_bf16 v[220:223], a[208:211], v[56:59], v[220:223]\n"
    "s_waitcnt lgkmcnt(4)\n"
    "buffer_load_dwordx4 v6, s[36:39], s56 offen offset:0 nt lds\n"
    "v_mfma_f32_16x16x32_bf16 v[216:219], a[208:211], v[52:55], v[216:219]\n"
    "buffer_store_dwordx4 v[168:171], v9, s[48:51], s74 offen offset:896 sc1\n"
    "v_mfma_f32_16x16x32_bf16 v[212:215], a[208:211], v[48:51], v[212:215]\n"
    "v_exp_f32 v71, v71\n"
    "v_mfma_f32_16x16x32_bf16 v[208:211], a[208:211], v[44:47], v[208:211]\n"
    "ds_read_b128 a[208:211], v0 offset:256\n"
    "v_cvt_pk_bf16_f32 v39, v70, v71\n"
    "v_mfma_f32_16x16x32_bf16 v[240:243], a[212:215], v[44:47], v[240:243]\n"
    "v_exp_f32 v72, v72\n"
    "v_mfma_f32_16x16x32_bf16 v[244:247], a[212:215], v[48:51], v[244:247]\n"
    "v_exp_f32 v73, v73\n"
    "v_mfma_f32_16x16x32_bf16 v[248:251], a[212:215], v[52:55], v[248:251]\n"
    "v_exp_f32 v74, v74\n"
    "v_mfma_f32_16x16x32_bf16 v[252:255], a[212:215], v[56:59], v[252:255]\n"
    "s_waitcnt lgkmcnt(4)\n"
    "ds_read_b128 a[212:215], v1 offset:256\n"
    "v_cvt_pk_bf16_f32 v42, v72, v73\n"
    "v_mfma_f32_16x16x32_bf16 v[44:47], a[192:195], a[96:99], 0\n"
    "s_waitcnt lgkmcnt(4)\n"
    "v_exp_f32 v75, v75\n"
    "v_mfma_f32_16x16x32_bf16 v[44:47], a[196:199], a[100:103], v[44:47]\n"
    "s_waitcnt lgkmcnt(3)\n"
    "v_cvt_pk_bf16_f32 v43, v74, v75\n"
    "v_accvgpr_read_b32 v140, a48\n"
    "v_mfma_f32_16x16x32_bf16 v[44:47], a[200:203], a[104:107], v[44:47]\n"
    "v_accvgpr_read_b32 v141, a49\n"
    "v_accvgpr_read_b32 v142, a50\n"
    "v_mfma_f32_16x16x32_bf16 v[48:51], a[200:203], a[128:131], 0\n"
    "ds_write_b64 v10, v[144:145] offset:7680\n"
    "v_mfma_f32_16x16x32_bf16 v[48:51], a[196:199], a[124:127], v[48:51]\n"
    "buffer_load_dwordx4 v7, s[36:39], s56 offen offset:1024 nt lds\n"
    "v_mfma_f32_16x16x32_bf16 v[48:51], a[192:195], a[120:123], v[48:51]\n"
    "v_accvgpr_read_b32 v143, a51\n"
    "v_accvgpr_read_b32 v168, a52\n"
    "v_mfma_f32_16x16x32_bf16 v[52:55], a[192:195], a[144:147], 0\n"
    "v_accvgpr_read_b32 v169, a53\n"
    "v_accvgpr_read_b32 v170, a54\n"
    "v_mfma_f32_16x16x32_bf16 v[52:55], a[196:199], a[148:151], v[52:55]\n"
    "v_accvgpr_read_b32 v171, a55\n"
    "v_mfma_f32_16x16x32_bf16 v[52:55], a[200:203], a[152:155], v[52:55]\n"
    "ds_write_b64 v10, v[146:147] offset:7712\n"
    "v_mfma_f32_16x16x32_bf16 v[56:59], a[200:203], a[176:179], 0\n"
    "ds_read_b128 a[200:203], v0 offset:6272\n"
    "v_mfma_f32_16x16x32_bf16 v[56:59], a[196:199], a[172:175], v[56:59]\n"
    "ds_read_b128 a[196:199], v1 offset:6144\n"
    "v_mfma_f32_16x16x32_bf16 v[56:59], a[192:195], a[168:171], v[56:59]\n"
    "s_waitcnt lgkmcnt(6)\n"
    "ds_read_b128 a[192:195], v0 offset:6144\n"
    "v_mfma_f32_16x16x32_bf16 v[44:47], a[204:207], a[108:111], v[44:47]\n"
    "s_waitcnt lgkmcnt(6)\n"
    "v_mfma_f32_16x16x32_bf16 v[44:47], a[208:211], a[112:115], v[44:47]\n"
    "s_waitcnt lgkmcnt(5)\n"
    "ds_write_b64 v10, v[172:173] offset:7744\n"
    "v_mfma_f32_16x16x32_bf16 v[44:47], a[212:215], a[116:119], v[44:47]\n"
    "v_mfma_f32_16x16x32_bf16 v[48:51], a[212:215], a[140:143], v[48:51]\n"
    "v_mfma_f32_16x16x32_bf16 v[48:51], a[208:211], a[136:139], v[48:51]\n"
    "buffer_load_dwordx4 v8, s[36:39], s56 offen offset:2048 nt lds\n"
    "v_mfma_f32_16x16x32_bf16 v[48:51], a[204:207], a[132:135], v[48:51]\n"
    "s_add_u32 m0, s54, 98304\n"
    "v_mfma_f32_16x16x32_bf16 v[52:55], a[204:207], a[156:159], v[52:55]\n"
    "ds_write_b64 v10, v[174:175] offset:7776\n"
    "v_mfma_f32_16x16x32_bf16 v[52:55], a[208:211], a[160:163], v[52:55]\n"
    "v_fma_f32 v44, v44, s52, v12\n"
    "v_fma_f32 v45, v45, s52, v12\n"
    "v_mfma_f32_16x16x32_bf16 v[52:55], a[212:215], a[164:167], v[52:55]\n"
    "v_exp_f32 v44, v44\n"
    "v_mfma_f32_16x16x32_bf16 v[56:59], a[212:215], a[188:191], v[56:59]\n"
    "ds_read_b128 a[212:215], v1 offset:6400\n"
    "v_fma_f32 v46, v46, s52, v12\n"
    "v_mfma_f32_16x16x32_bf16 v[56:59], a[208:211], a[184:187], v[56:59]\n"
    "ds_read_b128 a[208:211], v0 offset:6400\n"
    "v_fma_f32 v47, v47, s52, v12\n"
    "v_mfma_f32_16x16x32_bf16 v[56:59], a[204:207], a[180:183], v[56:59]\n"
    "s_waitcnt lgkmcnt(4)\n"
    "ds_read_b128 a[204:207], v1 offset:6272\n"
    "v_fma_f32 v48, v48, s52, v13\n"
    "v_mfma_f32_16x16x32_bf16 v[76:79], a[192:195], a[96:99], 0\n"
    "v_exp_f32 v45, v45\n"
    "v_mfma_f32_16x16x32_bf16 v[76:79], a[196:199], a[100:103], v[76:79]\n"
    "v_exp_f32 v46, v46\n"
    "v_mfma_f32_16x16x32_bf16 v[76:79], a[200:203], a[104:107], v[76:79]\n"
    "v_exp_f32 v47, v47\n"
    "v_mfma_f32_16x16x32_bf16 v[80:83], a[200:203], a[128:131], 0\n"
    "ds_read_b64 v[144:145], v11 offset:7680\n"
    "v_cvt_pk_bf16_f32 v44, v44, v45\n"
    "v_mfma_f32_16x16x32_bf16 v[80:83], a[196:199], a[124:127], v[80:83]\n"
    "buffer_load_dwordx4 v6, s[40:43], s55 offen offset:0 nt lds\n"
    "v_mfma_f32_16x16x32_bf16 v[80:83], a[192:195], a[120:123], v[80:83]\n"
    "ds_read_b64 v[146:147], v20 offset:7680\n"
    "v_cvt_pk_bf16_f32 v45, v46, v47\n"
    "v_mfma_f32_16x16x32_bf16 v[60:63], a[192:195], a[144:147], 0\n"
    "v_fma_f32 v49, v49, s52, v13\n"
    "v_fma_f32 v50, v50, s52, v13\n"
    "v_mfma_f32_16x16x32_bf16 v[60:63], a[196:199], a[148:151], v[60:63]\n"
    "v_exp_f32 v48, v48\n"
    "v_mfma_f32_16x16x32_bf16 v[60:63], a[200:203], a[152:155], v[60:63]\n"
    "v_exp_f32 v49, v49\n"
    "v_mfma_f32_16x16x32_bf16 v[64:67], a[200:203], a[176:179], 0\n"
    "ds_read_b64_tr_b16 a[200:201], v4 offset:49152\n"
    "v_fma_f32 v51, v51, s52, v13\n"
    "v_mfma_f32_16x16x32_bf16 v[64:67], a[196:199], a[172:175], v[64:67]\n"
    "ds_read_b64_tr_b16 a[196:197], v3 offset:49152\n"
    "v_cvt_pk_bf16_f32 v48, v48, v49\n"
    "v_mfma_f32_16x16x32_bf16 v[64:67], a[192:195], a[168:171], v[64:67]\n"
    "s_waitcnt lgkmcnt(4)\n"
    "ds_read_b64_tr_b16 a[192:193], v2 offset:49152\n"
    "v_fma_f32 v52, v52, s52, v14\n"
    "v_mfma_f32_16x16x32_bf16 v[76:79], a[204:207], a[108:111], v[76:79]\n"
    "ds_read_b64_tr_b16 a[194:195], v2 offset:55296\n"
    "v_fma_f32 v53, v53, s52, v14\n"
    "v_mfma_f32_16x16x32_bf16 v[76:79], a[208:211], a[112:115], v[76:79]\n"
    "ds_read_b64_tr_b16 a[198:199], v3 offset:55296\n"
    "v_fma_f32 v56, v56, s52, v15\n"
    "v_mfma_f32_16x16x32_bf16 v[76:79], a[212:215], a[116:119], v[76:79]\n"
    "ds_read_b64_tr_b16 a[202:203], v4 offset:55296\n"
    "v_fma_f32 v57, v57, s52, v15\n"
    "v_mfma_f32_16x16x32_bf16 v[80:83], a[212:215], a[140:143], v[80:83]\n"
    "v_exp_f32 v50, v50\n"
    "v_mfma_f32_16x16x32_bf16 v[80:83], a[208:211], a[136:139], v[80:83]\n"
    "buffer_load_dwordx4 v7, s[40:43], s55 offen offset:1024 nt lds\n"
    "v_mfma_f32_16x16x32_bf16 v[80:83], a[204:207], a[132:135], v[80:83]\n"
    "v_exp_f32 v51, v51\n"
    "v_mfma_f32_16x16x32_bf16 v[60:63], a[204:207], a[156:159], v[60:63]\n"
    "s_waitcnt lgkmcnt(6)\n"
    "v_fma_f32 v76, v76, s52, v12\n"
    "v_fma_f32 v77, v77, s52, v12\n"
    "v_mfma_f32_16x16x32_bf16 v[60:63], a[208:211], a[160:163], v[60:63]\n"
    "buffer_store_dwordx4 v[144:147], v9, s[48:51], s75 offen offset:128 sc1\n"
    "v_mfma_f32_16x16x32_bf16 v[60:63], a[212:215], a[164:167], v[60:63]\n"
    "v_fma_f32 v78, v78, s52, v12\n"
    "v_fma_f32 v79, v79, s52, v12\n"
    "v_mfma_f32_16x16x32_bf16 v[64:67], a[212:215], a[188:191], v[64:67]\n"
    "ds_read_b64_tr_b16 a[212:213], v3 offset:49280\n"
    "v_fma_f32 v80, v80, s52, v13\n"
    "v_mfma_f32_16x16x32_bf16 v[64:67], a[208:211], a[184:187], v[64:67]\n"
    "ds_read_b64_tr_b16 a[208:209], v2 offset:49280\n"
    "v_fma_f32 v81, v81, s52, v13\n"
    "v_mfma_f32_16x16x32_bf16 v[64:67], a[204:207], a[180:183], v[64:67]\n"
    "ds_read_b64_tr_b16 a[204:205], v5 offset:49152\n"
    "v_fma_f32 v82, v82, s52, v13\n"
    "v_smfmac_f32_16x16x64_bf16 a[244:247], a[252:255], v[28:35], v23\n"
    "ds_read_b64_tr_b16 a[206:207], v5 offset:55296\n"
    "v_fma_f32 v83, v83, s52, v13\n"
    "v_smfmac_f32_16x16x64_bf16 a[248:251], a[252:255], v[36:43], v23\n"
    "s_waitcnt lgkmcnt(6)\n"
    "ds_read_b64_tr_b16 a[210:211], v2 offset:55424\n"
    "v_cvt_pk_bf16_f32 v49, v50, v51\n"
    "v_mfma_f32_16x16x32_bf16 v[104:107], a[192:195], v[40:43], v[104:107]\n"
    "ds_read_b64_tr_b16 a[214:215], v3 offset:55424\n"
    "v_fma_f32 v54, v54, s52, v14\n"
    "v_mfma_f32_16x16x32_bf16 v[88:91], a[192:195], v[36:39], v[88:91]\n"
    "v_exp_f32 v76, v76\n"
    "v_mfma_f32_16x16x32_bf16 v[100:103], a[192:195], v[32:35], v[100:103]\n"
    "buffer_load_dwordx4 v8, s[40:43], s55 offen offset:2048 nt lds\n"
    "v_mfma_f32_16x16x32_bf16 v[84:87], a[192:195], v[28:31], v[84:87]\n"
    "s_waitcnt lgkmcnt(7)\n"
    "ds_read_b64_tr_b16 a[192:193], v4 offset:49280\n"
    "v_fma_f32 v55, v55, s52, v14\n"
    "v_mfma_f32_16x16x32_bf16 v[92:95], a[196:199], v[28:31], v[92:95]\n"
    "s_add_u32 s55, s55, 24576\n"
    "ds_read_b64_tr_b16 a[194:195], v4 offset:55424\n"
    "v_fma_f32 v58, v58, s52, v15\n"
    "v_mfma_f32_16x16x32_bf16 v[108:111], a[196:199], v[32:35], v[108:111]\n"
    "v_exp_f32 v77, v77\n"
    "v_mfma_f32_16x16x32_bf16 v[96:99], a[196:199], v[36:39], v[96:99]\n"
    "v_exp_f32 v78, v78\n"
    "v_mfma_f32_16x16x32_bf16 v[112:115], a[196:199], v[40:43], v[112:115]\n"
    "s_waitcnt lgkmcnt(8)\n"
    "ds_read_b64_tr_b16 a[196:197], v5 offset:49280\n"
    "v_cvt_pk_bf16_f32 v46, v76, v77\n"
    "v_mfma_f32_16x16x32_bf16 v[164:167], a[200:203], v[40:43], v[164:167]\n"
    "ds_read_b64_tr_b16 a[198:199], v5 offset:55424\n"
    "v_fma_f32 v59, v59, s52, v15\n"
    "v_mfma_f32_16x16x32_bf16 v[136:139], a[200:203], v[36:39], v[136:139]\n"
    "v_exp_f32 v79, v79\n"
    "v_mfma_f32_16x16x32_bf16 v[160:163], a[200:203], v[32:35], v[160:163]\n"
    "v_cvt_pk_bf16_f32 v47, v78, v79\n"
    "v_fma_f32 v60, v60, s52, v14\n"
    "v_mfma_f32_16x16x32_bf16 v[132:135], a[200:203], v[28:31], v[132:135]\n"
    "s_waitcnt lgkmcnt(6)\n"
    "ds_read_b64_tr_b16 a[200:201], v2 offset:49408\n"
    "v_fma_f32 v61, v61, s52, v14\n"
    "v_mfma_f32_16x16x32_bf16 v[140:143], a[204:207], v[28:31], v[140:143]\n"
    "ds_read_b64_tr_b16 a[202:203], v2 offset:55552\n"
    "v_fma_f32 v62, v62, s52, v14\n"
    "v_mfma_f32_16x16x32_bf16 v[168:171], a[204:207], v[32:35], v[168:171]\n"
    "v_exp_f32 v80, v80\n"
    "v_mfma_f32_16x16x32_bf16 a[56:59], a[204:207], v[36:39], a[56:59]\n"
    "v_exp_f32 v81, v81\n"
    "v_mfma_f32_16x16x32_bf16 a[60:63], a[204:207], v[40:43], a[60:63]\n"
    "s_waitcnt lgkmcnt(7)\n"
    "ds_read_b64_tr_b16 a[204:205], v3 offset:49408\n"
    "v_cvt_pk_bf16_f32 v50, v80, v81\n"
    "v_mfma_f32_16x16x32_bf16 a[76:79], a[208:211], v[40:43], a[76:79]\n"
    "ds_read_b64_tr_b16 a[206:207], v3 offset:55552\n"
    "v_fma_f32 v63, v63, s52, v14\n"
    "v_mfma_f32_16x16x32_bf16 a[72:75], a[208:211], v[36:39], a[72:75]\n"
    "v_exp_f32 v82, v82\n"
    "v_mfma_f32_16x16x32_bf16 a[68:71], a[208:211], v[32:35], a[68:71]\n"
    "v_exp_f32 v83, v83\n"
    "v_mfma_f32_16x16x32_bf16 a[64:67], a[208:211], v[28:31], a[64:67]\n"
    "s_waitcnt lgkmcnt(8)\n"
    "ds_read_b64_tr_b16 a[208:209], v4 offset:49408\n"
    "v_cvt_pk_bf16_f32 v51, v82, v83\n"
    "v_mfma_f32_16x16x32_bf16 a[80:83], a[212:215], v[28:31], a[80:83]\n"
    "ds_read_b64_tr_b16 a[210:211], v4 offset:55552\n"
    "v_fma_f32 v64, v64, s52, v15\n"
    "v_mfma_f32_16x16x32_bf16 a[84:87], a[212:215], v[32:35], a[84:87]\n"
    "v_exp_f32 v52, v52\n"
    "v_mfma_f32_16x16x32_bf16 a[88:91], a[212:215], v[36:39], a[88:91]\n"
    "v_exp_f32 v53, v53\n"
    "v_mfma_f32_16x16x32_bf16 a[92:95], a[212:215], v[40:43], a[92:95]\n"
    "s_waitcnt lgkmcnt(8)\n"
    "ds_read_b64_tr_b16 a[212:213], v5 offset:49408\n"
    "v_fma_f32 v65, v65, s52, v15\n"
    "v_mfma_f32_16x16x32_bf16 v[128:131], a[192:195], v[40:43], v[128:131]\n"
    "ds_read_b64_tr_b16 a[214:215], v5 offset:55552\n"
    "v_fma_f32 v66, v66, s52, v15\n"
    "v_mfma_f32_16x16x32_bf16 v[124:127], a[192:195], v[36:39], v[124:127]\n"
    "v_exp_f32 v56, v56\n"
    "v_mfma_f32_16x16x32_bf16 v[120:123], a[192:195], v[32:35], v[120:123]\n"
    "v_exp_f32 v57, v57\n"
    "v_mfma_f32_16x16x32_bf16 v[116:119], a[192:195], v[28:31], v[116:119]\n"
    "s_waitcnt lgkmcnt(8)\n"
    "ds_read_b64 v[172:173], v11 offset:8000\n"
    "v_fma_f32 v67, v67, s52, v15\n"
    "v_mfma_f32_16x16x32_bf16 a[216:219], a[196:199], v[28:31], a[216:219]\n"
    "ds_read_b64 v[174:175], v20 offset:8000\n"
    "v_cvt_pk_bf16_f32 v52, v52, v53\n"
    "v_mfma_f32_16x16x32_bf16 a[220:223], a[196:199], v[32:35], a[220:223]\n"
    "v_exp_f32 v54, v54\n"
    "v_mfma_f32_16x16x32_bf16 a[224:227], a[196:199], v[36:39], a[224:227]\n"
    "s_waitcnt vmcnt(24) lgkmcnt(2)\n"
    "v_exp_f32 v55, v55\n"
    "v_mfma_f32_16x16x32_bf16 a[228:231], a[196:199], v[40:43], a[228:231]\n"
    "s_barrier\n"
    "ds_read_b128 a[192:195], v0 offset:12288\n"
    "v_cvt_pk_bf16_f32 v56, v56, v57\n"
    "v_mfma_f32_16x16x32_bf16 v[188:191], a[200:203], v[40:43], v[188:191]\n"
    "s_add_u32 m0, s54, 0\n"
    "ds_read_b128 a[196:199], v1 offset:12288\n"
    "v_cvt_pk_bf16_f32 v53, v54, v55\n"
    "v_mfma_f32_16x16x32_bf16 v[184:187], a[200:203], v[36:39], v[184:187]\n"
    "v_exp_f32 v58, v58\n"
    "v_mfma_f32_16x16x32_bf16 v[180:183], a[200:203], v[32:35], v[180:183]\n"
    "v_exp_f32 v59, v59\n"
    "v_mfma_f32_16x16x32_bf16 v[176:179], a[200:203], v[28:31], v[176:179]\n"
    "ds_read_b128 a[200:203], v0 offset:12416\n"
    "v_cvt_pk_bf16_f32 v57, v58, v59\n"
    "v_mfma_f32_16x16x32_bf16 a[232:235], a[204:207], v[28:31], a[232:235]\n"
    "v_exp_f32 v60, v60\n"
    "v_mfma_f32_16x16x32_bf16 v[148:151], a[204:207], v[32:35], v[148:151]\n"
    "v_exp_f32 v61, v61\n"
    "v_mfma_f32_16x16x32_bf16 v[152:155], a[204:207], v[36:39], v[152:155]\n"
    "v_exp_f32 v62, v62\n"
    "v_mfma_f32_16x16x32_bf16 v[156:159], a[204:207], v[40:43], v[156:159]\n"
    "ds_read_b128 a[204:207], v1 offset:12416\n"
    "v_cvt_pk_bf16_f32 v54, v60, v61\n"
    "v_mfma_f32_16x16x32_bf16 v[220:223], a[208:211], v[40:43], v[220:223]\n"
    "buffer_load_dwordx4 v6, s[36:39], s55 offen offset:0 nt lds\n"
    "v_mfma_f32_16x16x32_bf16 v[216:219], a[208:211], v[36:39], v[216:219]\n"
    "v_exp_f32 v63, v63\n"
    "v_mfma_f32_16x16x32_bf16 v[212:215], a[208:211], v[32:35], v[212:215]\n"
    "s_waitcnt lgkmcnt(4)\n"
    "v_exp_f32 v64, v64\n"
    "v_mfma_f32_16x16x32_bf16 v[208:211], a[208:211], v[28:31], v[208:211]\n"
    "buffer_store_dwordx4 v[172:175], v9, s[48:51], s75 offen offset:896 sc1\n"
    "v_mfma_f32_16x16x32_bf16 v[240:243], a[212:215], v[28:31], v[240:243]\n"
    "ds_read_b128 a[208:211], v0 offset:12544\n"
    "v_cvt_pk_bf16_f32 v55, v62, v63\n"
    "v_mfma_f32_16x16x32_bf16 v[244:247], a[212:215], v[32:35], v[244:247]\n"
    "v_exp_f32 v65, v65\n"
    "v_mfma_f32_16x16x32_bf16 v[248:251], a[212:215], v[36:39], v[248:251]\n"
    "v_exp_f32 v66, v66\n"
    "v_mfma_f32_16x16x32_bf16 v[252:255], a[212:215], v[40:43], v[252:255]\n"
    "s_waitcnt lgkmcnt(4)\n"
    "ds_read_b128 a[212:215], v1 offset:12544\n"
    "v_cvt_pk_bf16_f32 v58, v64, v65\n"
    "v_mfma_f32_16x16x32_bf16 v[28:31], a[192:195], a[96:99], 0\n"
    "s_waitcnt lgkmcnt(4)\n"
    "v_exp_f32 v67, v67\n"
    "v_mfma_f32_16x16x32_bf16 v[28:31], a[196:199], a[100:103], v[28:31]\n"
    "s_waitcnt lgkmcnt(3)\n"
    "v_cvt_pk_bf16_f32 v59, v66, v67\n"
    "v_accvgpr_read_b32 v144, a56\n"
    "v_mfma_f32_16x16x32_bf16 v[28:31], a[200:203], a[104:107], v[28:31]\n"
    "v_accvgpr_read_b32 v145, a57\n"
    "v_accvgpr_read_b32 v146, a58\n"
    "v_mfma_f32_16x16x32_bf16 v[32:35], a[200:203], a[128:131], 0\n"
    "v_accvgpr_read_b32 v147, a59\n"
    "v_accvgpr_read_b32 v172, a60\n"
    "v_mfma_f32_16x16x32_bf16 v[32:35], a[196:199], a[124:127], v[32:35]\n"
    "buffer_load_dwordx4 v7, s[36:39], s55 offen offset:1024 nt lds\n"
    "v_mfma_f32_16x16x32_bf16 v[32:35], a[192:195], a[120:123], v[32:35]\n"
    "ds_write_b64 v10, v[192:193] offset:0\n"
    "v_mfma_f32_16x16x32_bf16 v[36:39], a[192:195], a[144:147], 0\n"
    "v_accvgpr_read_b32 v173, a61\n"
    "v_accvgpr_read_b32 v174, a62\n"
    "v_mfma_f32_16x16x32_bf16 v[36:39], a[196:199], a[148:151], v[36:39]\n"
    "v_accvgpr_read_b32 v175, a63\n"
    "v_mfma_f32_16x16x32_bf16 v[36:39], a[200:203], a[152:155], v[36:39]\n"
    "v_mfma_f32_16x16x32_bf16 v[40:43], a[200:203], a[176:179], 0\n"
    "ds_read_b128 a[200:203], v0 offset:18560\n"
    "v_mfma_f32_16x16x32_bf16 v[40:43], a[196:199], a[172:175], v[40:43]\n"
    "ds_write_b64 v10, v[194:195] offset:32\n"
    "v_mfma_f32_16x16x32_bf16 v[40:43], a[192:195], a[168:171], v[40:43]\n"
    "s_waitcnt lgkmcnt(5)\n"
    "ds_read_b128 a[192:195], v0 offset:18432\n"
    "ds_read_b128 a[196:199], v1 offset:18432\n"
    "v_mfma_f32_16x16x32_bf16 v[28:31], a[204:207], a[108:111], v[28:31]\n"
    "s_waitcnt lgkmcnt(6)\n"
    "v_mfma_f32_16x16x32_bf16 v[28:31], a[208:211], a[112:115], v[28:31]\n"
    "s_waitcnt lgkmcnt(5)\n"
    "v_mfma_f32_16x16x32_bf16 v[28:31], a[212:215], a[116:119], v[28:31]\n"
    "v_mfma_f32_16x16x32_bf16 v[32:35], a[212:215], a[140:143], v[32:35]\n"
    "ds_write_b64 v10, v[224:225] offset:64\n"
    "v_mfma_f32_16x16x32_bf16 v[32:35], a[208:211], a[136:139], v[32:35]\n"
    "buffer_load_dwordx4 v8, s[36:39], s55 offen offset:2048 nt lds\n"
    "v_mfma_f32_16x16x32_bf16 v[32:35], a[204:207], a[132:135], v[32:35]\n"
    "s_add_u32 m0, s54, 110592\n"
    "v_mfma_f32_16x16x32_bf16 v[36:39], a[204:207], a[156:159], v[36:39]\n"
    "v_mfma_f32_16x16x32_bf16 v[36:39], a[208:211], a[160:163], v[36:39]\n"
    "v_fma_f32 v28, v28, s52, v12\n"
    "v_fma_f32 v29, v29, s52, v12\n"
    "v_mfma_f32_16x16x32_bf16 v[36:39], a[212:215], a[164:167], v[36:39]\n"
    "ds_write_b64 v10, v[226:227] offset:96\n"
    "v_mfma_f32_16x16x32_bf16 v[40:43], a[212:215], a[188:191], v[40:43]\n"
    "ds_read_b128 a[212:215], v1 offset:18688\n"
    "v_fma_f32 v30, v30, s52, v12\n"
    "v_mfma_f32_16x16x32_bf16 v[40:43], a[208:211], a[184:187], v[40:43]\n"
    "ds_read_b128 a[208:211], v0 offset:18688\n"
    "v_fma_f32 v31, v31, s52, v12\n"
    "v_mfma_f32_16x16x32_bf16 v[40:43], a[204:207], a[180:183], v[40:43]\n"
    "s_waitcnt lgkmcnt(5)\n"
    "ds_read_b128 a[204:207], v1 offset:18560\n"
    "v_fma_f32 v32, v32, s52, v13\n"
    "v_mfma_f32_16x16x32_bf16 v[68:71], a[192:195], a[96:99], 0\n"
    "s_waitcnt lgkmcnt(5)\n"
    "v_exp_f32 v28, v28\n"
    "v_mfma_f32_16x16x32_bf16 v[68:71], a[196:199], a[100:103], v[68:71]\n"
    "v_exp_f32 v29, v29\n"
    "v_mfma_f32_16x16x32_bf16 v[68:71], a[200:203], a[104:107], v[68:71]\n"
    "v_exp_f32 v30, v30\n"
    "v_mfma_f32_16x16x32_bf16 v[72:75], a[200:203], a[128:131], 0\n"
    "v_exp_f32 v31, v31\n"
    "v_mfma_f32_16x16x32_bf16 v[72:75], a[196:199], a[124:127], v[72:75]\n"
    "buffer_load_dwordx4 v6, s[40:43], s56 offen offset:0 nt lds\n"
    "v_mfma_f32_16x16x32_bf16 v[72:75], a[192:195], a[120:123], v[72:75]\n"
    "ds_read_b64 v[192:193], v11 offset:0\n"
    "v_cvt_pk_bf16_f32 v28, v28, v29\n"
    "v_mfma_f32_16x16x32_bf16 v[76:79], a[192:195], a[144:147], 0\n"
    "ds_read_b64 v[194:195], v20 offset:0\n"
    "v_cvt_pk_bf16_f32 v29, v30, v31\n"
    "v_mfma_f32_16x16x32_bf16 v[76:79], a[196:199], a[148:151], v[76:79]\n"
    "v_fma_f32 v33, v33, s52, v13\n"
    "v_fma_f32 v34, v34, s52, v13\n"
    "v_mfma_f32_16x16x32_bf16 v[76:79], a[200:203], a[152:155], v[76:79]\n"
    "v_exp_f32 v32, v32\n"
    "v_mfma_f32_16x16x32_bf16 v[80:83], a[200:203], a[176:179], 0\n"
    "ds_read_b64_tr_b16 a[200:201], v4 offset:0\n"
    "v_fma_f32 v35, v35, s52, v13\n"
    "v_mfma_f32_16x16x32_bf16 v[80:83], a[196:199], a[172:175], v[80:83]\n"
    "ds_read_b64_tr_b16 a[196:197], v3 offset:0\n"
    "v_fma_f32 v36, v36, s52, v14\n"
    "v_mfma_f32_16x16x32_bf16 v[80:83], a[192:195], a[168:171], v[80:83]\n"
    "s_waitcnt lgkmcnt(4)\n"
    "ds_read_b64_tr_b16 a[192:193], v2 offset:0\n"
    "v_fma_f32 v37, v37, s52, v14\n"
    "v_mfma_f32_16x16x32_bf16 v[68:71], a[204:207], a[108:111], v[68:71]\n"
    "ds_read_b64_tr_b16 a[194:195], v2 offset:6144\n"
    "v_fma_f32 v40, v40, s52, v15\n"
    "v_mfma_f32_16x16x32_bf16 v[68:71], a[208:211], a[112:115], v[68:71]\n"
    "ds_read_b64_tr_b16 a[198:199], v3 offset:6144\n"
    "v_fma_f32 v41, v41, s52, v15\n"
    "v_mfma_f32_16x16x32_bf16 v[68:71], a[212:215], a[116:119], v[68:71]\n"
    "ds_read_b64_tr_b16 a[202:203], v4 offset:6144\n"
    "v_fma_f32 v38, v38, s52, v14\n"
    "v_mfma_f32_16x16x32_bf16 v[72:75], a[212:215], a[140:143], v[72:75]\n"
    "v_exp_f32 v33, v33\n"
    "v_mfma_f32_16x16x32_bf16 v[72:75], a[208:211], a[136:139], v[72:75]\n"
    "buffer_load_dwordx4 v7, s[40:43], s56 offen offset:1024 nt lds\n"
    "v_mfma_f32_16x16x32_bf16 v[72:75], a[204:207], a[132:135], v[72:75]\n"
    "v_exp_f32 v34, v34\n"
    "v_mfma_f32_16x16x32_bf16 v[76:79], a[204:207], a[156:159], v[76:79]\n"
    "v_fma_f32 v68, v68, s52, v12\n"
    "v_fma_f32 v69, v69, s52, v12\n"
    "v_mfma_f32_16x16x32_bf16 v[76:79], a[208:211], a[160:163], v[76:79]\n"
    "v_fma_f32 v70, v70, s52, v12\n"
    "v_fma_f32 v71, v71, s52, v12\n"
    "v_mfma_f32_16x16x32_bf16 v[76:79], a[212:215], a[164:167], v[76:79]\n"
    "s_waitcnt lgkmcnt(6)\n"
    "v_exp_f32 v68, v68\n"
    "v_mfma_f32_16x16x32_bf16 v[80:83], a[212:215], a[188:191], v[80:83]\n"
    "buffer_store_dwordx4 v[192:195], v9, s[48:51], s72 offen offset:256 sc1\n"
    "v_mfma_f32_16x16x32_bf16 v[80:83], a[208:211], a[184:187], v[80:83]\n"
    "ds_read_b64_tr_b16 a[208:209], v2 offset:128\n"
    "v_fma_f32 v72, v72, s52, v13\n"
    "v_mfma_f32_16x16x32_bf16 v[80:83], a[204:207], a[180:183], v[80:83]\n"
    "ds_read_b64_tr_b16 a[204:205], v5 offset:0\n"
    "v_fma_f32 v73, v73, s52, v13\n"
    "v_smfmac_f32_16x16x64_bf16 a[244:247], a[252:255], v[44:51], v23\n"
    "ds_read_b64_tr_b16 a[206:207], v5 offset:6144\n"
    "v_fma_f32 v74, v74, s52, v13\n"
    "v_smfmac_f32_16x16x64_bf16 a[248:251], a[252:255], v[52:59], v23\n"
    "s_waitcnt lgkmcnt(5)\n"
    "ds_read_b64_tr_b16 a[210:211], v2 offset:6272\n"
    "v_fma_f32 v75, v75, s52, v13\n"
    "v_mfma_f32_16x16x32_bf16 v[104:107], a[192:195], v[56:59], v[104:107]\n"
    "ds_read_b64_tr_b16 a[212:213], v3 offset:128\n"
    "v_cvt_pk_bf16_f32 v32, v32, v33\n"
    "v_mfma_f32_16x16x32_bf16 v[88:91], a[192:195], v[52:55], v[88:91]\n"
    "ds_read_b64_tr_b16 a[214:215], v3 offset:6272\n"
    "v_fma_f32 v39, v39, s52, v14\n"
    "v_mfma_f32_16x16x32_bf16 v[100:103], a[192:195], v[48:51], v[100:103]\n"
    "buffer_load_dwordx4 v8, s[40:43], s56 offen offset:2048 nt lds\n"
    "v_mfma_f32_16x16x32_bf16 v[84:87], a[192:195], v[44:47], v[84:87]\n"
    "s_waitcnt lgkmcnt(7)\n"
    "ds_read_b64_tr_b16 a[192:193], v4 offset:128\n"
    "v_fma_f32 v42, v42, s52, v15\n"
    "v_mfma_f32_16x16x32_bf16 v[92:95], a[196:199], v[44:47], v[92:95]\n"
    "s_add_u32 s56, s56, 24576\n"
    "ds_read_b64_tr_b16 a[194:195], v4 offset:6272\n"
    "v_fma_f32 v43, v43, s52, v15\n"
    "v_mfma_f32_16x16x32_bf16 v[108:111], a[196:199], v[48:51], v[108:111]\n"
    "v_exp_f32 v69, v69\n"
    "v_mfma_f32_16x16x32_bf16 v[96:99], a[196:199], v[52:55], v[96:99]\n"
    "v_exp_f32 v70, v70\n"
    "v_mfma_f32_16x16x32_bf16 v[112:115], a[196:199], v[56:59], v[112:115]\n"
    "s_waitcnt lgkmcnt(8)\n"
    "ds_read_b64_tr_b16 a[196:197], v5 offset:128\n"
    "v_cvt_pk_bf16_f32 v30, v68, v69\n"
    "v_mfma_f32_16x16x32_bf16 v[164:167], a[200:203], v[56:59], v[164:167]\n"
    "ds_read_b64_tr_b16 a[198:199], v5 offset:6272\n"
    "v_fma_f32 v76, v76, s52, v14\n"
    "v_mfma_f32_16x16x32_bf16 v[136:139], a[200:203], v[52:55], v[136:139]\n"
    "v_exp_f32 v71, v71\n"
    "v_mfma_f32_16x16x32_bf16 v[160:163], a[200:203], v[48:51], v[160:163]\n"
    "v_cvt_pk_bf16_f32 v31, v70, v71\n"
    "v_fma_f32 v77, v77, s52, v14\n"
    "v_mfma_f32_16x16x32_bf16 v[132:135], a[200:203], v[44:47], v[132:135]\n"
    "s_waitcnt lgkmcnt(7)\n"
    "ds_read_b64_tr_b16 a[200:201], v2 offset:256\n"
    "v_fma_f32 v78, v78, s52, v14\n"
    "v_mfma_f32_16x16x32_bf16 v[140:143], a[204:207], v[44:47], v[140:143]\n"
    "ds_read_b64_tr_b16 a[202:203], v2 offset:6400\n"
    "v_fma_f32 v79, v79, s52, v14\n"
    "v_mfma_f32_16x16x32_bf16 v[168:171], a[204:207], v[48:51], v[168:171]\n"
    "v_exp_f32 v35, v35\n"
    "v_mfma_f32_16x16x32_bf16 v[144:147], a[204:207], v[52:55], v[144:147]\n"
    "v_exp_f32 v72, v72\n"
    "v_mfma_f32_16x16x32_bf16 v[172:175], a[204:207], v[56:59], v[172:175]\n"
    "s_waitcnt lgkmcnt(8)\n"
    "ds_read_b64_tr_b16 a[204:205], v3 offset:256\n"
    "v_cvt_pk_bf16_f32 v33, v34, v35\n"
    "v_mfma_f32_16x16x32_bf16 a[76:79], a[208:211], v[56:59], a[76:79]\n"
    "ds_read_b64_tr_b16 a[206:207], v3 offset:6400\n"
    "v_fma_f32 v80, v80, s52, v15\n"
    "v_mfma_f32_16x16x32_bf16 a[72:75], a[208:211], v[52:55], a[72:75]\n"
    "v_exp_f32 v73, v73\n"
    "v_mfma_f32_16x16x32_bf16 a[68:71], a[208:211], v[48:51], a[68:71]\n"
    "v_exp_f32 v74, v74\n"
    "v_mfma_f32_16x16x32_bf16 a[64:67], a[208:211], v[44:47], a[64:67]\n"
    "s_waitcnt lgkmcnt(8)\n"
    "ds_read_b64_tr_b16 a[208:209], v4 offset:256\n"
    "v_cvt_pk_bf16_f32 v34, v72, v73\n"
    "v_mfma_f32_16x16x32_bf16 a[80:83], a[212:215], v[44:47], a[80:83]\n"
    "ds_read_b64_tr_b16 a[210:211], v4 offset:6400\n"
    "v_fma_f32 v81, v81, s52, v15\n"
    "v_mfma_f32_16x16x32_bf16 a[84:87], a[212:215], v[48:51], a[84:87]\n"
    "v_exp_f32 v75, v75\n"
    "v_mfma_f32_16x16x32_bf16 a[88:91], a[212:215], v[52:55], a[88:91]\n"
    "v_cvt_pk_bf16_f32 v35, v74, v75\n"
    "v_fma_f32 v82, v82, s52, v15\n"
    "v_mfma_f32_16x16x32_bf16 a[92:95], a[212:215], v[56:59], a[92:95]\n"
    "s_waitcnt lgkmcnt(8)\n"
    "ds_read_b64_tr_b16 a[212:213], v5 offset:256\n"
    "v_fma_f32 v83, v83, s52, v15\n"
    "v_mfma_f32_16x16x32_bf16 v[128:131], a[192:195], v[56:59], v[128:131]\n"
    "ds_read_b64_tr_b16 a[214:215], v5 offset:6400\n"
    "v_accvgpr_read_b32 v192, a64\n"
    "v_mfma_f32_16x16x32_bf16 v[124:127], a[192:195], v[52:55], v[124:127]\n"
    "v_exp_f32 v36, v36\n"
    "v_mfma_f32_16x16x32_bf16 v[120:123], a[192:195], v[48:51], v[120:123]\n"
    "v_exp_f32 v37, v37\n"
    "v_mfma_f32_16x16x32_bf16 v[116:119], a[192:195], v[44:47], v[116:119]\n"
    "s_waitcnt lgkmcnt(8)\n"
    "v_exp_f32 v40, v40\n"
    "v_mfma_f32_16x16x32_bf16 a[216:219], a[196:199], v[44:47], a[216:219]\n"
    "v_exp_f32 v41, v41\n"
    "v_mfma_f32_16x16x32_bf16 a[220:223], a[196:199], v[48:51], a[220:223]\n"
    "ds_read_b64 v[224:225], v11 offset:320\n"
    "v_cvt_pk_bf16_f32 v36, v36, v37\n"
    "v_mfma_f32_16x16x32_bf16 a[224:227], a[196:199], v[52:55], a[224:227]\n"
    "s_waitcnt vmcnt(24) lgkmcnt(1)\n"
    "ds_read_b64 v[226:227], v20 offset:320\n"
    "v_cvt_pk_bf16_f32 v40, v40, v41\n"
    "v_mfma_f32_16x16x32_bf16 a[228:231], a[196:199], v[56:59], a[228:231]\n"
    "s_barrier\n"
    "ds_read_b128 a[192:195], v0 offset:24576\n"
    "v_accvgpr_read_b32 v193, a65\n"
    "v_mfma_f32_16x16x32_bf16 v[188:191], a[200:203], v[56:59], v[188:191]\n"
    "s_add_u32 m0, s54, 12288\n"
    "ds_read_b128 a[196:199], v1 offset:24576\n"
    "v_accvgpr_read_b32 v194, a66\n"
    "v_mfma_f32_16x16x32_bf16 v[184:187], a[200:203], v[52:55], v[184:187]\n"
    "v_exp_f32 v38, v38\n"
    "v_mfma_f32_16x16x32_bf16 v[180:183], a[200:203], v[48:51], v[180:183]\n"
    "v_exp_f32 v39, v39\n"
    "v_mfma_f32_16x16x32_bf16 v[176:179], a[200:203], v[44:47], v[176:179]\n"
    "ds_read_b128 a[200:203], v0 offset:24704\n"
    "v_cvt_pk_bf16_f32 v37, v38, v39\n"
    "v_mfma_f32_16x16x32_bf16 a[232:235], a[204:207], v[44:47], a[232:235]\n"
    "v_exp_f32 v42, v42\n"
    "v_mfma_f32_16x16x32_bf16 v[148:151], a[204:207], v[48:51], v[148:151]\n"
    "v_exp_f32 v43, v43\n"
    "v_mfma_f32_16x16x32_bf16 v[152:155], a[204:207], v[52:55], v[152:155]\n"
    "v_exp_f32 v76, v76\n"
    "v_mfma_f32_16x16x32_bf16 v[156:159], a[204:207], v[56:59], v[156:159]\n"
    "ds_read_b128 a[204:207], v1 offset:24704\n"
    "v_cvt_pk_bf16_f32 v41, v42, v43\n"
    "v_mfma_f32_16x16x32_bf16 v[220:223], a[208:211], v[56:59], v[220:223]\n"
    "buffer_load_dwordx4 v6, s[36:39], s56 offen offset:0 nt lds\n"
    "v_mfma_f32_16x16x32_bf16 v[216:219], a[208:211], v[52:55], v[216:219]\n"
    "v_exp_f32 v77, v77\n"
    "v_mfma_f32_16x16x32_bf16 v[212:215], a[208:211], v[48:51], v[212:215]\n"
    "v_exp_f32 v78, v78\n"
    "v_mfma_f32_16x16x32_bf16 v[208:211], a[208:211], v[44:47], v[208:211]\n"
    "ds_read_b128 a[208:211], v0 offset:24832\n"
    "v_cvt_pk_bf16_f32 v38, v76, v77\n"
    "v_mfma_f32_16x16x32_bf16 v[240:243], a[212:215], v[44:47], v[240:243]\n"
    "s_waitcnt lgkmcnt(5)\n"
    "v_exp_f32 v79, v79\n"
    "v_mfma_f32_16x16x32_bf16 v[244:247], a[212:215], v[48:51], v[244:247]\n"
    "buffer_store_dwordx4 v[224:227], v9, s[48:51], s72 offen offset:1024 sc1\n"
    "v_mfma_f32_16x16x32_bf16 v[248:251], a[212:215], v[52:55], v[248:251]\n"
    "v_exp_f32 v80, v80\n"
    "v_mfma_f32_16x16x32_bf16 v[252:255], a[212:215], v[56:59], v[252:255]\n"
    "s_waitcnt lgkmcnt(4)\n"
    "ds_read_b128 a[212:215], v1 offset:24832\n"
    "v_cvt_pk_bf16_f32 v39, v78, v79\n"
    "v_mfma_f32_16x16x32_bf16 v[44:47], a[192:195], a[96:99], 0\n"
    "s_waitcnt lgkmcnt(4)\n"
    "v_exp_f32 v81, v81\n"
    "v_mfma_f32_16x16x32_bf16 v[44:47], a[196:199], a[100:103], v[44:47]\n"
    "s_waitcnt lgkmcnt(3)\n"
    "v_exp_f32 v82, v82\n"
    "v_mfma_f32_16x16x32_bf16 v[44:47], a[200:203], a[104:107], v[44:47]\n"
    "v_exp_f32 v83, v83\n"
    "v_mfma_f32_16x16x32_bf16 v[48:51], a[200:203], a[128:131], 0\n"
    "v_cvt_pk_bf16_f32 v42, v80, v81\n"
    "v_cvt_pk_bf16_f32 v43, v82, v83\n"
    "v_mfma_f32_16x16x32_bf16 v[48:51], a[196:199], a[124:127], v[48:51]\n"
    "buffer_load_dwordx4 v7, s[36:39], s56 offen offset:1024 nt lds\n"
    "v_mfma_f32_16x16x32_bf16 v[48:51], a[192:195], a[120:123], v[48:51]\n"
    "v_accvgpr_read_b32 v224, a68\n"
    "v_accvgpr_read_b32 v225, a69\n"
    "v_mfma_f32_16x16x32_bf16 v[52:55], a[192:195], a[144:147], 0\n"
    "v_accvgpr_read_b32 v226, a70\n"
    "v_accvgpr_read_b32 v227, a71\n"
    "v_mfma_f32_16x16x32_bf16 v[52:55], a[196:199], a[148:151], v[52:55]\n"
    "ds_write_b64 v10, v[196:197] offset:2560\n"
    "v_mfma_f32_16x16x32_bf16 v[52:55], a[200:203], a[152:155], v[52:55]\n"
    "v_accvgpr_read_b32 v195, a67\n"
    "v_mfma_f32_16x16x32_bf16 v[56:59], a[200:203], a[176:179], 0\n"
    "ds_read_b128 a[200:203], v0 offset:30848\n"
    "v_mfma_f32_16x16x32_bf16 v[56:59], a[196:199], a[172:175], v[56:59]\n"
    "ds_read_b128 a[196:199], v1 offset:30720\n"
    "v_mfma_f32_16x16x32_bf16 v[56:59], a[192:195], a[168:171], v[56:59]\n"
    "s_waitcnt lgkmcnt(5)\n"
    "ds_read_b128 a[192:195], v0 offset:30720\n"
    "v_mfma_f32_16x16x32_bf16 v[44:47], a[204:207], a[108:111], v[44:47]\n"
    "s_waitcnt lgkmcnt(5)\n"
    "ds_write_b64 v10, v[198:199] offset:2592\n"
    "v_mfma_f32_16x16x32_bf16 v[44:47], a[208:211], a[112:115], v[44:47]\n"
    "s_waitcnt lgkmcnt(5)\n"
    "v_mfma_f32_16x16x32_bf16 v[44:47], a[212:215], a[116:119], v[44:47]\n"
    "v_mfma_f32_16x16x32_bf16 v[48:51], a[212:215], a[140:143], v[48:51]\n"
    "v_mfma_f32_16x16x32_bf16 v[48:51], a[208:211], a[136:139], v[48:51]\n"
    "buffer_load_dwordx4 v8, s[36:39], s56 offen offset:2048 nt lds\n"
    "v_mfma_f32_16x16x32_bf16 v[48:51], a[204:207], a[132:135], v[48:51]\n"
    "s_add_u32 m0, s54, 61440\n"
    "ds_write_b64 v10, v[228:229] offset:2624\n"
    "v_mfma_f32_16x16x32_bf16 v[52:55], a[204:207], a[156:159], v[52:55]\n"
    "v_mfma_f32_16x16x32_bf16 v[52:55], a[208:211], a[160:163], v[52:55]\n"
    "v_fma_f32 v44, v44, s52, v12\n"
    "v_fma_f32 v45, v45, s52, v12\n"
    "v_mfma_f32_16x16x32_bf16 v[52:55], a[212:215], a[164:167], v[52:55]\n"
    "v_exp_f32 v44, v44\n"
    "v_mfma_f32_16x16x32_bf16 v[56:59], a[212:215], a[188:191], v[56:59]\n"
    "ds_read_b128 a[212:215], v1 offset:30976\n"
    "v_fma_f32 v46, v46, s52, v12\n"
    "v_mfma_f32_16x16x32_bf16 v[56:59], a[208:211], a[184:187], v[56:59]\n"
    "ds_write_b64 v10, v[230:231] offset:2656\n"
    "v_mfma_f32_16x16x32_bf16 v[56:59], a[204:207], a[180:183], v[56:59]\n"
    "s_waitcnt lgkmcnt(4)\n"
    "ds_read_b128 a[204:207], v1 offset:30848\n"
    "v_fma_f32 v47, v47, s52, v12\n"
    "v_mfma_f32_16x16x32_bf16 v[60:63], a[192:195], a[96:99], 0\n"
    "ds_read_b128 a[208:211], v0 offset:30976\n"
    "v_fma_f32 v48, v48, s52, v13\n"
    "v_mfma_f32_16x16x32_bf16 v[60:63], a[196:199], a[100:103], v[60:63]\n"
    "v_exp_f32 v45, v45\n"
    "v_mfma_f32_16x16x32_bf16 v[60:63], a[200:203], a[104:107], v[60:63]\n"
    "v_exp_f32 v46, v46\n"
    "v_mfma_f32_16x16x32_bf16 v[64:67], a[200:203], a[128:131], 0\n"
    "v_exp_f32 v47, v47\n"
    "v_mfma_f32_16x16x32_bf16 v[64:67], a[196:199], a[124:127], v[64:67]\n"
    "buffer_load_dwordx4 v6, s[40:43], s55 offen offset:0 nt lds\n"
    "v_mfma_f32_16x16x32_bf16 v[64:67], a[192:195], a[120:123], v[64:67]\n"
    "v_cvt_pk_bf16_f32 v44, v44, v45\n"
    "v_fma_f32 v49, v49, s52, v13\n"
    "v_mfma_f32_16x16x32_bf16 v[68:71], a[192:195], a[144:147], 0\n"
    "v_cvt_pk_bf16_f32 v45, v46, v47\n"
    "v_fma_f32 v50, v50, s52, v13\n"
    "v_mfma_f32_16x16x32_bf16 v[68:71], a[196:199], a[148:151], v[68:71]\n"
    "ds_read_b64 v[196:197], v11 offset:2560\n"
    "v_fma_f32 v51, v51, s52, v13\n"
    "v_mfma_f32_16x16x32_bf16 v[68:71], a[200:203], a[152:155], v[68:71]\n"
    "ds_read_b64 v[198:199], v20 offset:2560\n"
    "v_fma_f32 v52, v52, s52, v14\n"
    "v_mfma_f32_16x16x32_bf16 v[72:75], a[200:203], a[176:179], 0\n"
    "ds_read_b64_tr_b16 a[200:201], v4 offset:12288\n"
    "v_fma_f32 v53, v53, s52, v14\n"
    "v_mfma_f32_16x16x32_bf16 v[72:75], a[196:199], a[172:175], v[72:75]\n"
    "ds_read_b64_tr_b16 a[196:197], v3 offset:12288\n"
    "v_fma_f32 v56, v56, s52, v15\n"
    "v_mfma_f32_16x16x32_bf16 v[72:75], a[192:195], a[168:171], v[72:75]\n"
    "s_waitcnt lgkmcnt(5)\n"
    "ds_read_b64_tr_b16 a[192:193], v2 offset:12288\n"
    "v_fma_f32 v57, v57, s52, v15\n"
    "v_mfma_f32_16x16x32_bf16 v[60:63], a[204:207], a[108:111], v[60:63]\n"
    "s_waitcnt lgkmcnt(5)\n"
    "ds_read_b64_tr_b16 a[194:195], v2 offset:18432\n"
    "v_fma_f32 v54, v54, s52, v14\n"
    "v_mfma_f32_16x16x32_bf16 v[60:63], a[208:211], a[112:115], v[60:63]\n"
    "ds_read_b64_tr_b16 a[198:199], v3 offset:18432\n"
    "v_fma_f32 v55, v55, s52, v14\n"
    "v_mfma_f32_16x16x32_bf16 v[60:63], a[212:215], a[116:119], v[60:63]\n"
    "ds_read_b64_tr_b16 a[202:203], v4 offset:18432\n"
    "v_fma_f32 v58, v58, s52, v15\n"
    "v_mfma_f32_16x16x32_bf16 v[64:67], a[212:215], a[140:143], v[64:67]\n"
    "v_exp_f32 v48, v48\n"
    "v_mfma_f32_16x16x32_bf16 v[64:67], a[208:211], a[136:139], v[64:67]\n"
    "buffer_load_dwordx4 v7, s[40:43], s55 offen offset:1024 nt lds\n"
    "v_mfma_f32_16x16x32_bf16 v[64:67], a[204:207], a[132:135], v[64:67]\n"
    "v_exp_f32 v49, v49\n"
    "v_mfma_f32_16x16x32_bf16 v[68:71], a[204:207], a[156:159], v[68:71]\n"
    "v_fma_f32 v60, v60, s52, v12\n"
    "v_fma_f32 v61, v61, s52, v12\n"
    "v_mfma_f32_16x16x32_bf16 v[68:71], a[208:211], a[160:163], v[68:71]\n"
    "v_fma_f32 v62, v62, s52, v12\n"
    "v_fma_f32 v63, v63, s52, v12\n"
    "v_mfma_f32_16x16x32_bf16 v[68:71], a[212:215], a[164:167], v[68:71]\n"
    "v_exp_f32 v60, v60\n"
    "v_mfma_f32_16x16x32_bf16 v[72:75], a[212:215], a[188:191], v[72:75]\n"
    "ds_read_b64_tr_b16 a[212:213], v3 offset:12416\n"
    "v_fma_f32 v64, v64, s52, v13\n"
    "v_mfma_f32_16x16x32_bf16 v[72:75], a[208:211], a[184:187], v[72:75]\n"
    "s_waitcnt lgkmcnt(7)\n"
    "ds_read_b64_tr_b16 a[208:209], v2 offset:12416\n"
    "v_fma_f32 v65, v65, s52, v13\n"
    "v_mfma_f32_16x16x32_bf16 v[72:75], a[204:207], a[180:183], v[72:75]\n"
    "buffer_store_dwordx4 v[196:199], v9, s[48:51], s73 offen offset:256 sc1\n"
    "v_smfmac_f32_16x16x64_bf16 a[244:247], a[252:255], v[28:35], v23\n"
    "ds_read_b64_tr_b16 a[204:205], v5 offset:12288\n"
    "v_fma_f32 v66, v66, s52, v13\n"
    "v_smfmac_f32_16x16x64_bf16 a[248:251], a[252:255], v[36:43], v23\n"
    "s_waitcnt lgkmcnt(5)\n"
    "ds_read_b64_tr_b16 a[206:207], v5 offset:18432\n"
    "v_fma_f32 v67, v67, s52, v13\n"
    "v_mfma_f32_16x16x32_bf16 v[104:107], a[192:195], v[40:43], v[104:107]\n"
    "ds_read_b64_tr_b16 a[210:211], v2 offset:18560\n"
    "v_cvt_pk_bf16_f32 v48, v48, v49\n"
    "v_mfma_f32_16x16x32_bf16 v[88:91], a[192:195], v[36:39], v[88:91]\n"
    "ds_read_b64_tr_b16 a[214:215], v3 offset:18560\n"
    "v_fma_f32 v59, v59, s52, v15\n"
    "v_mfma_f32_16x16x32_bf16 v[100:103], a[192:195], v[32:35], v[100:103]\n"
    "buffer_load_dwordx4 v8, s[40:43], s55 offen offset:2048 nt lds\n"
    "v_mfma_f32_16x16x32_bf16 v[84:87], a[192:195], v[28:31], v[84:87]\n"
    "s_waitcnt lgkmcnt(7)\n"
    "ds_read_b64_tr_b16 a[192:193], v4 offset:12416\n"
    "v_fma_f32 v68, v68, s52, v14\n"
    "v_mfma_f32_16x16x32_bf16 v[92:95], a[196:199], v[28:31], v[92:95]\n"
    "ds_read_b64_tr_b16 a[194:195], v4 offset:18560\n"
    "v_fma_f32 v69, v69, s52, v14\n"
    "v_mfma_f32_16x16x32_bf16 v[108:111], a[196:199], v[32:35], v[108:111]\n"
    "v_exp_f32 v61, v61\n"
    "v_mfma_f32_16x16x32_bf16 v[96:99], a[196:199], v[36:39], v[96:99]\n"
    "v_exp_f32 v62, v62\n"
    "v_mfma_f32_16x16x32_bf16 v[112:115], a[196:199], v[40:43], v[112:115]\n"
    "s_waitcnt lgkmcnt(8)\n"
    "ds_read_b64_tr_b16 a[196:197], v5 offset:12416\n"
    "v_cvt_pk_bf16_f32 v46, v60, v61\n"
    "v_mfma_f32_16x16x32_bf16 v[164:167], a[200:203], v[40:43], v[164:167]\n"
    "ds_read_b64_tr_b16 a[198:199], v5 offset:18560\n"
    "v_fma_f32 v70, v70, s52, v14\n"
    "v_mfma_f32_16x16x32_bf16 v[136:139], a[200:203], v[36:39], v[136:139]\n"
    "v_exp_f32 v63, v63\n"
    "v_mfma_f32_16x16x32_bf16 v[160:163], a[200:203], v[32:35], v[160:163]\n"
    "v_cvt_pk_bf16_f32 v47, v62, v63\n"
    "v_fma_f32 v71, v71, s52, v14\n"
    "v_mfma_f32_16x16x32_bf16 v[132:135], a[200:203], v[28:31], v[132:135]\n"
    "s_waitcnt lgkmcnt(6)\n"
    "ds_read_b64_tr_b16 a[200:201], v2 offset:12544\n"
    "v_fma_f32 v72, v72, s52, v15\n"
    "v_mfma_f32_16x16x32_bf16 v[140:143], a[204:207], v[28:31], v[140:143]\n"
    "ds_read_b64_tr_b16 a[202:203], v2 offset:18688\n"
    "v_fma_f32 v73, v73, s52, v15\n"
    "v_mfma_f32_16x16x32_bf16 v[168:171], a[204:207], v[32:35], v[168:171]\n"
    "v_exp_f32 v50, v50\n"
    "v_mfma_f32_16x16x32_bf16 v[144:147], a[204:207], v[36:39], v[144:147]\n"
    "v_exp_f32 v51, v51\n"
    "v_mfma_f32_16x16x32_bf16 v[172:175], a[204:207], v[40:43], v[172:175]\n"
    "s_waitcnt lgkmcnt(7)\n"
    "ds_read_b64_tr_b16 a[204:205], v3 offset:12544\n"
    "v_cvt_pk_bf16_f32 v49, v50, v51\n"
    "v_mfma_f32_16x16x32_bf16 a[76:79], a[208:211], v[40:43], a[76:79]\n"
    "ds_read_b64_tr_b16 a[206:207], v3 offset:18688\n"
    "v_fma_f32 v74, v74, s52, v15\n"
    "v_mfma_f32_16x16x32_bf16 a[72:75], a[208:211], v[36:39], a[72:75]\n"
    "v_exp_f32 v64, v64\n"
    "v_mfma_f32_16x16x32_bf16 v[224:227], a[208:211], v[32:35], v[224:227]\n"
    "v_exp_f32 v65, v65\n"
    "v_mfma_f32_16x16x32_bf16 v[192:195], a[208:211], v[28:31], v[192:195]\n"
    "s_waitcnt lgkmcnt(8)\n"
    "ds_read_b64_tr_b16 a[208:209], v4 offset:12544\n"
    "v_cvt_pk_bf16_f32 v50, v64, v65\n"
    "v_mfma_f32_16x16x32_bf16 a[80:83], a[212:215], v[28:31], a[80:83]\n"
    "ds_read_b64_tr_b16 a[210:211], v4 offset:18688\n"
    "v_fma_f32 v75, v75, s52, v15\n"
    "v_mfma_f32_16x16x32_bf16 a[84:87], a[212:215], v[32:35], a[84:87]\n"
    "v_exp_f32 v66, v66\n"
    "v_mfma_f32_16x16x32_bf16 a[88:91], a[212:215], v[36:39], a[88:91]\n"
    "v_exp_f32 v67, v67\n"
    "v_mfma_f32_16x16x32_bf16 a[92:95], a[212:215], v[40:43], a[92:95]\n"
    "s_waitcnt lgkmcnt(8)\n"
    "ds_read_b64_tr_b16 a[212:213], v5 offset:12544\n"
    "v_cvt_pk_bf16_f32 v51, v66, v67\n"
    "v_mfma_f32_16x16x32_bf16 v[128:131], a[192:195], v[40:43], v[128:131]\n"
    "ds_read_b64_tr_b16 a[214:215], v5 offset:18688\n"
    "v_accvgpr_read_b32 v196, a72\n"
    "v_mfma_f32_16x16x32_bf16 v[124:127], a[192:195], v[36:39], v[124:127]\n"
    "v_exp_f32 v52, v52\n"
    "v_mfma_f32_16x16x32_bf16 v[120:123], a[192:195], v[32:35], v[120:123]\n"
    "v_exp_f32 v53, v53\n"
    "v_mfma_f32_16x16x32_bf16 v[116:119], a[192:195], v[28:31], v[116:119]\n"
    "s_waitcnt lgkmcnt(8)\n"
    "v_exp_f32 v56, v56\n"
    "v_mfma_f32_16x16x32_bf16 a[216:219], a[196:199], v[28:31], a[216:219]\n"
    "v_exp_f32 v57, v57\n"
    "v_mfma_f32_16x16x32_bf16 a[220:223], a[196:199], v[32:35], a[220:223]\n"
    "v_exp_f32 v54, v54\n"
    "v_mfma_f32_16x16x32_bf16 a[224:227], a[196:199], v[36:39], a[224:227]\n"
    "s_waitcnt vmcnt(24) lgkmcnt(0)\n"
    "v_exp_f32 v55, v55\n"
    "v_mfma_f32_16x16x32_bf16 a[228:231], a[196:199], v[40:43], a[228:231]\n"
    "s_barrier\n"
    "ds_read_b64 v[228:229], v11 offset:2880\n"
    "v_cvt_pk_bf16_f32 v52, v52, v53\n"
    "v_mfma_f32_16x16x32_bf16 v[188:191], a[200:203], v[40:43], v[188:191]\n"
    "s_add_u32 m0, s54, 73728\n"
    "ds_read_b64 v[230:231], v20 offset:2880\n"
    "v_cvt_pk_bf16_f32 v56, v56, v57\n"
    "v_mfma_f32_16x16x32_bf16 v[184:187], a[200:203], v[36:39], v[184:187]\n"
    "ds_read_b128 a[192:195], v0 offset:36864\n"
    "v_cvt_pk_bf16_f32 v53, v54, v55\n"
    "v_mfma_f32_16x16x32_bf16 v[180:183], a[200:203], v[32:35], v[180:183]\n"
    "ds_read_b128 a[196:199], v1 offset:36864\n"
    "v_accvgpr_read_b32 v197, a73\n"
    "v_mfma_f32_16x16x32_bf16 v[176:179], a[200:203], v[28:31], v[176:179]\n"
    "ds_read_b128 a[200:203], v0 offset:36992\n"
    "v_accvgpr_read_b32 v198, a74\n"
    "v_mfma_f32_16x16x32_bf16 a[232:235], a[204:207], v[28:31], a[232:235]\n"
    "v_exp_f32 v58, v58\n"
    "v_mfma_f32_16x16x32_bf16 v[148:151], a[204:207], v[32:35], v[148:151]\n"
    "v_exp_f32 v59, v59\n"
    "v_mfma_f32_16x16x32_bf16 v[152:155], a[204:207], v[36:39], v[152:155]\n"
    "v_exp_f32 v68, v68\n"
    "v_mfma_f32_16x16x32_bf16 v[156:159], a[204:207], v[40:43], v[156:159]\n"
    "ds_read_b128 a[204:207], v1 offset:36992\n"
    "v_cvt_pk_bf16_f32 v57, v58, v59\n"
    "v_mfma_f32_16x16x32_bf16 v[220:223], a[208:211], v[40:43], v[220:223]\n"
    "v_exp_f32 v69, v69\n"
    "v_mfma_f32_16x16x32_bf16 v[216:219], a[208:211], v[36:39], v[216:219]\n"
    "v_exp_f32 v70, v70\n"
    "v_mfma_f32_16x16x32_bf16 v[212:215], a[208:211], v[32:35], v[212:215]\n"
    "v_exp_f32 v71, v71\n"
    "v_mfma_f32_16x16x32_bf16 v[208:211], a[208:211], v[28:31], v[208:211]\n"
    "ds_read_b128 a[208:211], v0 offset:37120\n"
    "v_cvt_pk_bf16_f32 v54, v68, v69\n"
    "v_mfma_f32_16x16x32_bf16 v[240:243], a[212:215], v[28:31], v[240:243]\n"
    "v_exp_f32 v72, v72\n"
    "v_mfma_f32_16x16x32_bf16 v[244:247], a[212:215], v[32:35], v[244:247]\n"
    "v_exp_f32 v73, v73\n"
    "v_mfma_f32_16x16x32_bf16 v[248:251], a[212:215], v[36:39], v[248:251]\n"
    "s_waitcnt lgkmcnt(5)\n"
    "v_exp_f32 v74, v74\n"
    "v_mfma_f32_16x16x32_bf16 v[252:255], a[212:215], v[40:43], v[252:255]\n"
    "s_waitcnt lgkmcnt(4)\n"
    "buffer_store_dwordx4 v[228:231], v9, s[48:51], s73 offen offset:1024 sc1\n"
    "v_mfma_f32_16x16x32_bf16 v[28:31], a[192:195], a[96:99], 0\n"
    "s_waitcnt lgkmcnt(3)\n"
    "ds_read_b128 a[212:215], v1 offset:37120\n"
    "v_cvt_pk_bf16_f32 v55, v70, v71\n"
    "v_mfma_f32_16x16x32_bf16 v[28:31], a[196:199], a[100:103], v[28:31]\n"
    "s_waitcnt lgkmcnt(3)\n"
    "v_exp_f32 v75, v75\n"
    "v_mfma_f32_16x16x32_bf16 v[28:31], a[200:203], a[104:107], v[28:31]\n"
    "v_cvt_pk_bf16_f32 v58, v72, v73\n"
    "v_cvt_pk_bf16_f32 v59, v74, v75\n"
    "v_mfma_f32_16x16x32_bf16 v[32:35], a[200:203], a[128:131], 0\n"
    "v_accvgpr_read_b32 v228, a76\n"
    "v_accvgpr_read_b32 v229, a77\n"
    "v_mfma_f32_16x16x32_bf16 v[32:35], a[196:199], a[124:127], v[32:35]\n"
    "v_accvgpr_read_b32 v230, a78\n"
    "v_accvgpr_read_b32 v231, a79\n"
    "v_mfma_f32_16x16x32_bf16 v[32:35], a[192:195], a[120:123], v[32:35]\n"
    "v_accvgpr_read_b32 v199, a75\n"
    "v_mfma_f32_16x16x32_bf16 v[36:39], a[192:195], a[144:147], 0\n"
    "v_mfma_f32_16x16x32_bf16 v[36:39], a[196:199], a[148:151], v[36:39]\n"
    "v_mfma_f32_16x16x32_bf16 v[36:39], a[200:203], a[152:155], v[36:39]\n"
    "v_mfma_f32_16x16x32_bf16 v[40:43], a[200:203], a[176:179], 0\n"
    "ds_write_b64 v10, v[200:201] offset:5120\n"
    "v_mfma_f32_16x16x32_bf16 v[40:43], a[196:199], a[172:175], v[40:43]\n"
    "s_nop 0\n"
    "ds_read_b128 a[196:199], v1 offset:43008\n"
    "ds_read_b128 a[200:203], v0 offset:43136\n"
    "v_mfma_f32_16x16x32_bf16 v[40:43], a[192:195], a[168:171], v[40:43]\n"
    "s_waitcnt lgkmcnt(5)\n"
    "ds_read_b128 a[192:195], v0 offset:43008\n"
    "v_mfma_f32_16x16x32_bf16 v[28:31], a[204:207], a[108:111], v[28:31]\n"
    "s_waitcnt lgkmcnt(5)\n"
    "v_mfma_f32_16x16x32_bf16 v[28:31], a[208:211], a[112:115], v[28:31]\n"
    "s_waitcnt lgkmcnt(4)\n"
    "v_mfma_f32_16x16x32_bf16 v[28:31], a[212:215], a[116:119], v[28:31]\n"
    "ds_write_b64 v10, v[202:203] offset:5152\n"
    "v_mfma_f32_16x16x32_bf16 v[32:35], a[212:215], a[140:143], v[32:35]\n"
    "v_mfma_f32_16x16x32_bf16 v[32:35], a[208:211], a[136:139], v[32:35]\n"
    "v_mfma_f32_16x16x32_bf16 v[32:35], a[204:207], a[132:135], v[32:35]\n"
    "v_mfma_f32_16x16x32_bf16 v[36:39], a[204:207], a[156:159], v[36:39]\n"
    "v_mfma_f32_16x16x32_bf16 v[36:39], a[208:211], a[160:163], v[36:39]\n"
    "ds_write_b64 v10, v[232:233] offset:5184\n"
    "v_mfma_f32_16x16x32_bf16 v[36:39], a[212:215], a[164:167], v[36:39]\n"
    "v_fma_f32 v28, v28, s52, v12\n"
    "v_fma_f32 v29, v29, s52, v12\n"
    "v_mfma_f32_16x16x32_bf16 v[40:43], a[212:215], a[188:191], v[40:43]\n"
    "ds_read_b128 a[212:215], v1 offset:43264\n"
    "v_fma_f32 v30, v30, s52, v12\n"
    "v_mfma_f32_16x16x32_bf16 v[40:43], a[208:211], a[184:187], v[40:43]\n"
    "ds_read_b128 a[208:211], v0 offset:43264\n"
    "v_fma_f32 v31, v31, s52, v12\n"
    "v_mfma_f32_16x16x32_bf16 v[40:43], a[204:207], a[180:183], v[40:43]\n"
    "s_waitcnt lgkmcnt(4)\n"
    "ds_read_b128 a[204:207], v1 offset:43136\n"
    "v_fma_f32 v32, v32, s52, v13\n"
    "v_mfma_f32_16x16x32_bf16 v[76:79], a[192:195], a[96:99], 0\n"
    "ds_write_b64 v10, v[234:235] offset:5216\n"
    "v_mfma_f32_16x16x32_bf16 v[76:79], a[196:199], a[100:103], v[76:79]\n"
    "v_exp_f32 v28, v28\n"
    "v_mfma_f32_16x16x32_bf16 v[76:79], a[200:203], a[104:107], v[76:79]\n"
    "v_exp_f32 v29, v29\n"
    "v_mfma_f32_16x16x32_bf16 v[80:83], a[200:203], a[128:131], 0\n"
    "v_exp_f32 v30, v30\n"
    "v_mfma_f32_16x16x32_bf16 v[80:83], a[196:199], a[124:127], v[80:83]\n"
    "buffer_load_dwordx4 v6, s[40:43], s56 offen offset:0 nt lds\n"
    "v_mfma_f32_16x16x32_bf16 v[80:83], a[192:195], a[120:123], v[80:83]\n"
    "v_exp_f32 v31, v31\n"
    "v_mfma_f32_16x16x32_bf16 v[60:63], a[192:195], a[144:147], 0\n"
    "v_cvt_pk_bf16_f32 v28, v28, v29\n"
    "v_fma_f32 v33, v33, s52, v13\n"
    "v_mfma_f32_16x16x32_bf16 v[60:63], a[196:199], a[148:151], v[60:63]\n"
    "v_cvt_pk_bf16_f32 v29, v30, v31\n"
    "v_fma_f32 v34, v34, s52, v13\n"
    "v_mfma_f32_16x16x32_bf16 v[60:63], a[200:203], a[152:155], v[60:63]\n"
    "v_exp_f32 v32, v32\n"
    "v_mfma_f32_16x16x32_bf16 v[64:67], a[200:203], a[176:179], 0\n"
    "ds_read_b64 v[200:201], v11 offset:5120\n"
    "v_fma_f32 v35, v35, s52, v13\n"
    "v_mfma_f32_16x16x32_bf16 v[64:67], a[196:199], a[172:175], v[64:67]\n"
    "ds_read_b64 v[202:203], v20 offset:5120\n"
    "v_fma_f32 v36, v36, s52, v14\n"
    "v_mfma_f32_16x16x32_bf16 v[64:67], a[192:195], a[168:171], v[64:67]\n"
    "s_waitcnt lgkmcnt(3)\n"
    "ds_read_b64_tr_b16 a[192:193], v2 offset:24576\n"
    "v_fma_f32 v37, v37, s52, v14\n"
    "v_mfma_f32_16x16x32_bf16 v[76:79], a[204:207], a[108:111], v[76:79]\n"
    "ds_read_b64_tr_b16 a[194:195], v2 offset:30720\n"
    "v_fma_f32 v40, v40, s52, v15\n"
    "v_mfma_f32_16x16x32_bf16 v[76:79], a[208:211], a[112:115], v[76:79]\n"
    "ds_read_b64_tr_b16 a[196:197], v3 offset:24576\n"
    "v_fma_f32 v41, v41, s52, v15\n"
    "v_mfma_f32_16x16x32_bf16 v[76:79], a[212:215], a[116:119], v[76:79]\n"
    "ds_read_b64_tr_b16 a[198:199], v3 offset:30720\n"
    "v_fma_f32 v38, v38, s52, v14\n"
    "v_mfma_f32_16x16x32_bf16 v[80:83], a[212:215], a[140:143], v[80:83]\n"
    "ds_read_b64_tr_b16 a[200:201], v4 offset:24576\n"
    "v_fma_f32 v39, v39, s52, v14\n"
    "v_mfma_f32_16x16x32_bf16 v[80:83], a[208:211], a[136:139], v[80:83]\n"
    "buffer_load_dwordx4 v7, s[40:43], s56 offen offset:1024 nt lds\n"
    "v_mfma_f32_16x16x32_bf16 v[80:83], a[204:207], a[132:135], v[80:83]\n"
    "ds_read_b64_tr_b16 a[202:203], v4 offset:30720\n"
    "v_fma_f32 v76, v76, s52, v12\n"
    "v_mfma_f32_16x16x32_bf16 v[60:63], a[204:207], a[156:159], v[60:63]\n"
    "v_fma_f32 v77, v77, s52, v12\n"
    "v_fma_f32 v78, v78, s52, v12\n"
    "v_mfma_f32_16x16x32_bf16 v[60:63], a[208:211], a[160:163], v[60:63]\n"
    "v_fma_f32 v79, v79, s52, v12\n"
    "v_fma_f32 v42, v42, s52, v15\n"
    "v_mfma_f32_16x16x32_bf16 v[60:63], a[212:215], a[164:167], v[60:63]\n"
    "v_exp_f32 v76, v76\n"
    "v_mfma_f32_16x16x32_bf16 v[64:67], a[212:215], a[188:191], v[64:67]\n"
    "ds_read_b64_tr_b16 a[212:213], v3 offset:24704\n"
    "v_fma_f32 v80, v80, s52, v13\n"
    "v_mfma_f32_16x16x32_bf16 v[64:67], a[208:211], a[184:187], v[64:67]\n"
    "ds_read_b64_tr_b16 a[208:209], v2 offset:24704\n"
    "v_fma_f32 v81, v81, s52, v13\n"
    "v_mfma_f32_16x16x32_bf16 v[64:67], a[204:207], a[180:183], v[64:67]\n"
    "ds_read_b64_tr_b16 a[204:205], v5 offset:24576\n"
    "v_fma_f32 v82, v82, s52, v13\n"
    "v_smfmac_f32_16x16x64_bf16 a[244:247], a[252:255], v[44:51], v23\n"
    "s_waitcnt lgkmcnt(9)\n"
    "ds_read_b64_tr_b16 a[206:207], v5 offset:30720\n"
    "v_fma_f32 v83, v83, s52, v13\n"
    "v_smfmac_f32_16x16x64_bf16 a[248:251], a[252:255], v[52:59], v23\n"
    "s_waitcnt lgkmcnt(8)\n"
    "buffer_store_dwordx4 v[200:203], v9, s[48:51], s74 offen offset:256 sc1\n"
    "v_mfma_f32_16x16x32_bf16 v[104:107], a[192:195], v[56:59], v[104:107]\n"
    "ds_read_b64_tr_b16 a[210:211], v2 offset:30848\n"
    "v_fma_f32 v43, v43, s52, v15\n"
    "v_mfma_f32_16x16x32_bf16 v[88:91], a[192:195], v[52:55], v[88:91]\n"
    "ds_read_b64_tr_b16 a[214:215], v3 offset:30848\n"
    "v_fma_f32 v60, v60, s52, v14\n"
    "v_mfma_f32_16x16x32_bf16 v[100:103], a[192:195], v[48:51], v[100:103]\n"
    "buffer_load_dwordx4 v8, s[40:43], s56 offen offset:2048 nt lds\n"
    "v_mfma_f32_16x16x32_bf16 v[84:87], a[192:195], v[44:47], v[84:87]\n"
    "s_waitcnt lgkmcnt(8)\n"
    "ds_read_b64_tr_b16 a[192:193], v4 offset:24704\n"
    "v_fma_f32 v61, v61, s52, v14\n"
    "v_mfma_f32_16x16x32_bf16 v[92:95], a[196:199], v[44:47], v[92:95]\n"
    "ds_read_b64_tr_b16 a[194:195], v4 offset:30848\n"
    "v_fma_f32 v62, v62, s52, v14\n"
    "v_mfma_f32_16x16x32_bf16 v[108:111], a[196:199], v[48:51], v[108:111]\n"
    "v_exp_f32 v77, v77\n"
    "v_mfma_f32_16x16x32_bf16 v[96:99], a[196:199], v[52:55], v[96:99]\n"
    "v_exp_f32 v78, v78\n"
    "v_mfma_f32_16x16x32_bf16 v[112:115], a[196:199], v[56:59], v[112:115]\n"
    "s_waitcnt lgkmcnt(8)\n"
    "ds_read_b64_tr_b16 a[196:197], v5 offset:24704\n"
    "v_cvt_pk_bf16_f32 v30, v76, v77\n"
    "v_mfma_f32_16x16x32_bf16 v[164:167], a[200:203], v[56:59], v[164:167]\n"
    "ds_read_b64_tr_b16 a[198:199], v5 offset:30848\n"
    "v_fma_f32 v63, v63, s52, v14\n"
    "v_mfma_f32_16x16x32_bf16 v[136:139], a[200:203], v[52:55], v[136:139]\n"
    "v_exp_f32 v79, v79\n"
    "v_mfma_f32_16x16x32_bf16 v[160:163], a[200:203], v[48:51], v[160:163]\n"
    "v_cvt_pk_bf16_f32 v31, v78, v79\n"
    "v_fma_f32 v64, v64, s52, v15\n"
    "v_mfma_f32_16x16x32_bf16 v[132:135], a[200:203], v[44:47], v[132:135]\n"
    "s_waitcnt lgkmcnt(6)\n"
    "ds_read_b64_tr_b16 a[200:201], v2 offset:24832\n"
    "v_fma_f32 v65, v65, s52, v15\n"
    "v_mfma_f32_16x16x32_bf16 v[140:143], a[204:207], v[44:47], v[140:143]\n"
    "ds_read_b64_tr_b16 a[202:203], v2 offset:30976\n"
    "v_fma_f32 v66, v66, s52, v15\n"
    "v_mfma_f32_16x16x32_bf16 v[168:171], a[204:207], v[48:51], v[168:171]\n"
    "v_exp_f32 v33, v33\n"
    "v_mfma_f32_16x16x32_bf16 v[144:147], a[204:207], v[52:55], v[144:147]\n"
    "v_exp_f32 v34, v34\n"
    "v_mfma_f32_16x16x32_bf16 v[172:175], a[204:207], v[56:59], v[172:175]\n"
    "s_waitcnt lgkmcnt(7)\n"
    "ds_read_b64_tr_b16 a[204:205], v3 offset:24832\n"
    "v_cvt_pk_bf16_f32 v32, v32, v33\n"
    "v_mfma_f32_16x16x32_bf16 v[228:231], a[208:211], v[56:59], v[228:231]\n"
    "ds_read_b64_tr_b16 a[206:207], v3 offset:30976\n"
    "v_fma_f32 v67, v67, s52, v15\n"
    "v_mfma_f32_16x16x32_bf16 v[196:199], a[208:211], v[52:55], v[196:199]\n"
    "v_exp_f32 v35, v35\n"
    "v_mfma_f32_16x16x32_bf16 v[224:227], a[208:211], v[48:51], v[224:227]\n"
    "v_exp_f32 v80, v80\n"
    "v_mfma_f32_16x16x32_bf16 v[192:195], a[208:211], v[44:47], v[192:195]\n"
    "s_waitcnt lgkmcnt(8)\n"
    "ds_read_b64_tr_b16 a[208:209], v4 offset:24832\n"
    "v_cvt_pk_bf16_f32 v33, v34, v35\n"
    "v_mfma_f32_16x16x32_bf16 a[80:83], a[212:215], v[44:47], a[80:83]\n"
    "v_exp_f32 v81, v81\n"
    "v_mfma_f32_16x16x32_bf16 a[84:87], a[212:215], v[48:51], a[84:87]\n"
    "ds_read_b64_tr_b16 a[210:211], v4 offset:30976\n"
    "v_cvt_pk_bf16_f32 v34, v80, v81\n"
    "v_mfma_f32_16x16x32_bf16 a[88:91], a[212:215], v[52:55], a[88:91]\n"
    "v_exp_f32 v82, v82\n"
    "v_mfma_f32_16x16x32_bf16 a[92:95], a[212:215], v[56:59], a[92:95]\n"
    "s_waitcnt lgkmcnt(8)\n"
    "ds_read_b64_tr_b16 a[212:213], v5 offset:24832\n"
    "v_accvgpr_read_b32 v200, a80\n"
    "v_mfma_f32_16x16x32_bf16 v[128:131], a[192:195], v[56:59], v[128:131]\n"
    "ds_read_b64_tr_b16 a[214:215], v5 offset:30976\n"
    "v_accvgpr_read_b32 v201, a81\n"
    "v_mfma_f32_16x16x32_bf16 v[124:127], a[192:195], v[52:55], v[124:127]\n"
    "v_exp_f32 v83, v83\n"
    "v_mfma_f32_16x16x32_bf16 v[120:123], a[192:195], v[48:51], v[120:123]\n"
    "v_cvt_pk_bf16_f32 v35, v82, v83\n"
    "v_accvgpr_read_b32 v202, a82\n"
    "v_mfma_f32_16x16x32_bf16 v[116:119], a[192:195], v[44:47], v[116:119]\n"
    "s_waitcnt lgkmcnt(8)\n"
    "v_exp_f32 v36, v36\n"
    "v_mfma_f32_16x16x32_bf16 a[216:219], a[196:199], v[44:47], a[216:219]\n"
    "v_exp_f32 v37, v37\n"
    "v_mfma_f32_16x16x32_bf16 a[220:223], a[196:199], v[48:51], a[220:223]\n"
    "v_exp_f32 v40, v40\n"
    "v_mfma_f32_16x16x32_bf16 a[224:227], a[196:199], v[52:55], a[224:227]\n"
    "s_waitcnt vmcnt(21) lgkmcnt(0)\n"
    "v_exp_f32 v41, v41\n"
    "v_mfma_f32_16x16x32_bf16 a[228:231], a[196:199], v[56:59], a[228:231]\n"
    "s_barrier\n"
    "ds_read_b128 a[192:195], v0 offset:49152\n"
    "v_cvt_pk_bf16_f32 v36, v36, v37\n"
    "v_mfma_f32_16x16x32_bf16 v[188:191], a[200:203], v[56:59], v[188:191]\n"
    "ds_read_b128 a[196:199], v1 offset:49152\n"
    "v_cvt_pk_bf16_f32 v40, v40, v41\n"
    "v_mfma_f32_16x16x32_bf16 v[184:187], a[200:203], v[52:55], v[184:187]\n"
    "ds_read_b64 v[232:233], v11 offset:5440\n"
    "v_accvgpr_read_b32 v203, a83\n"
    "v_mfma_f32_16x16x32_bf16 v[180:183], a[200:203], v[48:51], v[180:183]\n"
    "v_exp_f32 v38, v38\n"
    "v_mfma_f32_16x16x32_bf16 v[176:179], a[200:203], v[44:47], v[176:179]\n"
    "s_nop 0\n"
    "ds_read_b64 v[234:235], v20 offset:5440\n"
    "ds_read_b128 a[200:203], v0 offset:49280\n"
    "v_mfma_f32_16x16x32_bf16 a[232:235], a[204:207], v[44:47], a[232:235]\n"
    "v_exp_f32 v39, v39\n"
    "v_mfma_f32_16x16x32_bf16 v[148:151], a[204:207], v[48:51], v[148:151]\n"
    "v_exp_f32 v42, v42\n"
    "v_mfma_f32_16x16x32_bf16 v[152:155], a[204:207], v[52:55], v[152:155]\n"
    "v_exp_f32 v43, v43\n"
    "v_mfma_f32_16x16x32_bf16 v[156:159], a[204:207], v[56:59], v[156:159]\n"
    "ds_read_b128 a[204:207], v1 offset:49280\n"
    "v_cvt_pk_bf16_f32 v37, v38, v39\n"
    "v_mfma_f32_16x16x32_bf16 v[220:223], a[208:211], v[56:59], v[220:223]\n"
    "v_exp_f32 v60, v60\n"
    "v_mfma_f32_16x16x32_bf16 v[216:219], a[208:211], v[52:55], v[216:219]\n"
    "v_exp_f32 v61, v61\n"
    "v_mfma_f32_16x16x32_bf16 v[212:215], a[208:211], v[48:51], v[212:215]\n"
    "v_exp_f32 v62, v62\n"
    "v_mfma_f32_16x16x32_bf16 v[208:211], a[208:211], v[44:47], v[208:211]\n"
    "ds_read_b128 a[208:211], v0 offset:49408\n"
    "v_cvt_pk_bf16_f32 v41, v42, v43\n"
    "v_mfma_f32_16x16x32_bf16 v[240:243], a[212:215], v[44:47], v[240:243]\n"
    "v_exp_f32 v63, v63\n"
    "v_mfma_f32_16x16x32_bf16 v[244:247], a[212:215], v[48:51], v[244:247]\n"
    "v_exp_f32 v64, v64\n"
    "v_mfma_f32_16x16x32_bf16 v[248:251], a[212:215], v[52:55], v[248:251]\n"
    "v_exp_f32 v65, v65\n"
    "v_mfma_f32_16x16x32_bf16 v[252:255], a[212:215], v[56:59], v[252:255]\n"
    "s_waitcnt lgkmcnt(6)\n"
    "ds_read_b128 a[212:215], v1 offset:49408\n"
    "v_cvt_pk_bf16_f32 v38, v60, v61\n"
    "v_mfma_f32_16x16x32_bf16 v[44:47], a[192:195], a[96:99], 0\n"
    "s_waitcnt lgkmcnt(4)\n"
    "v_exp_f32 v66, v66\n"
    "v_mfma_f32_16x16x32_bf16 v[44:47], a[196:199], a[100:103], v[44:47]\n"
    "s_waitcnt lgkmcnt(3)\n"
    "buffer_store_dwordx4 v[232:235], v9, s[48:51], s74 offen offset:1024 sc1\n"
    "v_mfma_f32_16x16x32_bf16 v[44:47], a[200:203], a[104:107], v[44:47]\n"
    "v_exp_f32 v67, v67\n"
    "v_mfma_f32_16x16x32_bf16 v[48:51], a[200:203], a[128:131], 0\n"
    "v_cvt_pk_bf16_f32 v39, v62, v63\n"
    "v_cvt_pk_bf16_f32 v42, v64, v65\n"
    "v_mfma_f32_16x16x32_bf16 v[48:51], a[196:199], a[124:127], v[48:51]\n"
    "v_cvt_pk_bf16_f32 v43, v66, v67\n"
    "v_accvgpr_read_b32 v232, a84\n"
    "v_mfma_f32_16x16x32_bf16 v[48:51], a[192:195], a[120:123], v[48:51]\n"
    "v_accvgpr_read_b32 v233, a85\n"
    "v_accvgpr_read_b32 v234, a86\n"
    "v_mfma_f32_16x16x32_bf16 v[52:55], a[192:195], a[144:147], 0\n"
    "v_accvgpr_read_b32 v235, a87\n"
    "v_mfma_f32_16x16x32_bf16 v[52:55], a[196:199], a[148:151], v[52:55]\n"
    "v_mfma_f32_16x16x32_bf16 v[52:55], a[200:203], a[152:155], v[52:55]\n"
    "v_mfma_f32_16x16x32_bf16 v[56:59], a[200:203], a[176:179], 0\n"
    "ds_read_b128 a[200:203], v0 offset:55424\n"
    "v_mfma_f32_16x16x32_bf16 v[56:59], a[196:199], a[172:175], v[56:59]\n"
    "ds_read_b128 a[196:199], v1 offset:55296\n"
    "v_mfma_f32_16x16x32_bf16 v[56:59], a[192:195], a[168:171], v[56:59]\n"
    "s_waitcnt lgkmcnt(4)\n"
    "ds_write_b64 v10, v[204:205] offset:7680\n"
    "v_mfma_f32_16x16x32_bf16 v[44:47], a[204:207], a[108:111], v[44:47]\n"
    "s_waitcnt lgkmcnt(4)\n"
    "ds_read_b128 a[192:195], v0 offset:55296\n"
    "v_mfma_f32_16x16x32_bf16 v[44:47], a[208:211], a[112:115], v[44:47]\n"
    "s_waitcnt lgkmcnt(4)\n"
    "v_mfma_f32_16x16x32_bf16 v[44:47], a[212:215], a[116:119], v[44:47]\n"
    "v_mfma_f32_16x16x32_bf16 v[48:51], a[212:215], a[140:143], v[48:51]\n"
    "v_mfma_f32_16x16x32_bf16 v[48:51], a[208:211], a[136:139], v[48:51]\n"
    "ds_write_b64 v10, v[206:207] offset:7712\n"
    "v_mfma_f32_16x16x32_bf16 v[48:51], a[204:207], a[132:135], v[48:51]\n"
    "v_mfma_f32_16x16x32_bf16 v[52:55], a[204:207], a[156:159], v[52:55]\n"
    "v_mfma_f32_16x16x32_bf16 v[52:55], a[208:211], a[160:163], v[52:55]\n"
    "v_mfma_f32_16x16x32_bf16 v[52:55], a[212:215], a[164:167], v[52:55]\n"
    "v_mfma_f32_16x16x32_bf16 v[56:59], a[212:215], a[188:191], v[56:59]\n"
    "ds_write_b64 v10, v[236:237] offset:7744\n"
    "v_mfma_f32_16x16x32_bf16 v[56:59], a[208:211], a[184:187], v[56:59]\n"
    "ds_read_b128 a[208:211], v0 offset:55552\n"
    "v_fma_f32 v44, v44, s52, v12\n"
    "v_mfma_f32_16x16x32_bf16 v[56:59], a[204:207], a[180:183], v[56:59]\n"
    "s_waitcnt lgkmcnt(3)\n"
    "ds_read_b128 a[204:207], v1 offset:55424\n"
    "v_fma_f32 v45, v45, s52, v12\n"
    "v_mfma_f32_16x16x32_bf16 v[68:71], a[192:195], a[96:99], 0\n"
    "ds_read_b128 a[212:215], v1 offset:55552\n"
    "v_fma_f32 v46, v46, s52, v12\n"
    "v_mfma_f32_16x16x32_bf16 v[68:71], a[196:199], a[100:103], v[68:71]\n"
    "v_exp_f32 v44, v44\n"
    "v_mfma_f32_16x16x32_bf16 v[68:71], a[200:203], a[104:107], v[68:71]\n"
    "ds_write_b64 v10, v[238:239] offset:7776\n"
    "v_mfma_f32_16x16x32_bf16 v[72:75], a[200:203], a[128:131], 0\n"
    "v_exp_f32 v45, v45\n"
    "v_mfma_f32_16x16x32_bf16 v[72:75], a[196:199], a[124:127], v[72:75]\n"
    "v_fma_f32 v47, v47, s52, v12\n"
    "v_cvt_pk_bf16_f32 v44, v44, v45\n"
    "v_mfma_f32_16x16x32_bf16 v[72:75], a[192:195], a[120:123], v[72:75]\n"
    "v_exp_f32 v46, v46\n"
    "v_mfma_f32_16x16x32_bf16 v[76:79], a[192:195], a[144:147], 0\n"
    "v_exp_f32 v47, v47\n"
    "v_mfma_f32_16x16x32_bf16 v[76:79], a[196:199], a[148:151], v[76:79]\n"
    "v_cvt_pk_bf16_f32 v45, v46, v47\n"
    "v_fma_f32 v48, v48, s52, v13\n"
    "v_mfma_f32_16x16x32_bf16 v[76:79], a[200:203], a[152:155], v[76:79]\n"
    "v_fma_f32 v49, v49, s52, v13\n"
    "v_fma_f32 v50, v50, s52, v13\n"
    "v_mfma_f32_16x16x32_bf16 v[80:83], a[200:203], a[176:179], 0\n"
    "ds_read_b64_tr_b16 a[200:201], v4 offset:36864\n"
    "v_fma_f32 v51, v51, s52, v13\n"
    "v_mfma_f32_16x16x32_bf16 v[80:83], a[196:199], a[172:175], v[80:83]\n"
    "ds_read_b64_tr_b16 a[196:197], v3 offset:36864\n"
    "v_fma_f32 v52, v52, s52, v14\n"
    "v_mfma_f32_16x16x32_bf16 v[80:83], a[192:195], a[168:171], v[80:83]\n"
    "s_waitcnt lgkmcnt(4)\n"
    "ds_read_b64_tr_b16 a[192:193], v2 offset:36864\n"
    "v_fma_f32 v53, v53, s52, v14\n"
    "v_mfma_f32_16x16x32_bf16 v[68:71], a[204:207], a[108:111], v[68:71]\n"
    "ds_read_b64_tr_b16 a[194:195], v2 offset:43008\n"
    "v_fma_f32 v56, v56, s52, v15\n"
    "v_mfma_f32_16x16x32_bf16 v[68:71], a[208:211], a[112:115], v[68:71]\n"
    "s_waitcnt lgkmcnt(5)\n"
    "ds_read_b64 v[204:205], v11 offset:7680\n"
    "v_fma_f32 v57, v57, s52, v15\n"
    "v_mfma_f32_16x16x32_bf16 v[68:71], a[212:215], a[116:119], v[68:71]\n"
    "ds_read_b64 v[206:207], v20 offset:7680\n"
    "v_fma_f32 v54, v54, s52, v14\n"
    "v_mfma_f32_16x16x32_bf16 v[72:75], a[212:215], a[140:143], v[72:75]\n"
    "ds_read_b64_tr_b16 a[198:199], v3 offset:43008\n"
    "v_fma_f32 v55, v55, s52, v14\n"
    "v_mfma_f32_16x16x32_bf16 v[72:75], a[208:211], a[136:139], v[72:75]\n"
    "ds_read_b64_tr_b16 a[202:203], v4 offset:43008\n"
    "v_fma_f32 v58, v58, s52, v15\n"
    "v_mfma_f32_16x16x32_bf16 v[72:75], a[204:207], a[132:135], v[72:75]\n"
    "v_fma_f32 v68, v68, s52, v12\n"
    "v_fma_f32 v69, v69, s52, v12\n"
    "v_mfma_f32_16x16x32_bf16 v[76:79], a[204:207], a[156:159], v[76:79]\n"
    "v_fma_f32 v70, v70, s52, v12\n"
    "v_fma_f32 v71, v71, s52, v12\n"
    "v_mfma_f32_16x16x32_bf16 v[76:79], a[208:211], a[160:163], v[76:79]\n"
    "v_exp_f32 v68, v68\n"
    "v_mfma_f32_16x16x32_bf16 v[76:79], a[212:215], a[164:167], v[76:79]\n"
    "v_exp_f32 v69, v69\n"
    "v_mfma_f32_16x16x32_bf16 v[80:83], a[212:215], a[188:191], v[80:83]\n"
    "ds_read_b64_tr_b16 a[212:213], v3 offset:36992\n"
    "v_cvt_pk_bf16_f32 v46, v68, v69\n"
    "v_mfma_f32_16x16x32_bf16 v[80:83], a[208:211], a[184:187], v[80:83]\n"
    "ds_read_b64_tr_b16 a[208:209], v2 offset:36992\n"
    "v_fma_f32 v72, v72, s52, v13\n"
    "v_mfma_f32_16x16x32_bf16 v[80:83], a[204:207], a[180:183], v[80:83]\n"
    "ds_read_b64_tr_b16 a[204:205], v5 offset:36864\n"
    "v_fma_f32 v73, v73, s52, v13\n"
    "v_smfmac_f32_16x16x64_bf16 a[244:247], a[252:255], v[28:35], v23\n"
    "ds_read_b64_tr_b16 a[206:207], v5 offset:43008\n"
    "v_fma_f32 v74, v74, s52, v13\n"
    "v_smfmac_f32_16x16x64_bf16 a[248:251], a[252:255], v[36:43], v23\n"
    "s_waitcnt lgkmcnt(8)\n"
    "ds_read_b64_tr_b16 a[210:211], v2 offset:43136\n"
    "v_fma_f32 v75, v75, s52, v13\n"
    "v_mfma_f32_16x16x32_bf16 v[104:107], a[192:195], v[40:43], v[104:107]\n"
    "s_waitcnt lgkmcnt(7)\n"
    "ds_read_b64_tr_b16 a[214:215], v3 offset:43136\n"
    "v_fma_f32 v59, v59, s52, v15\n"
    "v_mfma_f32_16x16x32_bf16 v[88:91], a[192:195], v[36:39], v[88:91]\n"
    "buffer_store_dwordx4 v[204:207], v9, s[48:51], s75 offen offset:256 sc1\n"
    "v_mfma_f32_16x16x32_bf16 v[100:103], a[192:195], v[32:35], v[100:103]\n"
    "v_exp_f32 v70, v70\n"
    "v_mfma_f32_16x16x32_bf16 v[84:87], a[192:195], v[28:31], v[84:87]\n"
    "s_waitcnt lgkmcnt(7)\n"
    "ds_read_b64_tr_b16 a[192:193], v4 offset:36992\n"
    "v_fma_f32 v76, v76, s52, v14\n"
    "v_mfma_f32_16x16x32_bf16 v[92:95], a[196:199], v[28:31], v[92:95]\n"
    "ds_read_b64_tr_b16 a[194:195], v4 offset:43136\n"
    "v_fma_f32 v77, v77, s52, v14\n"
    "v_mfma_f32_16x16x32_bf16 v[108:111], a[196:199], v[32:35], v[108:111]\n"
    "v_exp_f32 v71, v71\n"
    "v_mfma_f32_16x16x32_bf16 v[96:99], a[196:199], v[36:39], v[96:99]\n"
    "v_cvt_pk_bf16_f32 v47, v70, v71\n"
    "v_fma_f32 v78, v78, s52, v14\n"
    "v_mfma_f32_16x16x32_bf16 v[112:115], a[196:199], v[40:43], v[112:115]\n"
    "s_waitcnt lgkmcnt(8)\n"
    "ds_read_b64_tr_b16 a[196:197], v5 offset:36992\n"
    "v_fma_f32 v79, v79, s52, v14\n"
    "v_mfma_f32_16x16x32_bf16 v[164:167], a[200:203], v[40:43], v[164:167]\n"
    "ds_read_b64_tr_b16 a[198:199], v5 offset:43136\n"
    "v_fma_f32 v80, v80, s52, v15\n"
    "v_mfma_f32_16x16x32_bf16 v[136:139], a[200:203], v[36:39], v[136:139]\n"
    "v_exp_f32 v48, v48\n"
    "v_mfma_f32_16x16x32_bf16 v[160:163], a[200:203], v[32:35], v[160:163]\n"
    "v_exp_f32 v49, v49\n"
    "v_mfma_f32_16x16x32_bf16 v[132:135], a[200:203], v[28:31], v[132:135]\n"
    "s_waitcnt lgkmcnt(6)\n"
    "ds_read_b64_tr_b16 a[200:201], v2 offset:37120\n"
    "v_cvt_pk_bf16_f32 v48, v48, v49\n"
    "v_mfma_f32_16x16x32_bf16 v[140:143], a[204:207], v[28:31], v[140:143]\n"
    "ds_read_b64_tr_b16 a[202:203], v2 offset:43264\n"
    "v_fma_f32 v81, v81, s52, v15\n"
    "v_mfma_f32_16x16x32_bf16 v[168:171], a[204:207], v[32:35], v[168:171]\n"
    "v_exp_f32 v50, v50\n"
    "v_mfma_f32_16x16x32_bf16 v[144:147], a[204:207], v[36:39], v[144:147]\n"
    "v_exp_f32 v51, v51\n"
    "v_mfma_f32_16x16x32_bf16 v[172:175], a[204:207], v[40:43], v[172:175]\n"
    "s_waitcnt lgkmcnt(7)\n"
    "ds_read_b64_tr_b16 a[204:205], v3 offset:37120\n"
    "v_cvt_pk_bf16_f32 v49, v50, v51\n"
    "v_mfma_f32_16x16x32_bf16 v[228:231], a[208:211], v[40:43], v[228:231]\n"
    "ds_read_b64_tr_b16 a[206:207], v3 offset:43264\n"
    "v_fma_f32 v82, v82, s52, v15\n"
    "v_mfma_f32_16x16x32_bf16 v[196:199], a[208:211], v[36:39], v[196:199]\n"
    "v_exp_f32 v72, v72\n"
    "v_mfma_f32_16x16x32_bf16 v[224:227], a[208:211], v[32:35], v[224:227]\n"
    "v_exp_f32 v73, v73\n"
    "v_mfma_f32_16x16x32_bf16 v[192:195], a[208:211], v[28:31], v[192:195]\n"
    "s_waitcnt lgkmcnt(8)\n"
    "ds_read_b64_tr_b16 a[208:209], v4 offset:37120\n"
    "v_cvt_pk_bf16_f32 v50, v72, v73\n"
    "v_mfma_f32_16x16x32_bf16 v[200:203], a[212:215], v[28:31], v[200:203]\n"
    "ds_read_b64_tr_b16 a[210:211], v4 offset:43264\n"
    "v_fma_f32 v83, v83, s52, v15\n"
    "v_mfma_f32_16x16x32_bf16 v[232:235], a[212:215], v[32:35], v[232:235]\n"
    "v_exp_f32 v74, v74\n"
    "v_mfma_f32_16x16x32_bf16 a[88:91], a[212:215], v[36:39], a[88:91]\n"
    "v_exp_f32 v75, v75\n"
    "v_mfma_f32_16x16x32_bf16 a[92:95], a[212:215], v[40:43], a[92:95]\n"
    "s_waitcnt lgkmcnt(8)\n"
    "ds_read_b64_tr_b16 a[212:213], v5 offset:37120\n"
    "v_cvt_pk_bf16_f32 v51, v74, v75\n"
    "v_mfma_f32_16x16x32_bf16 v[128:131], a[192:195], v[40:43], v[128:131]\n"
    "v_exp_f32 v52, v52\n"
    "v_mfma_f32_16x16x32_bf16 v[124:127], a[192:195], v[36:39], v[124:127]\n"
    "ds_read_b64_tr_b16 a[214:215], v5 offset:43264\n"
    "v_accvgpr_read_b32 v204, a88\n"
    "v_mfma_f32_16x16x32_bf16 v[120:123], a[192:195], v[32:35], v[120:123]\n"
    "v_exp_f32 v53, v53\n"
    "v_mfma_f32_16x16x32_bf16 v[116:119], a[192:195], v[28:31], v[116:119]\n"
    "s_waitcnt lgkmcnt(8)\n"
    "v_exp_f32 v56, v56\n"
    "v_mfma_f32_16x16x32_bf16 a[216:219], a[196:199], v[28:31], a[216:219]\n"
    "v_exp_f32 v57, v57\n"
    "v_mfma_f32_16x16x32_bf16 a[220:223], a[196:199], v[32:35], a[220:223]\n"
    "v_exp_f32 v54, v54\n"
    "v_mfma_f32_16x16x32_bf16 a[224:227], a[196:199], v[36:39], a[224:227]\n"
    "v_exp_f32 v55, v55\n"
    "v_mfma_f32_16x16x32_bf16 a[228:231], a[196:199], v[40:43], a[228:231]\n"
    "s_waitcnt vmcnt(15) lgkmcnt(0)\n"
    "v_exp_f32 v58, v58\n"
    "v_mfma_f32_16x16x32_bf16 v[188:191], a[200:203], v[40:43], v[188:191]\n"
    "s_barrier\n"
    "ds_read_b128 a[192:195], v0 offset:0\n"
    "v_cvt_pk_bf16_f32 v52, v52, v53\n"
    "v_mfma_f32_16x16x32_bf16 v[184:187], a[200:203], v[36:39], v[184:187]\n"
    "ds_read_b128 a[196:199], v1 offset:0\n"
    "v_cvt_pk_bf16_f32 v56, v56, v57\n"
    "v_mfma_f32_16x16x32_bf16 v[180:183], a[200:203], v[32:35], v[180:183]\n"
    "v_exp_f32 v59, v59\n"
    "v_mfma_f32_16x16x32_bf16 v[176:179], a[200:203], v[28:31], v[176:179]\n"
    "ds_read_b128 a[200:203], v0 offset:128\n"
    "v_cvt_pk_bf16_f32 v53, v54, v55\n"
    "v_mfma_f32_16x16x32_bf16 a[232:235], a[204:207], v[28:31], a[232:235]\n"
    "ds_read_b64 v[236:237], v11 offset:8000\n"
    "v_cvt_pk_bf16_f32 v57, v58, v59\n"
    "v_mfma_f32_16x16x32_bf16 v[148:151], a[204:207], v[32:35], v[148:151]\n"
    "ds_read_b64 v[238:239], v20 offset:8000\n"
    "v_accvgpr_read_b32 v205, a89\n"
    "v_mfma_f32_16x16x32_bf16 v[152:155], a[204:207], v[36:39], v[152:155]\n"
    "v_exp_f32 v76, v76\n"
    "v_mfma_f32_16x16x32_bf16 v[156:159], a[204:207], v[40:43], v[156:159]\n"
    "ds_read_b128 a[204:207], v1 offset:128\n"
    "v_accvgpr_read_b32 v206, a90\n"
    "v_mfma_f32_16x16x32_bf16 v[220:223], a[208:211], v[40:43], v[220:223]\n"
    "v_exp_f32 v77, v77\n"
    "v_mfma_f32_16x16x32_bf16 v[216:219], a[208:211], v[36:39], v[216:219]\n"
    "v_exp_f32 v78, v78\n"
    "v_mfma_f32_16x16x32_bf16 v[212:215], a[208:211], v[32:35], v[212:215]\n"
    "v_exp_f32 v79, v79\n"
    "v_mfma_f32_16x16x32_bf16 v[208:211], a[208:211], v[28:31], v[208:211]\n"
    "ds_read_b128 a[208:211], v0 offset:256\n"
    "v_cvt_pk_bf16_f32 v54, v76, v77\n"
    "v_mfma_f32_16x16x32_bf16 v[240:243], a[212:215], v[28:31], v[240:243]\n"
    "v_exp_f32 v80, v80\n"
    "v_mfma_f32_16x16x32_bf16 v[244:247], a[212:215], v[32:35], v[244:247]\n"
    "v_exp_f32 v81, v81\n"
    "v_mfma_f32_16x16x32_bf16 v[248:251], a[212:215], v[36:39], v[248:251]\n"
    "v_exp_f32 v82, v82\n"
    "v_mfma_f32_16x16x32_bf16 v[252:255], a[212:215], v[40:43], v[252:255]\n"
    "s_waitcnt lgkmcnt(6)\n"
    "ds_read_b128 a[212:215], v1 offset:256\n"
    "v_cvt_pk_bf16_f32 v55, v78, v79\n"
    "v_mfma_f32_16x16x32_bf16 v[28:31], a[192:195], a[96:99], 0\n"
    "s_waitcnt lgkmcnt(6)\n"
    "v_exp_f32 v83, v83\n"
    "v_mfma_f32_16x16x32_bf16 v[28:31], a[196:199], a[100:103], v[28:31]\n"
    "s_waitcnt lgkmcnt(5)\n"
    "v_cvt_pk_bf16_f32 v58, v80, v81\n"
    "v_cvt_pk_bf16_f32 v59, v82, v83\n"
    "v_mfma_f32_16x16x32_bf16 v[28:31], a[200:203], a[104:107], v[28:31]\n"
    "s_waitcnt lgkmcnt(3)\n"
    "v_accvgpr_read_b32 v207, a91\n"
    "v_mfma_f32_16x16x32_bf16 v[32:35], a[200:203], a[128:131], 0\n"
    "buffer_store_dwordx4 v[236:239], v9, s[48:51], s75 offen offset:1024 sc1\n"
    "v_mfma_f32_16x16x32_bf16 v[32:35], a[196:199], a[124:127], v[32:35]\n"
    "v_mfma_f32_16x16x32_bf16 v[32:35], a[192:195], a[120:123], v[32:35]\n"
    "s_add_u32 s48, s48, 190464\ns_addc_u32 s49, s49, 0\n"
    "v_accvgpr_read_b32 v236, a92\n"
    "v_mfma_f32_16x16x32_bf16 v[36:39], a[192:195], a[144:147], 0\n"
    "v_accvgpr_read_b32 v237, a93\n"
    "v_accvgpr_read_b32 v238, a94\n"
    "v_mfma_f32_16x16x32_bf16 v[36:39], a[196:199], a[148:151], v[36:39]\n"
    "v_accvgpr_read_b32 v239, a95\n"
    "v_mfma_f32_16x16x32_bf16 v[36:39], a[200:203], a[152:155], v[36:39]\n"
    "v_mfma_f32_16x16x32_bf16 v[40:43], a[200:203], a[176:179], 0\n"
    "ds_read_b128 a[200:203], v0 offset:6272\n"
    "v_mfma_f32_16x16x32_bf16 v[40:43], a[196:199], a[172:175], v[40:43]\n"
    "ds_read_b128 a[196:199], v1 offset:6144\n"
    "v_mfma_f32_16x16x32_bf16 v[40:43], a[192:195], a[168:171], v[40:43]\n"
    "s_waitcnt lgkmcnt(4)\n"
    "ds_read_b128 a[192:195], v0 offset:6144\n"
    "v_mfma_f32_16x16x32_bf16 v[28:31], a[204:207], a[108:111], v[28:31]\n"
    "s_waitcnt lgkmcnt(4)\n"
    "v_mfma_f32_16x16x32_bf16 v[28:31], a[208:211], a[112:115], v[28:31]\n"
    "s_waitcnt lgkmcnt(3)\n"
    "v_mfma_f32_16x16x32_bf16 v[28:31], a[212:215], a[116:119], v[28:31]\n"
    "v_mfma_f32_16x16x32_bf16 v[32:35], a[212:215], a[140:143], v[32:35]\n"
    "v_mfma_f32_16x16x32_bf16 v[32:35], a[208:211], a[136:139], v[32:35]\n"
    "v_mfma_f32_16x16x32_bf16 v[32:35], a[204:207], a[132:135], v[32:35]\n"
    "v_mfma_f32_16x16x32_bf16 v[36:39], a[204:207], a[156:159], v[36:39]\n"
    "v_mfma_f32_16x16x32_bf16 v[36:39], a[208:211], a[160:163], v[36:39]\n"
    "v_mfma_f32_16x16x32_bf16 v[36:39], a[212:215], a[164:167], v[36:39]\n"
    "v_mfma_f32_16x16x32_bf16 v[40:43], a[212:215], a[188:191], v[40:43]\n"
    "ds_read_b128 a[212:215], v1 offset:6400\n"
    "v_fma_f32 v28, v28, s52, v12\n"
    "v_mfma_f32_16x16x32_bf16 v[40:43], a[208:211], a[184:187], v[40:43]\n"
    "ds_read_b128 a[208:211], v0 offset:6400\n"
    "v_fma_f32 v29, v29, s52, v12\n"
    "v_mfma_f32_16x16x32_bf16 v[40:43], a[204:207], a[180:183], v[40:43]\n"
    "s_waitcnt lgkmcnt(2)\n"
    "ds_read_b128 a[204:207], v1 offset:6272\n"
    "v_fma_f32 v30, v30, s52, v12\n"
    "v_mfma_f32_16x16x32_bf16 v[60:63], a[192:195], a[96:99], 0\n"
    "v_exp_f32 v28, v28\n"
    "v_mfma_f32_16x16x32_bf16 v[60:63], a[196:199], a[100:103], v[60:63]\n"
    "v_exp_f32 v29, v29\n"
    "v_mfma_f32_16x16x32_bf16 v[60:63], a[200:203], a[104:107], v[60:63]\n"
    "v_fma_f32 v31, v31, s52, v12\n"
    "v_cvt_pk_bf16_f32 v28, v28, v29\n"
    "v_mfma_f32_16x16x32_bf16 v[64:67], a[200:203], a[128:131], 0\n"
    "v_exp_f32 v30, v30\n"
    "v_mfma_f32_16x16x32_bf16 v[64:67], a[196:199], a[124:127], v[64:67]\n"
    "v_exp_f32 v31, v31\n"
    "v_mfma_f32_16x16x32_bf16 v[64:67], a[192:195], a[120:123], v[64:67]\n"
    "v_cvt_pk_bf16_f32 v29, v30, v31\n"
    "v_fma_f32 v32, v32, s52, v13\n"
    "v_mfma_f32_16x16x32_bf16 v[68:71], a[192:195], a[144:147], 0\n"
    "v_fma_f32 v33, v33, s52, v13\n"
    "v_fma_f32 v34, v34, s52, v13\n"
    "v_mfma_f32_16x16x32_bf16 v[68:71], a[196:199], a[148:151], v[68:71]\n"
    "v_exp_f32 v32, v32\n"
    "v_mfma_f32_16x16x32_bf16 v[68:71], a[200:203], a[152:155], v[68:71]\n"
    "v_exp_f32 v33, v33\n"
    "v_mfma_f32_16x16x32_bf16 v[72:75], a[200:203], a[176:179], 0\n"
    "ds_read_b64_tr_b16 a[200:201], v4 offset:49152\n"
    "v_fma_f32 v35, v35, s52, v13\n"
    "v_mfma_f32_16x16x32_bf16 v[72:75], a[196:199], a[172:175], v[72:75]\n"
    "ds_read_b64_tr_b16 a[196:197], v3 offset:49152\n"
    "v_cvt_pk_bf16_f32 v32, v32, v33\n"
    "v_mfma_f32_16x16x32_bf16 v[72:75], a[192:195], a[168:171], v[72:75]\n"
    "s_waitcnt lgkmcnt(2)\n"
    "ds_read_b64_tr_b16 a[192:193], v2 offset:49152\n"
    "v_fma_f32 v36, v36, s52, v14\n"
    "v_mfma_f32_16x16x32_bf16 v[60:63], a[204:207], a[108:111], v[60:63]\n"
    "ds_read_b64_tr_b16 a[194:195], v2 offset:55296\n"
    "v_fma_f32 v37, v37, s52, v14\n"
    "v_mfma_f32_16x16x32_bf16 v[60:63], a[208:211], a[112:115], v[60:63]\n"
    "ds_read_b64_tr_b16 a[198:199], v3 offset:55296\n"
    "v_fma_f32 v40, v40, s52, v15\n"
    "v_mfma_f32_16x16x32_bf16 v[60:63], a[212:215], a[116:119], v[60:63]\n"
    "ds_read_b64_tr_b16 a[202:203], v4 offset:55296\n"
    "v_fma_f32 v41, v41, s52, v15\n"
    "v_mfma_f32_16x16x32_bf16 v[64:67], a[212:215], a[140:143], v[64:67]\n"
    "v_exp_f32 v34, v34\n"
    "v_mfma_f32_16x16x32_bf16 v[64:67], a[208:211], a[136:139], v[64:67]\n"
    "v_exp_f32 v35, v35\n"
    "v_mfma_f32_16x16x32_bf16 v[64:67], a[204:207], a[132:135], v[64:67]\n"
    "v_cvt_pk_bf16_f32 v33, v34, v35\n"
    "v_fma_f32 v60, v60, s52, v12\n"
    "v_mfma_f32_16x16x32_bf16 v[68:71], a[204:207], a[156:159], v[68:71]\n"
    "v_fma_f32 v61, v61, s52, v12\n"
    "v_fma_f32 v62, v62, s52, v12\n"
    "v_mfma_f32_16x16x32_bf16 v[68:71], a[208:211], a[160:163], v[68:71]\n"
    "v_fma_f32 v63, v63, s52, v12\n"
    "v_fma_f32 v38, v38, s52, v14\n"
    "v_mfma_f32_16x16x32_bf16 v[68:71], a[212:215], a[164:167], v[68:71]\n"
    "v_exp_f32 v60, v60\n"
    "v_mfma_f32_16x16x32_bf16 v[72:75], a[212:215], a[188:191], v[72:75]\n"
    "ds_read_b64_tr_b16 a[212:213], v3 offset:49280\n"
    "v_fma_f32 v64, v64, s52, v13\n"
    "v_mfma_f32_16x16x32_bf16 v[72:75], a[208:211], a[184:187], v[72:75]\n"
    "ds_read_b64_tr_b16 a[208:209], v2 offset:49280\n"
    "v_fma_f32 v65, v65, s52, v13\n"
    "v_mfma_f32_16x16x32_bf16 v[72:75], a[204:207], a[180:183], v[72:75]\n"
    "ds_read_b64_tr_b16 a[204:205], v5 offset:49152\n"
    "v_fma_f32 v66, v66, s52, v13\n"
    "v_smfmac_f32_16x16x64_bf16 a[244:247], a[252:255], v[44:51], v23\n"
    "ds_read_b64_tr_b16 a[206:207], v5 offset:55296\n"
    "v_fma_f32 v67, v67, s52, v13\n"
    "v_smfmac_f32_16x16x64_bf16 a[248:251], a[252:255], v[52:59], v23\n"
    "s_waitcnt lgkmcnt(6)\n"
    "ds_read_b64_tr_b16 a[210:211], v2 offset:55424\n"
    "v_fma_f32 v39, v39, s52, v14\n"
    "v_mfma_f32_16x16x32_bf16 v[104:107], a[192:195], v[56:59], v[104:107]\n"
    "ds_read_b64_tr_b16 a[214:215], v3 offset:55424\n"
    "v_fma_f32 v42, v42, s52, v15\n"
    "v_mfma_f32_16x16x32_bf16 v[88:91], a[192:195], v[52:55], v[88:91]\n"
    "v_exp_f32 v61, v61\n"
    "v_mfma_f32_16x16x32_bf16 v[100:103], a[192:195], v[48:51], v[100:103]\n"
    "v_exp_f32 v62, v62\n"
    "v_mfma_f32_16x16x32_bf16 v[84:87], a[192:195], v[44:47], v[84:87]\n"
    "s_waitcnt lgkmcnt(7)\n"
    "ds_read_b64_tr_b16 a[192:193], v4 offset:49280\n"
    "v_cvt_pk_bf16_f32 v30, v60, v61\n"
    "v_mfma_f32_16x16x32_bf16 v[92:95], a[196:199], v[44:47], v[92:95]\n"
    "ds_read_b64_tr_b16 a[194:195], v4 offset:55424\n"
    "v_fma_f32 v43, v43, s52, v15\n"
    "v_mfma_f32_16x16x32_bf16 v[108:111], a[196:199], v[48:51], v[108:111]\n"
    "v_exp_f32 v63, v63\n"
    "v_mfma_f32_16x16x32_bf16 v[96:99], a[196:199], v[52:55], v[96:99]\n"
    "v_cvt_pk_bf16_f32 v31, v62, v63\n"
    "v_fma_f32 v68, v68, s52, v14\n"
    "v_mfma_f32_16x16x32_bf16 v[112:115], a[196:199], v[56:59], v[112:115]\n"
    "s_waitcnt lgkmcnt(8)\n"
    "ds_read_b64_tr_b16 a[196:197], v5 offset:49280\n"
    "v_fma_f32 v69, v69, s52, v14\n"
    "v_mfma_f32_16x16x32_bf16 v[164:167], a[200:203], v[56:59], v[164:167]\n"
    "ds_read_b64_tr_b16 a[198:199], v5 offset:55424\n"
    "v_fma_f32 v70, v70, s52, v14\n"
    "v_mfma_f32_16x16x32_bf16 v[136:139], a[200:203], v[52:55], v[136:139]\n"
    "v_exp_f32 v64, v64\n"
    "v_mfma_f32_16x16x32_bf16 v[160:163], a[200:203], v[48:51], v[160:163]\n"
    "v_exp_f32 v65, v65\n"
    "v_mfma_f32_16x16x32_bf16 v[132:135], a[200:203], v[44:47], v[132:135]\n"
    "s_waitcnt lgkmcnt(6)\n"
    "ds_read_b64_tr_b16 a[200:201], v2 offset:49408\n"
    "v_cvt_pk_bf16_f32 v34, v64, v65\n"
    "v_mfma_f32_16x16x32_bf16 v[140:143], a[204:207], v[44:47], v[140:143]\n"
    "ds_read_b64_tr_b16 a[202:203], v2 offset:55552\n"
    "v_fma_f32 v71, v71, s52, v14\n"
    "v_mfma_f32_16x16x32_bf16 v[168:171], a[204:207], v[48:51], v[168:171]\n"
    "v_exp_f32 v66, v66\n"
    "v_mfma_f32_16x16x32_bf16 v[144:147], a[204:207], v[52:55], v[144:147]\n"
    "v_exp_f32 v67, v67\n"
    "v_mfma_f32_16x16x32_bf16 v[172:175], a[204:207], v[56:59], v[172:175]\n"
    "s_waitcnt lgkmcnt(7)\n"
    "ds_read_b64_tr_b16 a[204:205], v3 offset:49408\n"
    "v_cvt_pk_bf16_f32 v35, v66, v67\n"
    "v_mfma_f32_16x16x32_bf16 v[228:231], a[208:211], v[56:59], v[228:231]\n"
    "ds_read_b64_tr_b16 a[206:207], v3 offset:55552\n"
    "v_fma_f32 v72, v72, s52, v15\n"
    "v_mfma_f32_16x16x32_bf16 v[196:199], a[208:211], v[52:55], v[196:199]\n"
    "v_exp_f32 v36, v36\n"
    "v_mfma_f32_16x16x32_bf16 v[224:227], a[208:211], v[48:51], v[224:227]\n"
    "v_exp_f32 v37, v37\n"
    "v_mfma_f32_16x16x32_bf16 v[192:195], a[208:211], v[44:47], v[192:195]\n"
    "s_waitcnt lgkmcnt(8)\n"
    "ds_read_b64_tr_b16 a[208:209], v4 offset:49408\n"
    "v_fma_f32 v73, v73, s52, v15\n"
    "v_mfma_f32_16x16x32_bf16 v[200:203], a[212:215], v[44:47], v[200:203]\n"
    "ds_read_b64_tr_b16 a[210:211], v4 offset:55552\n"
    "v_fma_f32 v74, v74, s52, v15\n"
    "v_mfma_f32_16x16x32_bf16 v[232:235], a[212:215], v[48:51], v[232:235]\n"
    "v_exp_f32 v40, v40\n"
    "v_mfma_f32_16x16x32_bf16 v[204:207], a[212:215], v[52:55], v[204:207]\n"
    "v_exp_f32 v41, v41\n"
    "v_mfma_f32_16x16x32_bf16 v[236:239], a[212:215], v[56:59], v[236:239]\n"
    "s_waitcnt lgkmcnt(8)\n"
    "ds_read_b64_tr_b16 a[212:213], v5 offset:49408\n"
    "v_fma_f32 v75, v75, s52, v15\n"
    "v_mfma_f32_16x16x32_bf16 v[128:131], a[192:195], v[56:59], v[128:131]\n"
    "ds_read_b64_tr_b16 a[214:215], v5 offset:55552\n"
    "v_cvt_pk_bf16_f32 v36, v36, v37\n"
    "v_mfma_f32_16x16x32_bf16 v[124:127], a[192:195], v[52:55], v[124:127]\n"
    "v_exp_f32 v38, v38\n"
    "v_mfma_f32_16x16x32_bf16 v[120:123], a[192:195], v[48:51], v[120:123]\n"
    "v_exp_f32 v39, v39\n"
    "v_mfma_f32_16x16x32_bf16 v[116:119], a[192:195], v[44:47], v[116:119]\n"
    "s_waitcnt lgkmcnt(8)\n"
    "v_exp_f32 v42, v42\n"
    "v_mfma_f32_16x16x32_bf16 a[216:219], a[196:199], v[44:47], a[216:219]\n"
    "v_exp_f32 v43, v43\n"
    "v_mfma_f32_16x16x32_bf16 a[220:223], a[196:199], v[48:51], a[220:223]\n"
    "v_cvt_pk_bf16_f32 v40, v40, v41\n"
    "v_cvt_pk_bf16_f32 v37, v38, v39\n"
    "v_mfma_f32_16x16x32_bf16 a[224:227], a[196:199], v[52:55], a[224:227]\n"
    "s_waitcnt vmcnt(3) lgkmcnt(0)\n"
    "v_exp_f32 v68, v68\n"
    "v_mfma_f32_16x16x32_bf16 a[228:231], a[196:199], v[56:59], a[228:231]\n"
    "s_barrier\n"
    "ds_read_b128 a[192:195], v0 offset:12288\n"
    "v_cvt_pk_bf16_f32 v41, v42, v43\n"
    "v_mfma_f32_16x16x32_bf16 v[188:191], a[200:203], v[56:59], v[188:191]\n"
    "s_nop 0\n"
    "ds_read_b128 a[196:199], v1 offset:12288\n"
    "ds_read_b64_tr_b16 a[0:1], v2 offset:12288\n"
    "v_mfma_f32_16x16x32_bf16 v[184:187], a[200:203], v[52:55], v[184:187]\n"
    "s_nop 0\n"
    "ds_read_b64_tr_b16 a[2:3], v2 offset:18432\n"
    "ds_read_b64_tr_b16 a[4:5], v2 offset:0\n"
    "v_mfma_f32_16x16x32_bf16 v[180:183], a[200:203], v[48:51], v[180:183]\n"
    "s_nop 0\n"
    "ds_read_b64_tr_b16 a[6:7], v2 offset:6144\n"
    "ds_read_b64_tr_b16 a[8:9], v3 offset:0\n"
    "v_mfma_f32_16x16x32_bf16 v[176:179], a[200:203], v[44:47], v[176:179]\n"
    "s_nop 0\n"
    "ds_read_b128 a[200:203], v0 offset:12416\n"
    "ds_read_b64_tr_b16 a[10:11], v3 offset:6144\n"
    "v_mfma_f32_16x16x32_bf16 a[232:235], a[204:207], v[44:47], a[232:235]\n"
    "s_nop 0\n"
    "ds_read_b64_tr_b16 a[12:13], v3 offset:12288\n"
    "ds_read_b64_tr_b16 a[14:15], v3 offset:18432\n"
    "v_mfma_f32_16x16x32_bf16 v[148:151], a[204:207], v[48:51], v[148:151]\n"
    "s_nop 0\n"
    "ds_read_b64_tr_b16 a[16:17], v4 offset:12288\n"
    "ds_read_b64_tr_b16 a[18:19], v4 offset:18432\n"
    "v_mfma_f32_16x16x32_bf16 v[152:155], a[204:207], v[52:55], v[152:155]\n"
    "s_nop 0\n"
    "ds_read_b64_tr_b16 a[20:21], v4 offset:0\n"
    "ds_read_b64_tr_b16 a[22:23], v4 offset:6144\n"
    "v_mfma_f32_16x16x32_bf16 v[156:159], a[204:207], v[56:59], v[156:159]\n"
    "s_nop 0\n"
    "ds_read_b128 a[204:207], v1 offset:12416\n"
    "ds_read_b64_tr_b16 a[24:25], v5 offset:0\n"
    "v_mfma_f32_16x16x32_bf16 v[220:223], a[208:211], v[56:59], v[220:223]\n"
    "s_nop 0\n"
    "ds_read_b64_tr_b16 a[26:27], v5 offset:6144\n"
    "ds_read_b64_tr_b16 a[28:29], v5 offset:12288\n"
    "v_mfma_f32_16x16x32_bf16 v[216:219], a[208:211], v[52:55], v[216:219]\n"
    "s_nop 0\n"
    "ds_read_b64_tr_b16 a[30:31], v5 offset:18432\n"
    "ds_read_b64_tr_b16 a[32:33], v2 offset:12416\n"
    "v_mfma_f32_16x16x32_bf16 v[212:215], a[208:211], v[48:51], v[212:215]\n"
    "s_nop 0\n"
    "ds_read_b64_tr_b16 a[34:35], v2 offset:18560\n"
    "ds_read_b64_tr_b16 a[36:37], v2 offset:128\n"
    "v_mfma_f32_16x16x32_bf16 v[208:211], a[208:211], v[44:47], v[208:211]\n"
    "s_nop 0\n"
    "ds_read_b128 a[208:211], v0 offset:12544\n"
    "ds_read_b64_tr_b16 a[38:39], v2 offset:6272\n"
    "v_mfma_f32_16x16x32_bf16 v[240:243], a[212:215], v[44:47], v[240:243]\n"
    "s_nop 0\n"
    "ds_read_b64_tr_b16 a[40:41], v3 offset:128\n"
    "ds_read_b64_tr_b16 a[42:43], v3 offset:6272\n"
    "v_mfma_f32_16x16x32_bf16 v[244:247], a[212:215], v[48:51], v[244:247]\n"
    "s_nop 0\n"
    "ds_read_b64_tr_b16 a[44:45], v3 offset:12416\n"
    "ds_read_b64_tr_b16 a[46:47], v3 offset:18560\n"
    "v_mfma_f32_16x16x32_bf16 v[248:251], a[212:215], v[52:55], v[248:251]\n"
    "s_nop 0\n"
    "ds_read_b64_tr_b16 a[48:49], v4 offset:12416\n"
    "ds_read_b64_tr_b16 a[50:51], v4 offset:18560\n"
    "v_mfma_f32_16x16x32_bf16 v[252:255], a[212:215], v[56:59], v[252:255]\n"
    "s_waitcnt lgkmcnt(15)\n"
    "ds_read_b128 a[212:215], v1 offset:12544\n"
    "ds_read_b64_tr_b16 a[52:53], v4 offset:128\n"
    "v_mfma_f32_16x16x32_bf16 v[44:47], a[192:195], a[96:99], 0\n"
    "s_nop 0\n"
    "ds_read_b64_tr_b16 a[54:55], v4 offset:6272\n"
    "ds_read_b64_tr_b16 a[56:57], v5 offset:128\n"
    "v_mfma_f32_16x16x32_bf16 v[44:47], a[196:199], a[100:103], v[44:47]\n"
    "s_nop 0\n"
    "ds_read_b64_tr_b16 a[58:59], v5 offset:6272\n"
    "ds_read_b64_tr_b16 a[60:61], v5 offset:12416\n"
    "v_mfma_f32_16x16x32_bf16 v[44:47], a[200:203], a[104:107], v[44:47]\n"
    "s_nop 0\n"
    "ds_read_b64_tr_b16 a[62:63], v5 offset:18560\n"
    "ds_read_b64_tr_b16 a[64:65], v2 offset:12544\n"
    "v_mfma_f32_16x16x32_bf16 v[48:51], a[200:203], a[128:131], 0\n"
    "s_nop 0\n"
    "ds_read_b64_tr_b16 a[66:67], v2 offset:18688\n"
    "ds_read_b64_tr_b16 a[68:69], v2 offset:256\n"
    "v_mfma_f32_16x16x32_bf16 v[48:51], a[196:199], a[124:127], v[48:51]\n"
    "s_nop 0\n"
    "ds_read_b64_tr_b16 a[70:71], v2 offset:6400\n"
    "ds_read_b64_tr_b16 a[72:73], v3 offset:256\n"
    "v_mfma_f32_16x16x32_bf16 v[48:51], a[192:195], a[120:123], v[48:51]\n"
    "s_nop 0\n"
    "ds_read_b64_tr_b16 a[74:75], v3 offset:6400\n"
    "ds_read_b64_tr_b16 a[76:77], v3 offset:12544\n"
    "v_mfma_f32_16x16x32_bf16 v[52:55], a[192:195], a[144:147], 0\n"
    "s_nop 0\n"
    "ds_read_b64_tr_b16 a[78:79], v3 offset:18688\n"
    "ds_read_b64_tr_b16 a[80:81], v4 offset:12544\n"
    "v_mfma_f32_16x16x32_bf16 v[52:55], a[196:199], a[148:151], v[52:55]\n"
    "s_nop 0\n"
    "ds_read_b64_tr_b16 a[82:83], v4 offset:18688\n"
    "ds_read_b64_tr_b16 a[84:85], v4 offset:256\n"
    "v_mfma_f32_16x16x32_bf16 v[52:55], a[200:203], a[152:155], v[52:55]\n"
    "s_nop 0\n"
    "ds_read_b64_tr_b16 a[86:87], v4 offset:6400\n"
    "ds_read_b64_tr_b16 a[88:89], v5 offset:256\n"
    "v_mfma_f32_16x16x32_bf16 v[56:59], a[200:203], a[176:179], 0\n"
    "s_nop 0\n"
    "ds_read_b128 a[200:203], v0 offset:18560\n"
    "ds_read_b64_tr_b16 a[90:91], v5 offset:6400\n"
    "v_mfma_f32_16x16x32_bf16 v[56:59], a[196:199], a[172:175], v[56:59]\n"
    "s_nop 0\n"
    "ds_read_b128 a[196:199], v1 offset:18432\n"
    "ds_read_b64_tr_b16 a[92:93], v5 offset:12544\n"
    "v_mfma_f32_16x16x32_bf16 v[56:59], a[192:195], a[168:171], v[56:59]\n"
    "s_nop 0\n"
    "ds_read_b128 a[192:195], v0 offset:18432\n"
    "ds_read_b64_tr_b16 a[94:95], v5 offset:18688\n"
    "v_mfma_f32_16x16x32_bf16 v[44:47], a[204:207], a[108:111], v[44:47]\n"
    "s_waitcnt lgkmcnt(15)\n"
    "v_exp_f32 v69, v69\n"
    "v_mfma_f32_16x16x32_bf16 v[44:47], a[208:211], a[112:115], v[44:47]\n"
    "v_exp_f32 v70, v70\n"
    "v_mfma_f32_16x16x32_bf16 v[44:47], a[212:215], a[116:119], v[44:47]\n"
    "v_exp_f32 v71, v71\n"
    "v_mfma_f32_16x16x32_bf16 v[48:51], a[212:215], a[140:143], v[48:51]\n"
    "v_exp_f32 v72, v72\n"
    "v_mfma_f32_16x16x32_bf16 v[48:51], a[208:211], a[136:139], v[48:51]\n"
    "v_exp_f32 v73, v73\n"
    "v_mfma_f32_16x16x32_bf16 v[48:51], a[204:207], a[132:135], v[48:51]\n"
    "v_exp_f32 v74, v74\n"
    "v_mfma_f32_16x16x32_bf16 v[52:55], a[204:207], a[156:159], v[52:55]\n"
    "v_fma_f32 v44, v44, s52, v12\n"
    "v_fma_f32 v45, v45, s52, v12\n"
    "v_mfma_f32_16x16x32_bf16 v[52:55], a[208:211], a[160:163], v[52:55]\n"
    "v_exp_f32 v44, v44\n"
    "v_mfma_f32_16x16x32_bf16 v[52:55], a[212:215], a[164:167], v[52:55]\n"
    "v_exp_f32 v45, v45\n"
    "v_mfma_f32_16x16x32_bf16 v[56:59], a[212:215], a[188:191], v[56:59]\n"
    "ds_read_b128 a[212:215], v1 offset:18688\n"
    "v_fma_f32 v46, v46, s52, v12\n"
    "v_mfma_f32_16x16x32_bf16 v[56:59], a[208:211], a[184:187], v[56:59]\n"
    "ds_read_b128 a[208:211], v0 offset:18688\n"
    "v_fma_f32 v47, v47, s52, v12\n"
    "v_mfma_f32_16x16x32_bf16 v[56:59], a[204:207], a[180:183], v[56:59]\n"
    "s_waitcnt lgkmcnt(3)\n"
    "ds_read_b128 a[204:207], v1 offset:18560\n"
    "v_cvt_pk_bf16_f32 v44, v44, v45\n"
    "v_mfma_f32_16x16x32_bf16 v[76:79], a[192:195], a[96:99], 0\n"
    "v_exp_f32 v46, v46\n"
    "v_mfma_f32_16x16x32_bf16 v[76:79], a[196:199], a[100:103], v[76:79]\n"
    "v_exp_f32 v47, v47\n"
    "v_mfma_f32_16x16x32_bf16 v[76:79], a[200:203], a[104:107], v[76:79]\n"
    "v_cvt_pk_bf16_f32 v45, v46, v47\n"
    "v_fma_f32 v48, v48, s52, v13\n"
    "v_mfma_f32_16x16x32_bf16 v[80:83], a[200:203], a[128:131], 0\n"
    "v_fma_f32 v49, v49, s52, v13\n"
    "v_fma_f32 v50, v50, s52, v13\n"
    "v_mfma_f32_16x16x32_bf16 v[80:83], a[196:199], a[124:127], v[80:83]\n"
    "v_exp_f32 v48, v48\n"
    "v_mfma_f32_16x16x32_bf16 v[80:83], a[192:195], a[120:123], v[80:83]\n"
    "v_exp_f32 v49, v49\n"
    "v_mfma_f32_16x16x32_bf16 v[60:63], a[192:195], a[144:147], 0\n"
    "v_fma_f32 v51, v51, s52, v13\n"
    "v_cvt_pk_bf16_f32 v48, v48, v49\n"
    "v_mfma_f32_16x16x32_bf16 v[60:63], a[196:199], a[148:151], v[60:63]\n"
    "v_exp_f32 v50, v50\n"
    "v_mfma_f32_16x16x32_bf16 v[60:63], a[200:203], a[152:155], v[60:63]\n"
    "v_exp_f32 v51, v51\n"
    "v_mfma_f32_16x16x32_bf16 v[64:67], a[200:203], a[176:179], 0\n"
    "v_cvt_pk_bf16_f32 v49, v50, v51\n"
    "v_fma_f32 v52, v52, s52, v14\n"
    "v_mfma_f32_16x16x32_bf16 v[64:67], a[196:199], a[172:175], v[64:67]\n"
    "v_fma_f32 v53, v53, s52, v14\n"
    "v_fma_f32 v54, v54, s52, v14\n"
    "v_mfma_f32_16x16x32_bf16 v[64:67], a[192:195], a[168:171], v[64:67]\n"
    "s_waitcnt lgkmcnt(0)\n"
    "v_exp_f32 v75, v75\n"
    "v_mfma_f32_16x16x32_bf16 v[76:79], a[204:207], a[108:111], v[76:79]\n"
    "v_exp_f32 v52, v52\n"
    "v_mfma_f32_16x16x32_bf16 v[76:79], a[208:211], a[112:115], v[76:79]\n"
    "v_exp_f32 v53, v53\n"
    "v_mfma_f32_16x16x32_bf16 v[76:79], a[212:215], a[116:119], v[76:79]\n"
    "v_fma_f32 v55, v55, s52, v14\n"
    "v_cvt_pk_bf16_f32 v38, v68, v69\n"
    "v_mfma_f32_16x16x32_bf16 v[80:83], a[212:215], a[140:143], v[80:83]\n"
    "v_exp_f32 v54, v54\n"
    "v_mfma_f32_16x16x32_bf16 v[80:83], a[208:211], a[136:139], v[80:83]\n"
    "v_exp_f32 v55, v55\n"
    "v_mfma_f32_16x16x32_bf16 v[80:83], a[204:207], a[132:135], v[80:83]\n"
    "v_cvt_pk_bf16_f32 v39, v70, v71\n"
    "v_fma_f32 v76, v76, s52, v12\n"
    "v_mfma_f32_16x16x32_bf16 v[60:63], a[204:207], a[156:159], v[60:63]\n"
    "v_fma_f32 v77, v77, s52, v12\n"
    "v_fma_f32 v78, v78, s52, v12\n"
    "v_mfma_f32_16x16x32_bf16 v[60:63], a[208:211], a[160:163], v[60:63]\n"
    "v_fma_f32 v79, v79, s52, v12\n"
    "v_cvt_pk_bf16_f32 v42, v72, v73\n"
    "v_mfma_f32_16x16x32_bf16 v[60:63], a[212:215], a[164:167], v[60:63]\n"
    "v_exp_f32 v76, v76\n"
    "v_mfma_f32_16x16x32_bf16 v[64:67], a[212:215], a[188:191], v[64:67]\n"
    "v_exp_f32 v77, v77\n"
    "v_mfma_f32_16x16x32_bf16 v[64:67], a[208:211], a[184:187], v[64:67]\n"
    "v_exp_f32 v78, v78\n"
    "v_mfma_f32_16x16x32_bf16 v[64:67], a[204:207], a[180:183], v[64:67]\n"
    "v_exp_f32 v79, v79\n"
    "v_smfmac_f32_16x16x64_bf16 a[244:247], a[252:255], v[28:35], v23\n"
    "v_cvt_pk_bf16_f32 v46, v76, v77\n"
    "v_cvt_pk_bf16_f32 v47, v78, v79\n"
    "s_nop 1\n"
    "v_mfma_f32_16x16x32_bf16 v[84:87], a[0:3], v[44:47], v[84:87]\n"
    "v_fma_f32 v80, v80, s52, v13\n"
    "v_fma_f32 v81, v81, s52, v13\n"
    "v_mfma_f32_16x16x32_bf16 v[84:87], a[4:7], v[28:31], v[84:87]\n"
    "v_fma_f32 v82, v82, s52, v13\n"
    "v_fma_f32 v83, v83, s52, v13\n"
    "v_mfma_f32_16x16x32_bf16 v[92:95], a[8:11], v[28:31], v[92:95]\n"
    "v_exp_f32 v80, v80\n"
    "v_mfma_f32_16x16x32_bf16 v[92:95], a[12:15], v[44:47], v[92:95]\n"
    "v_exp_f32 v81, v81\n"
    "v_mfma_f32_16x16x32_bf16 v[132:135], a[16:19], v[44:47], v[132:135]\n"
    "v_exp_f32 v82, v82\n"
    "v_mfma_f32_16x16x32_bf16 v[132:135], a[20:23], v[28:31], v[132:135]\n"
    "v_exp_f32 v83, v83\n"
    "v_mfma_f32_16x16x32_bf16 v[140:143], a[24:27], v[28:31], v[140:143]\n"
    "v_cvt_pk_bf16_f32 v50, v80, v81\n"
    "v_cvt_pk_bf16_f32 v51, v82, v83\n"
    "v_mfma_f32_16x16x32_bf16 v[140:143], a[28:31], v[44:47], v[140:143]\n"
    "v_fma_f32 v60, v60, s52, v14\n"
    "v_fma_f32 v61, v61, s52, v14\n"
    "v_mfma_f32_16x16x32_bf16 v[192:195], a[32:35], v[44:47], v[192:195]\n"
    "v_fma_f32 v62, v62, s52, v14\n"
    "v_fma_f32 v63, v63, s52, v14\n"
    "v_mfma_f32_16x16x32_bf16 v[192:195], a[36:39], v[28:31], v[192:195]\n"
    "v_cvt_pk_bf16_f32 v43, v74, v75\n"
    "v_cvt_pk_bf16_f32 v52, v52, v53\n"
    "v_mfma_f32_16x16x32_bf16 v[200:203], a[40:43], v[28:31], v[200:203]\n"
    "v_exp_f32 v60, v60\n"
    "v_mfma_f32_16x16x32_bf16 v[200:203], a[44:47], v[44:47], v[200:203]\n"
    "v_exp_f32 v61, v61\n"
    "v_smfmac_f32_16x16x64_bf16 a[244:247], a[252:255], v[44:51], v23\n"
    "v_exp_f32 v62, v62\n"
    "v_mfma_f32_16x16x32_bf16 v[116:119], a[48:51], v[44:47], v[116:119]\n"
    "v_exp_f32 v63, v63\n"
    "v_cvt_pk_bf16_f32 v53, v54, v55\n"
    "v_fma_f32 v56, v56, s52, v15\n"
    "v_mfma_f32_16x16x32_bf16 v[116:119], a[52:55], v[28:31], v[116:119]\n"
    "v_cvt_pk_bf16_f32 v54, v60, v61\n"
    "v_cvt_pk_bf16_f32 v55, v62, v63\n"
    "v_accvgpr_read_b32 v16, a244\n"
    "v_accvgpr_read_b32 v26, a245\n"
    "v_mfma_f32_16x16x32_bf16 a[216:219], a[56:59], v[28:31], a[216:219]\n"
    "v_add_f32 v16, v16, v26\n"
    "v_fma_f32 v57, v57, s52, v15\n"
    "v_mov_b32 v27, v16\n"
    "v_fma_f32 v58, v58, s52, v15\n"
    "v_mfma_f32_16x16x32_bf16 a[216:219], a[60:63], v[44:47], a[216:219]\n"
    "v_rcp_f32 v16, v16\n"
    "v_exp_f32 v56, v56\n"
    "v_mfma_f32_16x16x32_bf16 v[176:179], a[64:67], v[44:47], v[176:179]\n"
    "v_mul_f32 v84, v84, v16\n"
    "v_mul_f32 v85, v85, v16\n"
    "v_mul_f32 v86, v86, v16\n"
    "v_cvt_pk_bf16_f32 v84, v84, v85\n"
    "v_mfma_f32_16x16x32_bf16 v[176:179], a[68:71], v[28:31], v[176:179]\n"
    "v_mul_f32 v87, v87, v16\n"
    "v_mul_f32 v92, v92, v16\n"
    "v_cvt_pk_bf16_f32 v85, v86, v87\n"
    "v_mul_f32 v93, v93, v16\n"
    "v_mfma_f32_16x16x32_bf16 a[232:235], a[72:75], v[28:31], a[232:235]\n"
    "ds_write_b64 v10, v[84:85] offset:0\n"
    "v_cvt_pk_bf16_f32 v92, v92, v93\n"
    "v_mfma_f32_16x16x32_bf16 a[232:235], a[76:79], v[44:47], a[232:235]\n"
    "v_mul_f32 v94, v94, v16\n"
    "v_mul_f32 v95, v95, v16\n"
    "v_mul_f32 v132, v132, v16\n"
    "v_cvt_pk_bf16_f32 v93, v94, v95\n"
    "v_mfma_f32_16x16x32_bf16 v[208:211], a[80:83], v[44:47], v[208:211]\n"
    "ds_write_b64 v10, v[92:93] offset:32\n"
    "v_mul_f32 v133, v133, v16\n"
    "v_mfma_f32_16x16x32_bf16 v[208:211], a[84:87], v[28:31], v[208:211]\n"
    "v_cvt_pk_bf16_f32 v132, v132, v133\n"
    "v_mul_f32 v134, v134, v16\n"
    "v_mul_f32 v135, v135, v16\n"
    "v_mul_f32 v140, v140, v16\n"
    "v_mfma_f32_16x16x32_bf16 v[240:243], a[88:91], v[28:31], v[240:243]\n"
    "v_cvt_pk_bf16_f32 v133, v134, v135\n"
    "v_mul_f32 v141, v141, v16\n"
    "v_mul_f32 v142, v142, v16\n"
    "v_cvt_pk_bf16_f32 v140, v140, v141\n"
    "v_mfma_f32_16x16x32_bf16 v[240:243], a[92:95], v[44:47], v[240:243]\n"
    "ds_write_b64 v10, v[132:133] offset:64\n"
    "v_mul_f32 v143, v143, v16\n"
    "v_mfma_f32_16x16x32_bf16 v[100:103], a[0:3], v[48:51], v[100:103]\n"
    "v_cvt_pk_bf16_f32 v141, v142, v143\n"
    "v_mul_f32 v192, v192, v16\n"
    "v_mul_f32 v193, v193, v16\n"
    "v_mul_f32 v194, v194, v16\n"
    "v_mfma_f32_16x16x32_bf16 v[100:103], a[4:7], v[32:35], v[100:103]\n"
    "ds_write_b64 v10, v[140:141] offset:96\n"
    "v_cvt_pk_bf16_f32 v192, v192, v193\n"
    "v_mfma_f32_16x16x32_bf16 v[108:111], a[8:11], v[32:35], v[108:111]\n"
    "ds_read_b64 v[84:85], v11 offset:0\n"
    "ds_read_b64 v[86:87], v20 offset:0\n"
    "ds_read_b64 v[92:93], v11 offset:320\n"
    "ds_read_b64 v[94:95], v20 offset:320\n"
    "v_mfma_f32_16x16x32_bf16 v[108:111], a[12:15], v[48:51], v[108:111]\n"
    "v_mul_f32 v195, v195, v16\n"
    "v_mul_f32 v200, v200, v16\n"
    "v_cvt_pk_bf16_f32 v193, v194, v195\n"
    "v_mul_f32 v201, v201, v16\n"
    "v_mfma_f32_16x16x32_bf16 v[160:163], a[16:19], v[48:51], v[160:163]\n"
    "ds_write_b64 v10, v[192:193] offset:2560\n"
    "v_cvt_pk_bf16_f32 v200, v200, v201\n"
    "v_mfma_f32_16x16x32_bf16 v[160:163], a[20:23], v[32:35], v[160:163]\n"
    "v_mul_f32 v202, v202, v16\n"
    "v_mul_f32 v203, v203, v16\n"
    "v_mul_f32 v116, v116, v16\n"
    "v_cvt_pk_bf16_f32 v201, v202, v203\n"
    "v_mfma_f32_16x16x32_bf16 v[168:171], a[24:27], v[32:35], v[168:171]\n"
    "ds_write_b64 v10, v[200:201] offset:2592\n"
    "v_mul_f32 v117, v117, v16\n"
    "v_mfma_f32_16x16x32_bf16 v[168:171], a[28:31], v[48:51], v[168:171]\n"
    "s_waitcnt lgkmcnt(4)\n"
    "buffer_store_dwordx4 v[84:87], v9, s[48:51], s72 offen offset:0 sc1\n"
    "s_waitcnt lgkmcnt(2)\n"
    "buffer_store_dwordx4 v[92:95], v9, s[48:51], s72 offen offset:768 sc1\n"
    "v_mfma_f32_16x16x32_bf16 v[224:227], a[32:35], v[48:51], v[224:227]\n"
    "v_cvt_pk_bf16_f32 v116, v116, v117\n"
    "v_mul_f32 v118, v118, v16\n"
    "v_mul_f32 v119, v119, v16\n"
    "v_accvgpr_read_b32 v46, a216\n"
    "v_mfma_f32_16x16x32_bf16 v[224:227], a[36:39], v[32:35], v[224:227]\n"
    "v_cvt_pk_bf16_f32 v117, v118, v119\n"
    "v_accvgpr_read_b32 v47, a217\n"
    "v_mul_f32 v46, v46, v16\n"
    "v_mul_f32 v47, v47, v16\n"
    "v_mfma_f32_16x16x32_bf16 v[232:235], a[40:43], v[32:35], v[232:235]\n"
    "ds_write_b64 v10, v[116:117] offset:2624\n"
    "v_cvt_pk_bf16_f32 v118, v46, v47\n"
    "v_mfma_f32_16x16x32_bf16 v[232:235], a[44:47], v[48:51], v[232:235]\n"
    "v_accvgpr_read_b32 v44, a218\n"
    "v_accvgpr_read_b32 v45, a219\n"
    "v_mul_f32 v44, v44, v16\n"
    "v_mul_f32 v45, v45, v16\n"
    "v_mfma_f32_16x16x32_bf16 v[120:123], a[48:51], v[48:51], v[120:123]\n"
    "v_cvt_pk_bf16_f32 v119, v44, v45\n"
    "v_mul_f32 v176, v176, v16\n"
    "v_mul_f32 v177, v177, v16\n"
    "v_mul_f32 v178, v178, v16\n"
    "v_mfma_f32_16x16x32_bf16 v[120:123], a[52:55], v[32:35], v[120:123]\n"
    "ds_write_b64 v10, v[118:119] offset:2656\n"
    "v_cvt_pk_bf16_f32 v176, v176, v177\n"
    "v_mfma_f32_16x16x32_bf16 a[220:223], a[56:59], v[32:35], a[220:223]\n"
    "ds_read_b64 v[192:193], v11 offset:2560\n"
    "ds_read_b64 v[194:195], v20 offset:2560\n"
    "ds_read_b64 v[200:201], v11 offset:2880\n"
    "ds_read_b64 v[202:203], v20 offset:2880\n"
    "v_mfma_f32_16x16x32_bf16 a[220:223], a[60:63], v[48:51], a[220:223]\n"
    "v_mul_f32 v179, v179, v16\n"
    "v_accvgpr_read_b32 v46, a232\n"
    "v_cvt_pk_bf16_f32 v177, v178, v179\n"
    "v_accvgpr_read_b32 v47, a233\n"
    "v_mfma_f32_16x16x32_bf16 v[180:183], a[64:67], v[48:51], v[180:183]\n"
    "ds_write_b64 v10, v[176:177] offset:5120\n"
    "v_mul_f32 v46, v46, v16\n"
    "v_mfma_f32_16x16x32_bf16 v[180:183], a[68:71], v[32:35], v[180:183]\n"
    "v_mul_f32 v47, v47, v16\n"
    "v_accvgpr_read_b32 v44, a234\n"
    "v_cvt_pk_bf16_f32 v178, v46, v47\n"
    "v_accvgpr_read_b32 v45, a235\n"
    "v_mfma_f32_16x16x32_bf16 v[148:151], a[72:75], v[32:35], v[148:151]\n"
    "v_mul_f32 v44, v44, v16\n"
    "v_mul_f32 v45, v45, v16\n"
    "v_mul_f32 v208, v208, v16\n"
    "v_cvt_pk_bf16_f32 v179, v44, v45\n"
    "v_mfma_f32_16x16x32_bf16 v[148:151], a[76:79], v[48:51], v[148:151]\n"
    "s_waitcnt lgkmcnt(3)\n"
    "buffer_store_dwordx4 v[192:195], v9, s[48:51], s72 offen offset:128 sc1\n"
    "s_waitcnt lgkmcnt(1)\n"
    "buffer_store_dwordx4 v[200:203], v9, s[48:51], s72 offen offset:896 sc1\n"
    "v_mfma_f32_16x16x32_bf16 v[212:215], a[80:83], v[48:51], v[212:215]\n"
    "ds_write_b64 v10, v[178:179] offset:5152\n"
    "v_mul_f32 v209, v209, v16\n"
    "v_mfma_f32_16x16x32_bf16 v[212:215], a[84:87], v[32:35], v[212:215]\n"
    "v_cvt_pk_bf16_f32 v208, v208, v209\n"
    "v_mul_f32 v210, v210, v16\n"
    "v_mul_f32 v211, v211, v16\n"
    "v_mul_f32 v240, v240, v16\n"
    "v_mfma_f32_16x16x32_bf16 v[244:247], a[88:91], v[32:35], v[244:247]\n"
    "v_cvt_pk_bf16_f32 v209, v210, v211\n"
    "v_mul_f32 v241, v241, v16\n"
    "v_mul_f32 v242, v242, v16\n"
    "v_cvt_pk_bf16_f32 v240, v240, v241\n"
    "v_mfma_f32_16x16x32_bf16 v[244:247], a[92:95], v[48:51], v[244:247]\n"
    "ds_write_b64 v10, v[208:209] offset:5184\n"
    "v_mul_f32 v243, v243, v16\n"
    "v_smfmac_f32_16x16x64_bf16 a[248:251], a[252:255], v[36:43], v23\n"
    "v_cvt_pk_bf16_f32 v241, v242, v243\n"
    "v_exp_f32 v57, v57\n"
    "v_mfma_f32_16x16x32_bf16 v[88:91], a[0:3], v[52:55], v[88:91]\n"
    "ds_write_b64 v10, v[240:241] offset:5216\n"
    "v_fma_f32 v59, v59, s52, v15\n"
    "v_mfma_f32_16x16x32_bf16 v[88:91], a[4:7], v[36:39], v[88:91]\n"
    "ds_read_b64 v[176:177], v11 offset:5120\n"
    "ds_read_b64 v[178:179], v20 offset:5120\n"
    "ds_read_b64 v[208:209], v11 offset:5440\n"
    "ds_read_b64 v[210:211], v20 offset:5440\n"
    "v_mfma_f32_16x16x32_bf16 v[96:99], a[8:11], v[36:39], v[96:99]\n"
    "v_exp_f32 v58, v58\n"
    "v_exp_f32 v59, v59\n"
    "v_mfma_f32_16x16x32_bf16 v[96:99], a[12:15], v[52:55], v[96:99]\n"
    "v_fma_f32 v64, v64, s52, v15\n"
    "v_fma_f32 v65, v65, s52, v15\n"
    "v_fma_f32 v66, v66, s52, v15\n"
    "v_fma_f32 v67, v67, s52, v15\n"
    "v_mfma_f32_16x16x32_bf16 v[136:139], a[16:19], v[52:55], v[136:139]\n"
    "v_cvt_pk_bf16_f32 v56, v56, v57\n"
    "v_exp_f32 v64, v64\n"
    "v_cvt_pk_bf16_f32 v57, v58, v59\n"
    "v_mfma_f32_16x16x32_bf16 v[136:139], a[20:23], v[36:39], v[136:139]\n"
    "v_exp_f32 v65, v65\n"
    "v_exp_f32 v66, v66\n"
    "v_mfma_f32_16x16x32_bf16 v[144:147], a[24:27], v[36:39], v[144:147]\n"
    "v_exp_f32 v67, v67\n"
    "v_cvt_pk_bf16_f32 v58, v64, v65\n"
    "v_cvt_pk_bf16_f32 v59, v66, v67\n"
    "v_mfma_f32_16x16x32_bf16 v[144:147], a[28:31], v[52:55], v[144:147]\n"
    "s_waitcnt lgkmcnt(0)\n"
    "buffer_store_dwordx4 v[176:179], v9, s[48:51], s72 offen offset:256 sc1\n"
    "buffer_store_dwordx4 v[208:211], v9, s[48:51], s72 offen offset:1024 sc1\n"
    "v_mfma_f32_16x16x32_bf16 v[196:199], a[32:35], v[52:55], v[196:199]\n"
    "v_accvgpr_read_b32 v17, a246\n"
    "v_accvgpr_read_b32 v26, a247\n"
    "v_accvgpr_read_b32 v50, a220\n"
    "v_add_f32 v17, v17, v26\n"
    "v_mfma_f32_16x16x32_bf16 v[196:199], a[36:39], v[36:39], v[196:199]\n"
    "v_add_f32 v27, v27, v17\n"
    "v_accvgpr_read_b32 v51, a221\n"
    "v_rcp_f32 v17, v17\n"
    "v_mfma_f32_16x16x32_bf16 v[204:207], a[40:43], v[36:39], v[204:207]\n"
    "v_mul_f32 v100, v100, v17\n"
    "v_mul_f32 v101, v101, v17\n"
    "v_mul_f32 v102, v102, v17\n"
    "v_cvt_pk_bf16_f32 v100, v100, v101\n"
    "v_mfma_f32_16x16x32_bf16 v[204:207], a[44:47], v[52:55], v[204:207]\n"
    "v_mul_f32 v103, v103, v17\n"
    "v_mul_f32 v108, v108, v17\n"
    "v_cvt_pk_bf16_f32 v101, v102, v103\n"
    "v_mul_f32 v109, v109, v17\n"
    "v_smfmac_f32_16x16x64_bf16 a[248:251], a[252:255], v[52:59], v23\n"
    "ds_write_b64 v10, v[100:101] offset:7680\n"
    "v_mfma_f32_16x16x32_bf16 v[124:127], a[48:51], v[52:55], v[124:127]\n"
    "v_cvt_pk_bf16_f32 v108, v108, v109\n"
    "v_mul_f32 v110, v110, v17\n"
    "v_mul_f32 v111, v111, v17\n"
    "v_mul_f32 v160, v160, v17\n"
    "v_mfma_f32_16x16x32_bf16 v[124:127], a[52:55], v[36:39], v[124:127]\n"
    "v_cvt_pk_bf16_f32 v109, v110, v111\n"
    "v_mul_f32 v161, v161, v17\n"
    "v_mul_f32 v162, v162, v17\n"
    "v_cvt_pk_bf16_f32 v160, v160, v161\n"
    "v_mfma_f32_16x16x32_bf16 a[224:227], a[56:59], v[36:39], a[224:227]\n"
    "ds_write_b64 v10, v[108:109] offset:7712\n"
    "v_mul_f32 v163, v163, v17\n"
    "v_mfma_f32_16x16x32_bf16 a[224:227], a[60:63], v[52:55], a[224:227]\n"
    "v_cvt_pk_bf16_f32 v161, v162, v163\n"
    "v_mul_f32 v168, v168, v17\n"
    "v_mul_f32 v169, v169, v17\n"
    "v_mul_f32 v170, v170, v17\n"
    "v_mfma_f32_16x16x32_bf16 v[184:187], a[64:67], v[52:55], v[184:187]\n"
    "ds_write_b64 v10, v[160:161] offset:7744\n"
    "v_cvt_pk_bf16_f32 v168, v168, v169\n"
    "v_mfma_f32_16x16x32_bf16 v[184:187], a[68:71], v[36:39], v[184:187]\n"
    "v_mul_f32 v171, v171, v17\n"
    "v_mul_f32 v224, v224, v17\n"
    "v_cvt_pk_bf16_f32 v169, v170, v171\n"
    "v_mul_f32 v225, v225, v17\n"
    "v_mfma_f32_16x16x32_bf16 v[152:155], a[72:75], v[36:39], v[152:155]\n"
    "ds_write_b64 v10, v[168:169] offset:7776\n"
    "v_cvt_pk_bf16_f32 v224, v224, v225\n"
    "v_mfma_f32_16x16x32_bf16 v[152:155], a[76:79], v[52:55], v[152:155]\n"
    "ds_read_b64 v[100:101], v11 offset:7680\n"
    "ds_read_b64 v[102:103], v20 offset:7680\n"
    "ds_read_b64 v[108:109], v11 offset:8000\n"
    "ds_read_b64 v[110:111], v20 offset:8000\n"
    "v_mfma_f32_16x16x32_bf16 v[216:219], a[80:83], v[52:55], v[216:219]\n"
    "v_mul_f32 v226, v226, v17\n"
    "v_mul_f32 v227, v227, v17\n"
    "v_mul_f32 v232, v232, v17\n"
    "v_cvt_pk_bf16_f32 v225, v226, v227\n"
    "v_mfma_f32_16x16x32_bf16 v[216:219], a[84:87], v[36:39], v[216:219]\n"
    "ds_write_b64 v10, v[224:225] offset:0\n"
    "v_mul_f32 v233, v233, v17\n"
    "v_mfma_f32_16x16x32_bf16 v[248:251], a[88:91], v[36:39], v[248:251]\n"
    "v_cvt_pk_bf16_f32 v232, v232, v233\n"
    "v_mul_f32 v234, v234, v17\n"
    "v_mul_f32 v235, v235, v17\n"
    "v_mul_f32 v120, v120, v17\n"
    "v_mfma_f32_16x16x32_bf16 v[248:251], a[92:95], v[52:55], v[248:251]\n"
    "v_cvt_pk_bf16_f32 v233, v234, v235\n"
    "v_mul_f32 v121, v121, v17\n"
    "v_mul_f32 v122, v122, v17\n"
    "v_cvt_pk_bf16_f32 v120, v120, v121\n"
    "v_mfma_f32_16x16x32_bf16 v[104:107], a[0:3], v[56:59], v[104:107]\n"
    "s_waitcnt lgkmcnt(3)\n"
    "buffer_store_dwordx4 v[100:103], v9, s[48:51], s73 offen offset:0 sc1\n"
    "s_waitcnt lgkmcnt(1)\n"
    "buffer_store_dwordx4 v[108:111], v9, s[48:51], s73 offen offset:768 sc1\n"
    "v_mfma_f32_16x16x32_bf16 v[104:107], a[4:7], v[40:43], v[104:107]\n"
    "ds_write_b64 v10, v[232:233] offset:32\n"
    "v_mul_f32 v123, v123, v17\n"
    "v_mfma_f32_16x16x32_bf16 v[112:115], a[8:11], v[40:43], v[112:115]\n"
    "v_cvt_pk_bf16_f32 v121, v122, v123\n"
    "v_mul_f32 v50, v50, v17\n"
    "v_mul_f32 v51, v51, v17\n"
    "v_accvgpr_read_b32 v48, a222\n"
    "v_mfma_f32_16x16x32_bf16 v[112:115], a[12:15], v[56:59], v[112:115]\n"
    "ds_write_b64 v10, v[120:121] offset:64\n"
    "v_cvt_pk_bf16_f32 v122, v50, v51\n"
    "v_mfma_f32_16x16x32_bf16 v[164:167], a[16:19], v[56:59], v[164:167]\n"
    "v_accvgpr_read_b32 v49, a223\n"
    "v_mul_f32 v48, v48, v17\n"
    "v_mul_f32 v49, v49, v17\n"
    "v_mul_f32 v180, v180, v17\n"
    "v_mfma_f32_16x16x32_bf16 v[164:167], a[20:23], v[40:43], v[164:167]\n"
    "v_cvt_pk_bf16_f32 v123, v48, v49\n"
    "v_mul_f32 v181, v181, v17\n"
    "v_mul_f32 v182, v182, v17\n"
    "v_cvt_pk_bf16_f32 v180, v180, v181\n"
    "v_mfma_f32_16x16x32_bf16 v[172:175], a[24:27], v[40:43], v[172:175]\n"
    "ds_write_b64 v10, v[122:123] offset:96\n"
    "v_mul_f32 v183, v183, v17\n"
    "v_mfma_f32_16x16x32_bf16 v[172:175], a[28:31], v[56:59], v[172:175]\n"
    "ds_read_b64 v[224:225], v11 offset:0\n"
    "ds_read_b64 v[226:227], v20 offset:0\n"
    "ds_read_b64 v[232:233], v11 offset:320\n"
    "ds_read_b64 v[234:235], v20 offset:320\n"
    "v_mfma_f32_16x16x32_bf16 v[228:231], a[32:35], v[56:59], v[228:231]\n"
    "v_cvt_pk_bf16_f32 v181, v182, v183\n"
    "v_mul_f32 v148, v148, v17\n"
    "v_mul_f32 v149, v149, v17\n"
    "v_mul_f32 v150, v150, v17\n"
    "v_mfma_f32_16x16x32_bf16 v[228:231], a[36:39], v[40:43], v[228:231]\n"
    "ds_write_b64 v10, v[180:181] offset:2560\n"
    "v_cvt_pk_bf16_f32 v148, v148, v149\n"
    "v_mfma_f32_16x16x32_bf16 v[236:239], a[40:43], v[40:43], v[236:239]\n"
    "v_mul_f32 v151, v151, v17\n"
    "v_mul_f32 v212, v212, v17\n"
    "v_cvt_pk_bf16_f32 v149, v150, v151\n"
    "v_mul_f32 v213, v213, v17\n"
    "v_mfma_f32_16x16x32_bf16 v[236:239], a[44:47], v[56:59], v[236:239]\n"
    "ds_write_b64 v10, v[148:149] offset:2592\n"
    "v_cvt_pk_bf16_f32 v212, v212, v213\n"
    "v_mfma_f32_16x16x32_bf16 v[128:131], a[48:51], v[56:59], v[128:131]\n"
    "s_waitcnt lgkmcnt(4)\n"
    "buffer_store_dwordx4 v[224:227], v9, s[48:51], s73 offen offset:128 sc1\n"
    "s_waitcnt lgkmcnt(2)\n"
    "buffer_store_dwordx4 v[232:235], v9, s[48:51], s73 offen offset:896 sc1\n"
    "v_mfma_f32_16x16x32_bf16 v[128:131], a[52:55], v[40:43], v[128:131]\n"
    "v_mul_f32 v214, v214, v17\n"
    "v_mul_f32 v215, v215, v17\n"
    "v_mul_f32 v244, v244, v17\n"
    "v_cvt_pk_bf16_f32 v213, v214, v215\n"
    "v_mfma_f32_16x16x32_bf16 a[228:231], a[56:59], v[40:43], a[228:231]\n"
    "ds_write_b64 v10, v[212:213] offset:2624\n"
    "v_mul_f32 v245, v245, v17\n"
    "v_mfma_f32_16x16x32_bf16 a[228:231], a[60:63], v[56:59], a[228:231]\n"
    "v_cvt_pk_bf16_f32 v244, v244, v245\n"
    "v_mul_f32 v246, v246, v17\n"
    "v_mul_f32 v247, v247, v17\n"
    "v_accvgpr_read_b32 v18, a248\n"
    "v_mfma_f32_16x16x32_bf16 v[188:191], a[64:67], v[56:59], v[188:191]\n"
    "v_cvt_pk_bf16_f32 v245, v246, v247\n"
    "v_accvgpr_read_b32 v26, a249\n"
    "v_accvgpr_read_b32 v54, a224\n"
    "v_add_f32 v18, v18, v26\n"
    "v_mfma_f32_16x16x32_bf16 v[188:191], a[68:71], v[40:43], v[188:191]\n"
    "ds_write_b64 v10, v[244:245] offset:2656\n"
    "v_add_f32 v27, v27, v18\n"
    "v_mfma_f32_16x16x32_bf16 v[156:159], a[72:75], v[40:43], v[156:159]\n"
    "ds_read_b64 v[180:181], v11 offset:2560\n"
    "ds_read_b64 v[182:183], v20 offset:2560\n"
    "ds_read_b64 v[148:149], v11 offset:2880\n"
    "ds_read_b64 v[150:151], v20 offset:2880\n"
    "v_mfma_f32_16x16x32_bf16 v[156:159], a[76:79], v[56:59], v[156:159]\n"
    "v_rcp_f32 v18, v18\n"
    "v_accvgpr_read_b32 v55, a225\n"
    "v_mul_f32 v88, v88, v18\n"
    "v_mfma_f32_16x16x32_bf16 v[220:223], a[80:83], v[56:59], v[220:223]\n"
    "v_mul_f32 v89, v89, v18\n"
    "v_mul_f32 v90, v90, v18\n"
    "v_cvt_pk_bf16_f32 v88, v88, v89\n"
    "v_mul_f32 v91, v91, v18\n"
    "v_mfma_f32_16x16x32_bf16 v[220:223], a[84:87], v[40:43], v[220:223]\n"
    "v_cvt_pk_bf16_f32 v89, v90, v91\n"
    "v_mul_f32 v96, v96, v18\n"
    "v_mul_f32 v97, v97, v18\n"
    "v_mul_f32 v98, v98, v18\n"
    "v_mfma_f32_16x16x32_bf16 v[252:255], a[88:91], v[40:43], v[252:255]\n"
    "ds_write_b64 v10, v[88:89] offset:5120\n"
    "v_cvt_pk_bf16_f32 v96, v96, v97\n"
    "v_mfma_f32_16x16x32_bf16 v[252:255], a[92:95], v[56:59], v[252:255]\n"
    "s_waitcnt lgkmcnt(3)\n"
    "buffer_store_dwordx4 v[180:183], v9, s[48:51], s73 offen offset:256 sc1\n"
    "s_waitcnt lgkmcnt(1)\n"
    "buffer_store_dwordx4 v[148:151], v9, s[48:51], s73 offen offset:1024 sc1\n"
    "v_mul_f32 v99, v99, v18\n"
    "v_mul_f32 v136, v136, v18\n"
    "v_cvt_pk_bf16_f32 v97, v98, v99\n"
    "v_mul_f32 v137, v137, v18\n"
    "ds_write_b64 v10, v[96:97] offset:5152\n"
    "v_cvt_pk_bf16_f32 v136, v136, v137\n"
    "v_mul_f32 v138, v138, v18\n"
    "v_mul_f32 v139, v139, v18\n"
    "v_mul_f32 v144, v144, v18\n"
    "v_cvt_pk_bf16_f32 v137, v138, v139\n"
    "v_mul_f32 v145, v145, v18\n"
    "ds_write_b64 v10, v[136:137] offset:5184\n"
    "v_cvt_pk_bf16_f32 v144, v144, v145\n"
    "v_mul_f32 v146, v146, v18\n"
    "v_mul_f32 v147, v147, v18\n"
    "v_pk_mul_f32 v[196:197], v[196:197], v[18:19] op_sel_hi:[1,0]\n"
    "v_cvt_pk_bf16_f32 v145, v146, v147\n"
    "v_cvt_pk_bf16_f32 v196, v196, v197\n"
    "ds_write_b64 v10, v[144:145] offset:5216\n"
    "v_pk_mul_f32 v[198:199], v[198:199], v[18:19] op_sel_hi:[1,0]\n"
    "ds_read_b64 v[88:89], v11 offset:5120\n"
    "ds_read_b64 v[90:91], v20 offset:5120\n"
    "ds_read_b64 v[96:97], v11 offset:5440\n"
    "ds_read_b64 v[98:99], v20 offset:5440\n"
    "v_cvt_pk_bf16_f32 v197, v198, v199\n"
    "v_pk_mul_f32 v[204:205], v[204:205], v[18:19] op_sel_hi:[1,0]\n"
    "ds_write_b64 v10, v[196:197] offset:7680\n"
    "v_cvt_pk_bf16_f32 v204, v204, v205\n"
    "v_pk_mul_f32 v[206:207], v[206:207], v[18:19] op_sel_hi:[1,0]\n"
    "v_pk_mul_f32 v[124:125], v[124:125], v[18:19] op_sel_hi:[1,0]\n"
    "v_cvt_pk_bf16_f32 v205, v206, v207\n"
    "v_cvt_pk_bf16_f32 v124, v124, v125\n"
    "ds_write_b64 v10, v[204:205] offset:7712\n"
    "v_pk_mul_f32 v[126:127], v[126:127], v[18:19] op_sel_hi:[1,0]\n"
    "v_pk_mul_f32 v[54:55], v[54:55], v[18:19] op_sel_hi:[1,0]\n"
    "v_cvt_pk_bf16_f32 v125, v126, v127\n"
    "v_accvgpr_read_b32 v52, a226\n"
    "ds_write_b64 v10, v[124:125] offset:7744\n"
    "v_cvt_pk_bf16_f32 v126, v54, v55\n"
    "v_accvgpr_read_b32 v53, a227\n"
    "v_pk_mul_f32 v[184:185], v[184:185], v[18:19] op_sel_hi:[1,0]\n"
    "s_waitcnt lgkmcnt(5)\n"
    "buffer_store_dwordx4 v[88:91], v9, s[48:51], s74 offen offset:0 sc1\n"
    "s_waitcnt lgkmcnt(3)\n"
    "buffer_store_dwordx4 v[96:99], v9, s[48:51], s74 offen offset:768 sc1\n"
    "v_pk_mul_f32 v[52:53], v[52:53], v[18:19] op_sel_hi:[1,0]\n"
    "v_cvt_pk_bf16_f32 v184, v184, v185\n"
    "v_cvt_pk_bf16_f32 v127, v52, v53\n"
    "v_pk_mul_f32 v[186:187], v[186:187], v[18:19] op_sel_hi:[1,0]\n"
    "ds_write_b64 v10, v[126:127] offset:7776\n"
    "v_cvt_pk_bf16_f32 v185, v186, v187\n"
    "ds_read_b64 v[196:197], v11 offset:7680\n"
    "ds_read_b64 v[198:199], v20 offset:7680\n"
    "ds_read_b64 v[204:205], v11 offset:8000\n"
    "ds_read_b64 v[206:207], v20 offset:8000\n"
    "ds_write_b64 v10, v[184:185] offset:0\n"
    "v_pk_mul_f32 v[152:153], v[152:153], v[18:19] op_sel_hi:[1,0]\n"
    "v_pk_mul_f32 v[154:155], v[154:155], v[18:19] op_sel_hi:[1,0]\n"
    "v_cvt_pk_bf16_f32 v152, v152, v153\n"
    "v_pk_mul_f32 v[216:217], v[216:217], v[18:19] op_sel_hi:[1,0]\n"
    "v_cvt_pk_bf16_f32 v153, v154, v155\n"
    "v_cvt_pk_bf16_f32 v216, v216, v217\n"
    "ds_write_b64 v10, v[152:153] offset:32\n"
    "v_pk_mul_f32 v[218:219], v[218:219], v[18:19] op_sel_hi:[1,0]\n"
    "v_pk_mul_f32 v[248:249], v[248:249], v[18:19] op_sel_hi:[1,0]\n"
    "v_cvt_pk_bf16_f32 v217, v218, v219\n"
    "v_cvt_pk_bf16_f32 v248, v248, v249\n"
    "ds_write_b64 v10, v[216:217] offset:64\n"
    "v_pk_mul_f32 v[250:251], v[250:251], v[18:19] op_sel_hi:[1,0]\n"
    "v_accvgpr_read_b32 v19, a250\n"
    "v_cvt_pk_bf16_f32 v249, v250, v251\n"
    "v_accvgpr_read_b32 v26, a251\n"
    "s_waitcnt lgkmcnt(5)\n"
    "buffer_store_dwordx4 v[196:199], v9, s[48:51], s74 offen offset:128 sc1\n"
    "s_waitcnt lgkmcnt(3)\n"
    "buffer_store_dwordx4 v[204:207], v9, s[48:51], s74 offen offset:896 sc1\n"
    "ds_write_b64 v10, v[248:249] offset:96\n"
    "v_add_f32 v19, v19, v26\n"
    "ds_read_b64 v[184:185], v11 offset:0\n"
    "ds_read_b64 v[186:187], v20 offset:0\n"
    "ds_read_b64 v[152:153], v11 offset:320\n"
    "ds_read_b64 v[154:155], v20 offset:320\n"
    "v_add_f32 v27, v27, v19\n"
    "v_accvgpr_read_b32 v58, a228\n"
    "v_rcp_f32 v19, v19\n"
    "v_cmp_ngt_f32 vcc, s53, v27\n"
    "v_pk_mul_f32 v[104:105], v[104:105], v[18:19] op_sel:[0,1] op_sel_hi:[1,1]\n"
    "v_pk_mul_f32 v[106:107], v[106:107], v[18:19] op_sel:[0,1] op_sel_hi:[1,1]\n"
    "s_cmp_lg_u64 vcc, 0\ns_cselect_b32 s58, 2, 0\ns_or_b32 %0, %0, s58\n"
    "v_cvt_pk_bf16_f32 v104, v104, v105\n"
    "v_pk_mul_f32 v[112:113], v[112:113], v[18:19] op_sel:[0,1] op_sel_hi:[1,1]\n"
    "v_cvt_pk_bf16_f32 v105, v106, v107\n"
    "v_cvt_pk_bf16_f32 v112, v112, v113\n"
    "ds_write_b64 v10, v[104:105] offset:2560\n"
    "v_pk_mul_f32 v[114:115], v[114:115], v[18:19] op_sel:[0,1] op_sel_hi:[1,1]\n"
    "v_pk_mul_f32 v[164:165], v[164:165], v[18:19] op_sel:[0,1] op_sel_hi:[1,1]\n"
    "v_cvt_pk_bf16_f32 v113, v114, v115\n"
    "s_waitcnt lgkmcnt(3)\n"
    "buffer_store_dwordx4 v[184:187], v9, s[48:51], s74 offen offset:256 sc1\n"
    "s_waitcnt lgkmcnt(1)\n"
    "buffer_store_dwordx4 v[152:155], v9, s[48:51], s74 offen offset:1024 sc1\n"
    "ds_write_b64 v10, v[112:113] offset:2592\n"
    "v_cvt_pk_bf16_f32 v164, v164, v165\n"
    "v_pk_mul_f32 v[166:167], v[166:167], v[18:19] op_sel:[0,1] op_sel_hi:[1,1]\n"
    "v_pk_mul_f32 v[172:173], v[172:173], v[18:19] op_sel:[0,1] op_sel_hi:[1,1]\n"
    "v_cvt_pk_bf16_f32 v165, v166, v167\n"
    "v_cvt_pk_bf16_f32 v172, v172, v173\n"
    "ds_write_b64 v10, v[164:165] offset:2624\n"
    "v_pk_mul_f32 v[174:175], v[174:175], v[18:19] op_sel:[0,1] op_sel_hi:[1,1]\n"
    "v_pk_mul_f32 v[228:229], v[228:229], v[18:19] op_sel:[0,1] op_sel_hi:[1,1]\n"
    "v_cvt_pk_bf16_f32 v173, v174, v175\n"
    "v_cvt_pk_bf16_f32 v228, v228, v229\n"
    "ds_write_b64 v10, v[172:173] offset:2656\n"
    "v_pk_mul_f32 v[230:231], v[230:231], v[18:19] op_sel:[0,1] op_sel_hi:[1,1]\n"
    "ds_read_b64 v[104:105], v11 offset:2560\n"
    "ds_read_b64 v[106:107], v20 offset:2560\n"
    "ds_read_b64 v[112:113], v11 offset:2880\n"
    "ds_read_b64 v[114:115], v20 offset:2880\n"
    "v_cvt_pk_bf16_f32 v229, v230, v231\n"
    "v_pk_mul_f32 v[236:237], v[236:237], v[18:19] op_sel:[0,1] op_sel_hi:[1,1]\n"
    "ds_write_b64 v10, v[228:229] offset:5120\n"
    "v_cvt_pk_bf16_f32 v236, v236, v237\n"
    "v_pk_mul_f32 v[238:239], v[238:239], v[18:19] op_sel:[0,1] op_sel_hi:[1,1]\n"
    "v_pk_mul_f32 v[128:129], v[128:129], v[18:19] op_sel:[0,1] op_sel_hi:[1,1]\n"
    "v_cvt_pk_bf16_f32 v237, v238, v239\n"
    "v_cvt_pk_bf16_f32 v128, v128, v129\n"
    "ds_write_b64 v10, v[236:237] offset:5152\n"
    "v_pk_mul_f32 v[130:131], v[130:131], v[18:19] op_sel:[0,1] op_sel_hi:[1,1]\n"
    "v_accvgpr_read_b32 v59, a229\n"
    "v_cvt_pk_bf16_f32 v129, v130, v131\n"
    "v_pk_mul_f32 v[58:59], v[58:59], v[18:19] op_sel:[0,1] op_sel_hi:[1,1]\n"
    "ds_write_b64 v10, v[128:129] offset:5184\n"
    "v_cvt_pk_bf16_f32 v130, v58, v59\n"
    "v_accvgpr_read_b32 v56, a230\n"
    "v_accvgpr_read_b32 v57, a231\n"
    "s_waitcnt lgkmcnt(5)\n"
    "buffer_store_dwordx4 v[104:107], v9, s[48:51], s75 offen offset:0 sc1\n"
    "s_waitcnt lgkmcnt(3)\n"
    "buffer_store_dwordx4 v[112:115], v9, s[48:51], s75 offen offset:768 sc1\n"
    "v_pk_mul_f32 v[56:57], v[56:57], v[18:19] op_sel:[0,1] op_sel_hi:[1,1]\n"
    "v_pk_mul_f32 v[188:189], v[188:189], v[18:19] op_sel:[0,1] op_sel_hi:[1,1]\n"
    "v_cvt_pk_bf16_f32 v131, v56, v57\n"
    "v_cvt_pk_bf16_f32 v188, v188, v189\n"
    "ds_write_b64 v10, v[130:131] offset:5216\n"
    "v_pk_mul_f32 v[190:191], v[190:191], v[18:19] op_sel:[0,1] op_sel_hi:[1,1]\n"
    "ds_read_b64 v[228:229], v11 offset:5120\n"
    "ds_read_b64 v[230:231], v20 offset:5120\n"
    "ds_read_b64 v[236:237], v11 offset:5440\n"
    "ds_read_b64 v[238:239], v20 offset:5440\n"
    "v_cvt_pk_bf16_f32 v189, v190, v191\n"
    "v_pk_mul_f32 v[156:157], v[156:157], v[18:19] op_sel:[0,1] op_sel_hi:[1,1]\n"
    "ds_write_b64 v10, v[188:189] offset:7680\n"
    "v_cvt_pk_bf16_f32 v156, v156, v157\n"
    "v_pk_mul_f32 v[158:159], v[158:159], v[18:19] op_sel:[0,1] op_sel_hi:[1,1]\n"
    "v_pk_mul_f32 v[220:221], v[220:221], v[18:19] op_sel:[0,1] op_sel_hi:[1,1]\n"
    "v_cvt_pk_bf16_f32 v157, v158, v159\n"
    "v_cvt_pk_bf16_f32 v220, v220, v221\n"
    "ds_write_b64 v10, v[156:157] offset:7712\n"
    "v_pk_mul_f32 v[222:223], v[222:223], v[18:19] op_sel:[0,1] op_sel_hi:[1,1]\n"
    "v_pk_mul_f32 v[252:253], v[252:253], v[18:19] op_sel:[0,1] op_sel_hi:[1,1]\n"
    "v_cvt_pk_bf16_f32 v221, v222, v223\n"
    "v_cvt_pk_bf16_f32 v252, v252, v253\n"
    "ds_write_b64 v10, v[220:221] offset:7744\n"
    "v_pk_mul_f32 v[254:255], v[254:255], v[18:19] op_sel:[0,1] op_sel_hi:[1,1]\n"
    "s_nop 0\n"
    "v_cvt_pk_bf16_f32 v253, v254, v255\n"
    "ds_write_b64 v10, v[252:253] offset:7776\n"
    "s_nop 0\n"
    "ds_read_b64 v[188:189], v11 offset:7680\n"
    "ds_read_b64 v[190:191], v20 offset:7680\n"
    "ds_read_b64 v[156:157], v11 offset:8000\n"
    "ds_read_b64 v[158:159], v20 offset:8000\n"
    "s_waitcnt lgkmcnt(10)\n"
    "buffer_store_dwordx4 v[228:231], v9, s[48:51], s75 offen offset:128 sc1\n"
    "s_waitcnt lgkmcnt(8)\n"
    "buffer_store_dwordx4 v[236:239], v9, s[48:51], s75 offen offset:896 sc1\n"
    "s_waitcnt lgkmcnt(2)\n"
    "buffer_store_dwordx4 v[188:191], v9, s[48:51], s75 offen offset:256 sc1\n"
    "s_waitcnt lgkmcnt(0)\n"
    "buffer_store_dwordx4 v[156:159], v9, s[48:51], s75 offen offset:1024 sc1\n"
    "END_%=:\n"
    : "=&s"(flag)
    : "s"((unsigned long long)__builtin_amdgcn_kernarg_segment_ptr()), "s"(b), "s"(w), "s"((unsigned)(size_t)lds)
    : "v0","v1","v2","v3","v4","v5","v6","v7","v8","v9","v10","v11","v12","v13","v14","v15","v16","v17","v18","v19","v20","v21","v22","v23","v24","v25","v26","v27","v28","v29","v30","v31","v32","v33","v34","v35","v36","v37","v38","v39","v40","v41","v42","v43","v44","v45","v46","v47","v48","v49","v50","v51","v52","v53","v54","v55","v56","v57","v58","v59","v60","v61","v62","v63","v64","v65","v66","v67","v68","v69","v70","v71","v72","v73","v74","v75","v76","v77","v78","v79","v80","v81","v82","v83","v84","v85","v86","v87","v88","v89","v90","v91","v92","v93","v94","v95","v96","v97","v98","v99","v100","v101","v102","v103","v104","v105","v106","v107","v108","v109","v110","v111","v112","v113","v114","v115","v116","v117","v118","v119","v120","v121","v122","v123","v124","v125","v126","v127","v128","v129","v130","v131","v132","v133","v134","v135","v136","v137","v138","v139","v140","v141","v142","v143","v144","v145","v146","v147","v148","v149","v150","v151","v152","v153","v154","v155","v156","v157","v158","v159","v160","v161","v162","v163","v164","v165","v166","v167","v168","v169","v170","v171","v172","v173","v174","v175","v176","v177","v178","v179","v180","v181","v182","v183","v184","v185","v186","v187","v188","v189","v190","v191","v192","v193","v194","v195","v196","v197","v198","v199","v200","v201","v202","v203","v204","v205","v206","v207","v208","v209","v210","v211","v212","v213","v214","v215","v216","v217","v218","v219","v220","v221","v222","v223","v224","v225","v226","v227","v228","v229","v230","v231","v232","v233","v234","v235","v236","v237","v238","v239","v240","v241","v242","v243","v244","v245","v246","v247","v248","v249","v250","v251","v252","v253","v254","v255","a0","a1","a2","a3","a4","a5","a6","a7","a8","a9","a10","a11","a12","a13","a14","a15","a16","a17","a18","a19","a20","a21","a22","a23","a24","a25","a26","a27","a28","a29","a30","a31","a32","a33","a34","a35","a36","a37","a38","a39","a40","a41","a42","a43","a44","a45","a46","a47","a48","a49","a50","a51","a52","a53","a54","a55","a56","a57","a58","a59","a60","a61","a62","a63","a64","a65","a66","a67","a68","a69","a70","a71","a72","a73","a74","a75","a76","a77","a78","a79","a80","a81","a82","a83","a84","a85","a86","a87","a88","a89","a90","a91","a92","a93","a94","a95","a96","a97","a98","a99","a100","a101","a102","a103","a104","a105","a106","a107","a108","a109","a110","a111","a112","a113","a114","a115","a116","a117","a118","a119","a120","a121","a122","a123","a124","a125","a126","a127","a128","a129","a130","a131","a132","a133","a134","a135","a136","a137","a138","a139","a140","a141","a142","a143","a144","a145","a146","a147","a148","a149","a150","a151","a152","a153","a154","a155","a156","a157","a158","a159","a160","a161","a162","a163","a164","a165","a166","a167","a168","a169","a170","a171","a172","a173","a174","a175","a176","a177","a178","a179","a180","a181","a182","a183","a184","a185","a186","a187","a188","a189","a190","a191","a192","a193","a194","a195","a196","a197","a198","a199","a200","a201","a202","a203","a204","a205","a206","a207","a208","a209","a210","a211","a212","a213","a214","a215","a216","a217","a218","a219","a220","a221","a222","a223","a224","a225","a226","a227","a228","a229","a230","a231","a232","a233","a234","a235","a236","a237","a238","a239","a240","a241","a242","a243","a244","a245","a246","a247","a248","a249","a250","a251","a252","a253","a254","a255","s36","s37","s38","s39","s40","s41","s42","s43","s44","s45","s46","s47","s48","s49","s50","s51","s52","s53","s54","s55","s56","s57","s58","s59","s60","s61","s62","s63","s64","s65","s66","s67","s68","s69","s70","s71","s72","s73","s74","s75","s76","s77","s78","s79","vcc","scc","memory");
  unsigned* fl = (unsigned*)(lds + 122880);
  const unsigned lane = __builtin_amdgcn_mbcnt_hi(~0u, __builtin_amdgcn_mbcnt_lo(~0u, 0u));
  if (lane == 0) fl[2560 * w] = flag;
  __syncthreads();
  flag = __builtin_amdgcn_readfirstlane(fl[0] | fl[2560] | fl[5120] | fl[7680]);
  if (flag) {
    asm volatile("s_waitcnt vmcnt(0)" ::: "memory");
    const unsigned pair = (b >> 4) * 8 + (b & 7), j = (b >> 3) & 1;
    for (unsigned t = 0; t < 4; ++t)
      if ((flag >> (t >> 1)) & 1) redo_rows(Q, K, V, O, lds, 2 * pair + (t >> 1), 248 * j + 128 * (t & 1), 248 * j + 248, w, lane);
  }
}
extern "C" void kernel_launch(void* const* d_in, const int* in_sizes, int n_in, void* d_out, int out_size, void* d_ws, size_t ws_size, hipStream_t stream) {
  mha_fwd<<<256, 256, 0, stream>>>((const char*)d_in[0], (const char*)d_in[1], (const char*)d_in[2], (char*)d_out, (char*)d_ws);
}
```
